```python
import torch

def _softmax(x, axis=-1):
    return torch.softmax(x, dim=axis)
def _gelu(x):
    return torch.nn.functional.gelu(x, approximate="tanh")
def _silu(x):
    return torch.nn.functional.silu(x)
def _sigmoid(x):
    return torch.sigmoid(x)
def _layernorm(x, axis=-1, eps=1e-5):
    m = x.mean(dim=axis, keepdim=True)
    v = x.var(dim=axis, keepdim=True, unbiased=False)
    return (x - m) / torch.sqrt(v + eps)
def _scatter_add(tgt, idx, src, axis=0):
    return tgt.clone().index_add_(axis, idx, src)
def _segment_sum(src, seg, n):
    out = torch.zeros((n,) + src.shape[1:], dtype=src.dtype)
    return out.index_add_(0, seg, src)
def _pad(x, pw):
    flat = tuple(v for pair in reversed(pw) for v in pair)
    return torch.nn.functional.pad(x, flat)

kernel_name = 'interleave_layers__grouped_kv_mqa__dense_activation_relu__prompt'

def setup_inputs():

    _w = (lambda a: (lambda t: t.to(torch.bfloat16) if t.is_floating_point() else t)(torch.as_tensor(a)))
    def _A_setup():
        B, S, D, H = 1, 2048, 512, 32
        Dh = D // H
        return {
            "x": _w((lambda *s: torch.randn(*s, dtype=torch.bfloat16) * 0.05)(B, S, D)),
            "wq": _w((lambda *s: torch.randn(*s, dtype=torch.bfloat16) * 0.05)(D, H * Dh)),
            "wk": _w((lambda *s: torch.randn(*s, dtype=torch.bfloat16) * 0.05)(D, Dh)),
            "wv": _w((lambda *s: torch.randn(*s, dtype=torch.bfloat16) * 0.05)(D, Dh)),
            "wo": _w((lambda *s: torch.randn(*s, dtype=torch.bfloat16) * 0.05)(H * Dh, D)),
        }
    _A_in = _A_setup()

    def _B_setup():
        B, S, D, Dff = 1, 2048, 512, 1792
        return {
            "x": _w((lambda *s: torch.randn(*s, dtype=torch.bfloat16) * 0.05)(B, S, D)),
            "w1": _w((lambda *s: torch.randn(*s, dtype=torch.bfloat16) * 0.05)(D, Dff)),
            "w2": _w((lambda *s: torch.randn(*s, dtype=torch.bfloat16) * 0.05)(Dff, D)),
        }
    _B_in = _B_setup()

    return {
        "A_x": _A_in["x"],
        "A_wq": _A_in["wq"],
        "A_wk": _A_in["wk"],
        "A_wv": _A_in["wv"],
        "A_wo": _A_in["wo"],
        "B_x": _B_in["x"],
        "B_w1": _B_in["w1"],
        "B_w2": _B_in["w2"],
    }

def reference(A_x, A_wq, A_wk, A_wv, A_wo, B_x, B_w1, B_w2):
    def _A_ref(x, wq, wk, wv, wo):
        B, S, D = x.shape
        H = 32
        Dh = D // H
        q = (lambda x, *d: x.permute(*d))((x @ wq).reshape(B, S, H, Dh), 0, 2, 1, 3)
        k = x @ wk
        v = x @ wv
        scores = torch.einsum("bhqd,bkd->bhqk", q, k) * (Dh ** -0.5)
        p = _softmax(scores, axis=-1)
        o = torch.einsum("bhqk,bkd->bhqd", p, v)
        return (lambda x, *d: x.permute(*d))(o, 0, 2, 1, 3).reshape(B, S, H * Dh) @ wo

    def _B_ref(x, w1, w2):
        h = torch.einsum("bsd,df->bsf", x, w1)
        zero = torch.zeros_like(h)
        h = torch.maximum(h, zero)
        return torch.einsum("bsf,fd->bsd", h, w2)

    x = A_x
    x = x + _A_ref(_layernorm(x, axis=-1), A_wq, A_wk, A_wv, A_wo)
    x = x + _B_ref(_layernorm(x, axis=-1), B_w1, B_w2)
    x = x + _A_ref(_layernorm(x, axis=-1), A_wq, A_wk, A_wv, A_wo)
    x = x + _B_ref(_layernorm(x, axis=-1), B_w1, B_w2)
    return x
```

```cpp
#include <hip/hip_runtime.h>
#include <hip/hip_bf16.h>
#include <stdint.h>

typedef __attribute__((ext_vector_type(8))) short bf16x8;
typedef __attribute__((ext_vector_type(4))) float f32x4;
typedef __attribute__((ext_vector_type(16))) float f32x16;
typedef __attribute__((ext_vector_type(4))) unsigned u32x4;
typedef __attribute__((ext_vector_type(2))) unsigned u32x2;
typedef unsigned short u16;
#define DI __device__ __forceinline__
#define LAS __attribute__((address_space(3)))

constexpr int S_ = 2048, D_ = 512, DFF = 1792;
constexpr float QSCALE = 0.25f * 1.4426950408889634f;
constexpr float LN_EPS = 1e-5f;

constexpr size_t KB = 1024;
constexpr size_t OFF_WQKV = 0;
constexpr size_t OFF_WO   = OFF_WQKV + 544 * KB;
constexpr size_t OFF_W1   = OFF_WO + 512 * KB;
constexpr size_t OFF_W2   = OFF_W1 + 1792 * KB;
constexpr size_t OFF_CSUM = OFF_W2 + 1792 * KB;
constexpr size_t OFF_X0   = OFF_CSUM + 4 * KB;
constexpr size_t OFF_Q    = OFF_X0 + 2048 * KB;
constexpr size_t OFF_K    = OFF_Q + 2048 * KB;
constexpr size_t OFF_VT   = OFF_K + 64 * KB;
constexpr size_t OFF_KN2  = OFF_VT + 72 * KB;
constexpr size_t OFF_QN2  = OFF_KN2 + 1 * KB;
constexpr size_t OFF_X1   = OFF_QN2 + 128 * KB;
constexpr size_t OFF_XN1  = OFF_X1 + 2048 * KB;
constexpr size_t OFF_H    = OFF_XN1 + 2048 * KB;
constexpr size_t OFF_X2   = OFF_H + 7168 * KB;
constexpr size_t WS_NEED  = OFF_X2 + 2048 * KB;

DI float bf2f(u16 v) { return __uint_as_float(((unsigned)v) << 16); }
DI float bflo(unsigned v) { return __uint_as_float(v << 16); }
DI float bfhi(unsigned v) { return __uint_as_float(v & 0xffff0000u); }
DI unsigned pk2(float lo, float hi) {
  typedef __attribute__((ext_vector_type(2))) float f32x2_;
  typedef __attribute__((ext_vector_type(2))) __bf16 bf16x2_;
  f32x2_ v = {lo, hi};
  bf16x2_ r = __builtin_convertvector(v, bf16x2_);
  return __builtin_bit_cast(unsigned, r);
}
DI f32x4 mfma16(bf16x8 a, bf16x8 b, f32x4 c) {
  typedef __attribute__((ext_vector_type(8))) __bf16 b8;
  return __builtin_amdgcn_mfma_f32_16x16x32_bf16(__builtin_bit_cast(b8, a), __builtin_bit_cast(b8, b), c, 0, 0, 0);
}
DI f32x16 mfma32(bf16x8 a, bf16x8 b, f32x16 c) {
  typedef __attribute__((ext_vector_type(8))) __bf16 b8;
  return __builtin_amdgcn_mfma_f32_32x32x16_bf16(__builtin_bit_cast(b8, a), __builtin_bit_cast(b8, b), c, 0, 0, 0);
}
DI void glds16(const void* g, unsigned lds_base) {
  unsigned sv;
  asm volatile("s_mov_b32 %0, m0\n\ts_mov_b32 m0, %2\n\ts_nop 0\n\tglobal_load_lds_dwordx4 %1, off\n\ts_mov_b32 m0, %0"
               : "=&s"(sv) : "v"(g), "s"(lds_base) : "memory");
}
template <int N> DI void wait_vm() { asm volatile("s_waitcnt vmcnt(%0)" :: "i"(N) : "memory"); }
DI void raw_barrier() { asm volatile("s_waitcnt lgkmcnt(0)\n\ts_barrier" ::: "memory"); }
DI bf16x8 lds_frag(const char* lds, int off) { return *(const LAS bf16x8*)((const LAS char*)lds + off); }
DI int kperm(int g, int j) { return (j < 4) ? 4 * g + j : 16 + 4 * g + (j - 4); }

__global__ __launch_bounds__(1024) void k_prep(const u16* __restrict__ x, const u16* __restrict__ wq, const u16* __restrict__ wk,
                                               const u16* __restrict__ wv, const u16* __restrict__ wo, const u16* __restrict__ w1,
                                               const u16* __restrict__ w2, char* __restrict__ ws) {
  __shared__ float red[16][16];
  const int wv_ = threadIdx.x >> 6, lane = threadIdx.x & 63, g = lane >> 4, nn = lane & 15;
  int b = blockIdx.x;
  const u16* W; int N, nt, ks; char* dst; bool qkv = false;
  if (b < 34) { nt = b; ks = wv_; qkv = true; dst = ws + OFF_WQKV + (size_t)(nt * 16 + ks) * KB;
    if (nt < 32) { W = wq + 16 * nt; N = 512; } else { W = (nt == 32) ? wk : wv; N = 16; } }
  else if (b < 34 + 32) { nt = b - 34; ks = wv_; W = wo + 16 * nt; N = 512; dst = ws + OFF_WO + (size_t)(nt * 16 + ks) * KB; }
  else if (b < 34 + 32 + 112) { nt = b - 66; ks = wv_; W = w1 + 16 * nt; N = DFF; dst = ws + OFF_W1 + (size_t)(nt * 16 + ks) * KB; }
  else if (b < 34 + 32 + 112 + 112) { int c = (b - 178) * 16 + wv_; nt = c / 56; ks = c % 56; W = w2 + 16 * nt; N = 512; dst = ws + OFF_W2 + (size_t)c * KB; }
  else {
    int mt = b - 290; ks = wv_;
    const u16* src = x + (size_t)(16 * mt + nn) * D_ + 32 * ks + 4 * g;
    u32x2 a = *(const u32x2*)src, c = *(const u32x2*)(src + 16);
    u32x4 o = {a[0], a[1], c[0], c[1]};
    *(u32x4*)(ws + OFF_X0 + (size_t)(mt * 16 + ks) * KB + lane * 16) = o;
    return;
  }
  u16 v[8]; float s = 0.f;
#pragma unroll
  for (int j = 0; j < 8; ++j) { v[j] = W[(size_t)(32 * ks + kperm(g, j)) * N + nn]; s += bf2f(v[j]); }
  u32x4 o = {v[0] | ((unsigned)v[1] << 16), v[2] | ((unsigned)v[3] << 16), v[4] | ((unsigned)v[5] << 16), v[6] | ((unsigned)v[7] << 16)};
  *(u32x4*)(dst + lane * 16) = o;
  if (qkv) {
    s += __shfl_xor(s, 16); s += __shfl_xor(s, 32);
    if (lane < 16) red[wv_][lane] = s;
    __syncthreads();
    if (threadIdx.x < 16) { float t = 0.f;
#pragma unroll
      for (int i = 0; i < 16; ++i) t += red[i][threadIdx.x];
      ((float*)(ws + OFF_CSUM))[nt * 16 + threadIdx.x] = t; }
  }
}

__global__ __launch_bounds__(512, 2) void k_qkv(const char* __restrict__ xfm, char* __restrict__ ws) {
  extern __shared__ __attribute__((aligned(16))) char lds[];
  const int tid = threadIdx.x, lane = tid & 63, g = lane >> 4, mm = lane & 15;
  const int w = __builtin_amdgcn_readfirstlane(tid >> 6);
  const int xcd = blockIdx.x & 7, idx = blockIdx.x >> 3, rb = idx >> 3, cb = idx & 7;
  const int mt0 = xcd * 16 + rb * 4;
  const int kvnt = 32 + (cb & 1), kvmt = cb >> 1;
  const unsigned lds0 = (unsigned)(uintptr_t)lds;
  const char* wq = ws + OFF_WQKV;
  auto issue = [&](int s) {
#pragma unroll
    for (int i = 0; i < 3; ++i) {
      int c = w + 8 * i;
      if (c < 18) {
        int kk = c >= 9 ? 1 : 0, t = c - 9 * kk, ks = 2 * s + kk;
        const char* src = (t < 4) ? wq + (size_t)((4 * cb + t) * 16 + ks) * KB : (t == 4) ? wq + (size_t)(kvnt * 16 + ks) * KB
                                                                                           : xfm + (size_t)((mt0 + t - 5) * 16 + ks) * KB;
        glds16(src + lane * 16, lds0 + (s * 18 + c) * 1024);
      }
    }
  };
  const int mt = w & 3, np = w >> 2;
  const bool extra = (w < 2);
  const bool do_kv = (np == 1) && (mt == kvmt);
  f32x4 acc0 = {}, acc1 = {}, accs = {}, accg = {};
  bf16x8 ones; for (int i = 0; i < 8; ++i) ones[i] = 0x3F80;
  issue(0); issue(1); issue(2); issue(3);
#pragma unroll
  for (int s = 0; s < 8; ++s) {
    constexpr int DUMMY = 0; (void)DUMMY;
    const int ahead = (7 - s) < 3 ? (7 - s) : 3;
    if (extra) { if (ahead == 3) wait_vm<9>(); else if (ahead == 2) wait_vm<6>(); else if (ahead == 1) wait_vm<3>(); else wait_vm<0>(); }
    else       { if (ahead == 3) wait_vm<6>(); else if (ahead == 2) wait_vm<4>(); else if (ahead == 1) wait_vm<2>(); else wait_vm<0>(); }
    raw_barrier();
    if (s + 4 < 8) issue(s + 4);
#pragma unroll
    for (int kk = 0; kk < 2; ++kk) {
      const int base = (s * 18 + kk * 9) * 1024 + lane * 16;
      bf16x8 b = lds_frag(lds, base + (5 + mt) * 1024);
      bf16x8 a0 = lds_frag(lds, base + (2 * np) * 1024);
      bf16x8 a1 = lds_frag(lds, base + (2 * np + 1) * 1024);
      acc0 = mfma16(a0, b, acc0);
      acc1 = mfma16(a1, b, acc1);
      if (np == 0) { accs = mfma16(ones, b, accs); accg = mfma16(b, b, accg); }
      else if (do_kv) {
        bf16x8 akv = lds_frag(lds, base + 4 * 1024);
        if (cb & 1) accs = mfma16(b, akv, accs);
        else        accs = mfma16(akv, b, accs);
      }
    }
  }
  __syncthreads();
  float* st = (float*)lds;
  if (np == 0 && g == (mm >> 2)) {
    float ss = (mm & 3) == 0 ? accg[0] : (mm & 3) == 1 ? accg[1] : (mm & 3) == 2 ? accg[2] : accg[3];
    float mean = accs[0] * (1.f / D_);
    float var = fmaxf(ss * (1.f / D_) - mean * mean, 0.f);
    st[(mt * 16 + mm) * 2] = mean;
    st[(mt * 16 + mm) * 2 + 1] = rsqrtf(var + LN_EPS);
  }
  __syncthreads();
  const float* csum = (const float*)(ws + OFF_CSUM);
  const float mean = st[(mt * 16 + mm) * 2], rstd = st[(mt * 16 + mm) * 2 + 1];
  const int tok = (mt0 + mt) * 16 + mm;
  float qn = 0.f;
#pragma unroll
  for (int t = 0; t < 2; ++t) {
    const int nt = 4 * cb + 2 * np + t;
    f32x4 a = t ? acc1 : acc0;
    f32x4 cs = *(const f32x4*)(csum + nt * 16 + 4 * g);
    float q[4];
#pragma unroll
    for (int i = 0; i < 4; ++i) q[i] = (a[i] - mean * cs[i]) * (rstd * QSCALE);
    u32x2 o = {pk2(q[0], q[1]), pk2(q[2], q[3])};
    *(u32x2*)(ws + OFF_Q + ((size_t)tok * D_ + nt * 16 + 4 * g) * 2) = o;
    float n2 = bflo(o[0]) * bflo(o[0]) + bfhi(o[0]) * bfhi(o[0]) + bflo(o[1]) * bflo(o[1]) + bfhi(o[1]) * bfhi(o[1]);
    n2 += __shfl_xor(n2, 16); n2 += __shfl_xor(n2, 32);
    qn = fmaxf(qn, n2);
  }
  if (g == 0) ((float*)(ws + OFF_QN2))[(cb * 2 + np) * S_ + tok] = qn;
  if (do_kv) {
    if (cb & 1) {
      const float cs = csum[33 * 16 + mm];
      float v[4];
#pragma unroll
      for (int i = 0; i < 4; ++i) { float mn = st[(mt * 16 + 4 * g + i) * 2], rs = st[(mt * 16 + 4 * g + i) * 2 + 1]; v[i] = (accs[i] - mn * cs) * rs; }
      u32x2 o = {pk2(v[0], v[1]), pk2(v[2], v[3])};
      char* vb = ws + OFF_VT + (size_t)((mt0 + mt) * 2 + (g & 1)) * 272;
      *(u32x2*)(vb + mm * 16 + (g >> 1) * 8) = o;
      if (mm == 0) { u32x2 on = {0x3F803F80u, 0x3F803F80u}; *(u32x2*)(vb + 256 + (g >> 1) * 8) = on; }
    } else {
      f32x4 cs = *(const f32x4*)(csum + 32 * 16 + 4 * g);
      float k[4];
#pragma unroll
      for (int i = 0; i < 4; ++i) k[i] = (accs[i] - mean * cs[i]) * rstd;
      u32x2 o = {pk2(k[0], k[1]), pk2(k[2], k[3])};
      *(u32x2*)(ws + OFF_K + ((size_t)tok * 16 + 4 * g) * 2) = o;
      float n2 = bflo(o[0]) * bflo(o[0]) + bfhi(o[0]) * bfhi(o[0]) + bflo(o[1]) * bflo(o[1]) + bfhi(o[1]) * bfhi(o[1]);
      n2 += __shfl_xor(n2, 16); n2 += __shfl_xor(n2, 32);
#pragma unroll
      for (int o2 = 1; o2 < 16; o2 <<= 1) n2 = fmaxf(n2, __shfl_xor(n2, o2));
      if (lane == 0) ((float*)(ws + OFF_KN2))[mt0 + mt] = n2;
    }
  }
}

constexpr int AL_RO = 0;
constexpr int AL_RL = 32768;
constexpr int AL_O  = AL_RL + 2048;
constexpr int AL_MX = AL_O + 4096;
constexpr int AL_ST = AL_MX + 8192;
constexpr int AL_BYTES = AL_ST + 512;
constexpr float SHIFT_MAX = 32.f;

__global__ __launch_bounds__(512, 2) void k_attn(const char* __restrict__ xres, char* __restrict__ ws) {
  extern __shared__ __attribute__((aligned(16))) char lds[];
  const int tid = threadIdx.x, lane = tid & 63, r = lane & 31, h = lane >> 5;
  const int w = __builtin_amdgcn_readfirstlane(tid >> 6);
  const int xcd = blockIdx.x & 7, idx = blockIdx.x >> 3;
  const int t0 = xcd * 256 + idx * 8;
  bf16x8 kf[8], vf[16];
  {
    const char* kp = ws + OFF_K + (size_t)(256 * w + r) * 32 + 16 * h;
#pragma unroll
    for (int t = 0; t < 8; ++t) kf[t] = *(const bf16x8*)(kp + t * 1024);
    const char* vp = ws + OFF_VT + (size_t)(16 * w * 2 + h) * 272 + (r < 16 ? r : 16) * 16;
#pragma unroll
    for (int t = 0; t < 16; ++t) vf[t] = *(const bf16x8*)(vp + t * 544);
  }
  const char* qp = ws + OFF_Q + (size_t)(t0 + (r & 7)) * 1024 + (r >> 3) * 32 + 16 * h;
  float Mb;
  {
    const float* kn2 = (const float*)(ws + OFF_KN2);
    const float* qn2 = (const float*)(ws + OFF_QN2);
    float km = fmaxf(kn2[lane], kn2[lane + 64]);
    float qm = fmaxf(qn2[(lane >> 3) * S_ + t0 + (lane & 7)], qn2[(8 + (lane >> 3)) * S_ + t0 + (lane & 7)]);
#pragma unroll
    for (int o = 1; o < 64; o <<= 1) { km = fmaxf(km, __shfl_xor(km, o)); qm = fmaxf(qm, __shfl_xor(qm, o)); }
    Mb = sqrtf(km * qm) * 1.001f;
  }
  const bool slow = !(__builtin_amdgcn_readfirstlane(__float_as_uint(Mb <= SHIFT_MAX ? 1.f : 0.f)) != 0u);
  f32x16 cinit;
#pragma unroll
  for (int i = 0; i < 16; ++i) cinit[i] = -Mb;
  if (slow) {
    f32x16 z = {};
    float* mx = (float*)(lds + AL_MX);
    for (int g = 0; g < 8; ++g) {
      bf16x8 qf = *(const bf16x8*)(qp + g * 128);
      float m = -INFINITY;
#pragma unroll
      for (int t = 0; t < 8; ++t) {
        f32x16 s = mfma32(kf[t], qf, z);
#pragma unroll
        for (int i = 0; i < 16; ++i) m = fmaxf(m, s[i]);
      }
      m = fmaxf(m, __shfl_xor(m, 32));
      if (h == 0) mx[w * 256 + g * 32 + r] = m;
    }
    __syncthreads();
  }
  const char* wop = ws + OFF_WO + (size_t)(4 * w * 16) * KB + lane * 16;
  f32x4 oacc[4] = {};
  bf16x8 wof[8];
  bf16x8 qf = *(const bf16x8*)(qp);
#pragma unroll 1
  for (int g = 0; g <= 8; ++g) {
    if (g < 8) {
      if (slow) {
        const float* mx = (const float*)(lds + AL_MX);
        float m = mx[g * 32 + r];
#pragma unroll
        for (int i = 1; i < 8; ++i) m = fmaxf(m, mx[i * 256 + g * 32 + r]);
#pragma unroll
        for (int i = 0; i < 16; ++i) cinit[i] = -m;
      }
      bf16x8 qn = *(const bf16x8*)(qp + (g < 7 ? g + 1 : 7) * 128);
      f32x16 o = {};
#pragma unroll
      for (int t = 0; t < 8; ++t) {
        f32x16 s = mfma32(kf[t], qf, cinit);
        u32x4 p0, p1;
#pragma unroll
        for (int i = 0; i < 4; ++i) {
          p0[i] = pk2(__builtin_amdgcn_exp2f(s[2 * i]), __builtin_amdgcn_exp2f(s[2 * i + 1]));
          p1[i] = pk2(__builtin_amdgcn_exp2f(s[8 + 2 * i]), __builtin_amdgcn_exp2f(s[8 + 2 * i + 1]));
        }
        o = mfma32(vf[2 * t], __builtin_bit_cast(bf16x8, p0), o);
        o = mfma32(vf[2 * t + 1], __builtin_bit_cast(bf16x8, p1), o);
      }
      qf = qn;
      char* ro = lds + AL_RO + ((g & 1) * 8 + w) * 2048 + lane * 16;
      *(f32x4*)(ro) = (f32x4){o[0], o[1], o[2], o[3]};
      *(f32x4*)(ro + 1024) = (f32x4){o[4], o[5], o[6], o[7]};
      if (h == 0) ((float*)(lds + AL_RL))[((g & 1) * 8 + w) * 32 + r] = o[8];
    }
    __syncthreads();
    if (g < 8 && (w >> 1) == (g & 3)) {
      const int half = w & 1;
      f32x4 a = {}; float l = 0.f;
#pragma unroll
      for (int i = 0; i < 8; ++i) {
        a += *(const f32x4*)(lds + AL_RO + ((g & 1) * 8 + i) * 2048 + half * 1024 + lane * 16);
        l += ((const float*)(lds + AL_RL))[((g & 1) * 8 + i) * 32 + r];
      }
      const float inv = 1.f / l;
      u32x2 ob = {pk2(a[0] * inv, a[1] * inv), pk2(a[2] * inv, a[3] * inv)};
      const int hl = r >> 3, tok = r & 7;
      *(u32x2*)(lds + AL_O + (g & 1) * 2048 + (hl >> 1) * 1024 + ((2 * half + h) * 16 + tok) * 16 + (hl & 1) * 8) = ob;
    }
    if (g >= 1) {
      const int gp = g - 1;
      bf16x8 of0 = lds_frag(lds, AL_O + (gp & 1) * 2048 + lane * 16);
      bf16x8 of1 = lds_frag(lds, AL_O + (gp & 1) * 2048 + 1024 + lane * 16);
#pragma unroll
      for (int t = 0; t < 4; ++t) { oacc[t] = mfma16(wof[2 * t], of0, oacc[t]); oacc[t] = mfma16(wof[2 * t + 1], of1, oacc[t]); }
    }
    if (g < 8) {
#pragma unroll
      for (int t = 0; t < 4; ++t) {
        wof[2 * t] = *(const bf16x8*)(wop + (size_t)(t * 16 + 2 * g) * KB);
        wof[2 * t + 1] = *(const bf16x8*)(wop + (size_t)(t * 16 + 2 * g + 1) * KB);
      }
    }
  }
  const int g4 = lane >> 4, mm = lane & 15;
  const int mt = t0 >> 4, mrow = (t0 & 15) + (mm & 7);
  const size_t fo = (size_t)(mt * 16 + 2 * w) * KB + (g4 * 16 + mrow) * 16;
  float xv[16];
#pragma unroll
  for (int p = 0; p < 2; ++p) {
    u32x4 xr = *(const u32x4*)(xres + fo + p * KB);
    u32x4 o;
#pragma unroll
    for (int q = 0; q < 4; ++q) {
      const f32x4 a = oacc[2 * p + (q >> 1)];
      o[q] = pk2(bflo(xr[q]) + a[(q & 1) * 2], bfhi(xr[q]) + a[(q & 1) * 2 + 1]);
      xv[p * 8 + q * 2] = bflo(o[q]); xv[p * 8 + q * 2 + 1] = bfhi(o[q]);
    }
    if (mm < 8) *(u32x4*)(ws + OFF_X1 + fo + p * KB) = o;
  }
  float sm = 0.f;
#pragma unroll
  for (int i = 0; i < 16; ++i) sm += xv[i];
  sm += __shfl_xor(sm, 16); sm += __shfl_xor(sm, 32);
  const float mw = sm * (1.f / 64.f);
  float m2 = 0.f;
#pragma unroll
  for (int i = 0; i < 16; ++i) { float d = xv[i] - mw; m2 += d * d; }
  m2 += __shfl_xor(m2, 16); m2 += __shfl_xor(m2, 32);
  float* st = (float*)(lds + AL_ST);
  if (lane < 8) { st[(w * 8 + lane) * 2] = mw; st[(w * 8 + lane) * 2 + 1] = m2; }
  __syncthreads();
  float mean = 0.f;
#pragma unroll
  for (int i = 0; i < 8; ++i) mean += st[(i * 8 + (mm & 7)) * 2];
  mean *= 0.125f;
  float M2 = 0.f;
#pragma unroll
  for (int i = 0; i < 8; ++i) { float d = st[(i * 8 + (mm & 7)) * 2] - mean; M2 += st[(i * 8 + (mm & 7)) * 2 + 1] + 64.f * d * d; }
  const float rstd = rsqrtf(M2 * (1.f / D_) + LN_EPS);
#pragma unroll
  for (int p = 0; p < 2; ++p) {
    u32x4 o;
#pragma unroll
    for (int q = 0; q < 4; ++q) o[q] = pk2((xv[p * 8 + q * 2] - mean) * rstd, (xv[p * 8 + q * 2 + 1] - mean) * rstd);
    if (mm < 8) *(u32x4*)(ws + OFF_XN1 + fo + p * KB) = o;
  }
}

__global__ __launch_bounds__(512, 2) void k_ffn1(char* __restrict__ ws) {
  extern __shared__ __attribute__((aligned(16))) char lds[];
  const int tid = threadIdx.x, lane = tid & 63;
  const int w = __builtin_amdgcn_readfirstlane(tid >> 6);
  const int xcd = blockIdx.x & 7, idx = blockIdx.x >> 3, rb = idx >> 4, cb = idx & 15;
  const int mt0 = xcd * 16 + rb * 8, nt0 = cb * 7;
  const unsigned lds0 = (unsigned)(uintptr_t)lds;
  const char* asrc = ws + OFF_W1 + (size_t)((nt0 + (w < 7 ? w : 0)) * 16) * KB + lane * 16;
  const char* bsrc = ws + OFF_XN1 + (size_t)((mt0 + w) * 16) * KB + lane * 16;
  auto issue = [&](int s) {
    const unsigned slot = lds0 + (s % 5) * 30720;
#pragma unroll
    for (int kk = 0; kk < 2; ++kk) {
      glds16(bsrc + (size_t)(2 * s + kk) * KB, slot + (kk * 15 + 7 + w) * 1024);
      if (w < 7) glds16(asrc + (size_t)(2 * s + kk) * KB, slot + (kk * 15 + w) * 1024);
    }
  };
  const int mp = w & 3, nh = w >> 2;
  f32x4 acc[4][2] = {};
  issue(0); issue(1); issue(2); issue(3);
#pragma unroll
  for (int s = 0; s < 8; ++s) {
    const int ahead = (7 - s) < 3 ? (7 - s) : 3;
    if (w < 7) { if (ahead == 3) wait_vm<12>(); else if (ahead == 2) wait_vm<8>(); else if (ahead == 1) wait_vm<4>(); else wait_vm<0>(); }
    else       { if (ahead == 3) wait_vm<6>(); else if (ahead == 2) wait_vm<4>(); else if (ahead == 1) wait_vm<2>(); else wait_vm<0>(); }
    raw_barrier();
    if (s + 4 < 8) issue(s + 4);
#pragma unroll
    for (int kk = 0; kk < 2; ++kk) {
      const int base = (s % 5) * 30720 + kk * 15 * 1024 + lane * 16;
      bf16x8 b0 = lds_frag(lds, base + (7 + 2 * mp) * 1024);
      bf16x8 b1 = lds_frag(lds, base + (8 + 2 * mp) * 1024);
#pragma unroll
      for (int t = 0; t < 4; ++t) {
        if (t < 3 || nh == 0) {
          bf16x8 a = lds_frag(lds, base + (4 * nh + t) * 1024);
          acc[t][0] = mfma16(a, b0, acc[t][0]);
          acc[t][1] = mfma16(a, b1, acc[t][1]);
        }
      }
    }
  }
#pragma unroll
  for (int t = 0; t < 4; ++t) {
    if (t < 3 || nh == 0) {
      const int nt = nt0 + 4 * nh + t;
#pragma unroll
      for (int m = 0; m < 2; ++m) {
        f32x4 a = acc[t][m];
        u32x2 o = {pk2(fmaxf(a[0], 0.f), fmaxf(a[1], 0.f)), pk2(fmaxf(a[2], 0.f), fmaxf(a[3], 0.f))};
        *(u32x2*)(ws + OFF_H + (size_t)((mt0 + 2 * mp + m) * 56 + (nt >> 1)) * KB + lane * 16 + (nt & 1) * 8) = o;
      }
    }
  }
}

template <bool FINAL>
__global__ __launch_bounds__(512, 2) void k_ffn2(char* __restrict__ ws, u16* __restrict__ out) {
  extern __shared__ __attribute__((aligned(16))) char lds[];
  const int tid = threadIdx.x, lane = tid & 63, g = lane >> 4, mm = lane & 15;
  const int w = __builtin_amdgcn_readfirstlane(tid >> 6);
  const int xcd = blockIdx.x & 7, idx = blockIdx.x >> 3, rb = idx >> 3, cb = idx & 7;
  const int mt0 = xcd * 16 + rb * 4, nt0 = cb * 4;
  const unsigned lds0 = (unsigned)(uintptr_t)lds;
  const char* src = (w < 4 ? ws + OFF_W2 + (size_t)((nt0 + w) * 56) * KB : ws + OFF_H + (size_t)((mt0 + w - 4) * 56) * KB) + lane * 16;
  constexpr int R = 9, NS = 28;
  auto issue = [&](int s, int slot) {
    glds16(src + (size_t)(2 * s) * KB, lds0 + slot * 16384 + w * 1024);
    glds16(src + (size_t)(2 * s + 1) * KB, lds0 + slot * 16384 + (8 + w) * 1024);
  };
  const int mt = w & 3, np = w >> 2;
  f32x4 acc0 = {}, acc1 = {};
#pragma unroll
  for (int s = 0; s < R - 1; ++s) issue(s, s);
  int slot = 0;
  auto comp = [&](int sl) {
#pragma unroll
    for (int kk = 0; kk < 2; ++kk) {
      const int base = sl * 16384 + kk * 8192 + lane * 16;
      bf16x8 b = lds_frag(lds, base + (4 + mt) * 1024);
      bf16x8 a0 = lds_frag(lds, base + (2 * np) * 1024);
      bf16x8 a1 = lds_frag(lds, base + (2 * np + 1) * 1024);
      acc0 = mfma16(a0, b, acc0);
      acc1 = mfma16(a1, b, acc1);
    }
  };
#pragma unroll 1
  for (int s = 0; s < NS - (R - 1); ++s) {
    wait_vm<2 * (R - 2)>();
    raw_barrier();
    issue(s + R - 1, slot == 0 ? R - 1 : slot - 1);
    comp(slot);
    slot = slot == R - 1 ? 0 : slot + 1;
  }
#pragma unroll
  for (int i = 0; i < R - 1; ++i) {
    if (i == 0) wait_vm<14>(); else if (i == 1) wait_vm<12>(); else if (i == 2) wait_vm<10>(); else if (i == 3) wait_vm<8>();
    else if (i == 4) wait_vm<6>(); else if (i == 5) wait_vm<4>(); else if (i == 6) wait_vm<2>(); else wait_vm<0>();
    raw_barrier();
    comp(slot);
    slot = slot == R - 1 ? 0 : slot + 1;
  }
  const size_t fo = (size_t)((mt0 + mt) * 16 + (nt0 >> 1) + np) * KB + lane * 16;
  u32x4 xr = *(const u32x4*)(ws + OFF_X1 + fo);
  u32x4 o;
  o[0] = pk2(bflo(xr[0]) + acc0[0], bfhi(xr[0]) + acc0[1]);
  o[1] = pk2(bflo(xr[1]) + acc0[2], bfhi(xr[1]) + acc0[3]);
  o[2] = pk2(bflo(xr[2]) + acc1[0], bfhi(xr[2]) + acc1[1]);
  o[3] = pk2(bflo(xr[3]) + acc1[2], bfhi(xr[3]) + acc1[3]);
  if (FINAL) {
    u16* op = out + (size_t)((mt0 + mt) * 16 + mm) * D_ + (nt0 + 2 * np) * 16 + 4 * g;
    *(u32x2*)op = (u32x2){o[0], o[1]};
    *(u32x2*)(op + 16) = (u32x2){o[2], o[3]};
  } else {
    *(u32x4*)(ws + OFF_X2 + fo) = o;
  }
}

extern "C" void kernel_launch(void* const* d_in, const int* in_sizes, int n_in, void* d_out, int out_size, void* d_ws, size_t ws_size,
                              hipStream_t stream) {
  const u16* x = (const u16*)d_in[0];
  const u16* wq = (const u16*)d_in[1];
  const u16* wk = (const u16*)d_in[2];
  const u16* wv = (const u16*)d_in[3];
  const u16* wo = (const u16*)d_in[4];
  const u16* w1 = (const u16*)d_in[6];
  const u16* w2 = (const u16*)d_in[7];
  char* ws = (char*)d_ws;
  (void)hipFuncSetAttribute((const void*)k_qkv, hipFuncAttributeMaxDynamicSharedMemorySize, 147456);
  (void)hipFuncSetAttribute((const void*)k_ffn1, hipFuncAttributeMaxDynamicSharedMemorySize, 153600);
  (void)hipFuncSetAttribute((const void*)k_ffn2<false>, hipFuncAttributeMaxDynamicSharedMemorySize, 147456);
  (void)hipFuncSetAttribute((const void*)k_ffn2<true>, hipFuncAttributeMaxDynamicSharedMemorySize, 147456);
  k_prep<<<290 + 128, 1024, 0, stream>>>(x, wq, wk, wv, wo, w1, w2, ws);
  for (int layer = 0; layer < 2; ++layer) {
    const char* xin = ws + (layer == 0 ? OFF_X0 : OFF_X2);
    k_qkv<<<256, 512, 147456, stream>>>(xin, ws);
    k_attn<<<256, 512, AL_BYTES, stream>>>(xin, ws);
    k_ffn1<<<256, 512, 153600, stream>>>(ws);
    if (layer == 0) k_ffn2<false><<<256, 512, 147456, stream>>>(ws, (u16*)d_out);
    else            k_ffn2<true><<<256, 512, 147456, stream>>>(ws, (u16*)d_out);
  }
}
```

```cpp
#include <hip/hip_runtime.h>
#include <hip/hip_bf16.h>
#include <stdint.h>
#include <type_traits>

typedef __attribute__((ext_vector_type(8))) short bf16x8;
typedef __attribute__((ext_vector_type(2))) float f32x2;
typedef __attribute__((ext_vector_type(4))) float f32x4;
typedef __attribute__((ext_vector_type(16))) float f32x16;
typedef __attribute__((ext_vector_type(4))) unsigned u32x4;
typedef __attribute__((ext_vector_type(2))) unsigned u32x2;
typedef unsigned short u16;
typedef __amdgpu_buffer_rsrc_t rsrc_t;
typedef __attribute__((address_space(1))) unsigned gu32;
#define DI __device__ __forceinline__
#define LAS __attribute__((address_space(3)))

constexpr int S_ = 2048, D_ = 512, DFF = 1792;
constexpr float QSCALE = 0.25f * 1.4426950408889634f;
constexpr float LN_EPS = 1e-5f;

constexpr unsigned KB = 1024;
constexpr unsigned OFF_FLAG = 0;
constexpr unsigned FLAG_BYTES = 96 * KB;
constexpr unsigned OFF_WQKV = OFF_FLAG + FLAG_BYTES;
constexpr unsigned OFF_WO   = OFF_WQKV + 544 * KB;
constexpr unsigned OFF_W1   = OFF_WO + 512 * KB;
constexpr unsigned OFF_W2   = OFF_W1 + 1792 * KB;
constexpr unsigned OFF_X2   = OFF_W2 + 1792 * KB;
constexpr unsigned OFF_X1   = OFF_X2 + 2048 * KB;
constexpr unsigned OFF_ST   = OFF_X1 + 2048 * KB;
constexpr unsigned OFF_H    = OFF_ST + 128 * KB;
constexpr unsigned OFF_QKV  = OFF_H + 7168 * KB;
constexpr unsigned QO_Q = 0;
constexpr unsigned QO_K = QO_Q + 2048 * KB;
constexpr unsigned QO_VT = QO_K + 64 * KB;
constexpr unsigned QO_KN2 = QO_VT + 64 * KB;
constexpr unsigned QO_QN2 = QO_KN2 + 1 * KB;
constexpr unsigned QKV_BYTES = QO_QN2 + 8 * KB;
constexpr unsigned WS_NEED  = OFF_QKV + 2 * QKV_BYTES;
DI unsigned flag_off(int layer, int edge, int line) { return OFF_FLAG + ((layer * 3 + edge) * 128 + line) * 128; }

DI float bf2f(u16 v) { return __uint_as_float(((unsigned)v) << 16); }
DI float bflo(unsigned v) { return __uint_as_float(v << 16); }
DI float bfhi(unsigned v) { return __uint_as_float(v & 0xffff0000u); }
DI unsigned pk2(float lo, float hi) {
  typedef __attribute__((ext_vector_type(2))) __bf16 bf16x2_;
  f32x2 v = {lo, hi};
  bf16x2_ r = __builtin_convertvector(v, bf16x2_);
  return __builtin_bit_cast(unsigned, r);
}
DI unsigned relu_pk(unsigned v) {
  typedef __attribute__((ext_vector_type(2))) short s16x2;
  const s16x2 z = {0, 0};
  return __builtin_bit_cast(unsigned, __builtin_elementwise_max(__builtin_bit_cast(s16x2, v), z));
}
DI f32x4 mfma16(bf16x8 a, bf16x8 b, f32x4 c) {
  typedef __attribute__((ext_vector_type(8))) __bf16 b8;
  return __builtin_amdgcn_mfma_f32_16x16x32_bf16(__builtin_bit_cast(b8, a), __builtin_bit_cast(b8, b), c, 0, 0, 0);
}
DI f32x16 mfma32(bf16x8 a, bf16x8 b, f32x16 c) {
  typedef __attribute__((ext_vector_type(8))) __bf16 b8;
  return __builtin_amdgcn_mfma_f32_32x32x16_bf16(__builtin_bit_cast(b8, a), __builtin_bit_cast(b8, b), c, 0, 0, 0);
}
template <int N> DI void wait_vm() { asm volatile("s_waitcnt vmcnt(%0)" :: "i"(N) : "memory"); }
DI void raw_barrier() { asm volatile("s_waitcnt lgkmcnt(0)\n\ts_barrier" ::: "memory"); }
DI bf16x8 lds_frag(const char* lds, int off) { return *(const LAS bf16x8*)((const LAS char*)lds + off); }
#define STAMP(kid, i)
DI int kperm(int g, int j) { return (j < 4) ? 4 * g + j : 16 + 4 * g + (j - 4); }

template <int CTRL> DI float dpp(float x) { return __builtin_bit_cast(float, __builtin_amdgcn_mov_dpp(__builtin_bit_cast(int, x), CTRL, 0xf, 0xf, true)); }
template <int CTRL> DI unsigned dppu(unsigned x) { return (unsigned)__builtin_amdgcn_mov_dpp((int)x, CTRL, 0xf, 0xf, true); }
DI unsigned umax(unsigned a, unsigned b) { return a > b ? a : b; }
DI unsigned row16_umax(unsigned x) { x = umax(x, dppu<0xB1>(x)); x = umax(x, dppu<0x4E>(x)); x = umax(x, dppu<0x141>(x)); x = umax(x, dppu<0x140>(x)); return x; }
DI unsigned xrow_umax(unsigned x) {
  auto s = __builtin_amdgcn_permlane16_swap(x, x, false, false); x = umax(s[0], s[1]);
  auto t = __builtin_amdgcn_permlane32_swap(x, x, false, false); return umax(t[0], t[1]); }
DI float xrow_sum(float x) {
  auto s = __builtin_amdgcn_permlane16_swap(__float_as_uint(x), __float_as_uint(x), false, false);
  x = __uint_as_float(s[0]) + __uint_as_float(s[1]);
  auto t = __builtin_amdgcn_permlane32_swap(__float_as_uint(x), __float_as_uint(x), false, false);
  return __uint_as_float(t[0]) + __uint_as_float(t[1]); }
DI void xrow_sum2(float& a, float& b) {
  auto s = __builtin_amdgcn_permlane16_swap(__float_as_uint(a), __float_as_uint(b), false, false);
  const float x = __uint_as_float(s[0]) + __uint_as_float(s[1]);
  auto t = __builtin_amdgcn_permlane32_swap(__float_as_uint(x), __float_as_uint(x), false, false);
  const float y = __uint_as_float(t[0]) + __uint_as_float(t[1]);
  auto u = __builtin_amdgcn_permlane16_swap(__float_as_uint(y), __float_as_uint(y), false, false);
  a = __uint_as_float(u[0]); b = __uint_as_float(u[1]); }
DI float sq4(u32x2 o) { return bflo(o[0]) * bflo(o[0]) + bfhi(o[0]) * bfhi(o[0]) + bflo(o[1]) * bflo(o[1]) + bfhi(o[1]) * bfhi(o[1]); }
#define SB() __builtin_amdgcn_sched_barrier(0)
#define EX(x) __builtin_amdgcn_exp2f(x)
#define PIN1(a) asm volatile("" : "+v"(a))
#define PIN2(a, b) asm volatile("" : "+v"(a), "+v"(b))
#define PIN4(a, b, c, d) asm volatile("" : "+v"(a), "+v"(b), "+v"(c), "+v"(d))
constexpr int SC1 = 16;
DI void mfma16_ip(f32x4& acc, bf16x8 a, bf16x8 b) { asm volatile("v_mfma_f32_16x16x32_bf16 %0, %1, %2, %0" : "+v"(acc) : "v"(a), "v"(b)); }
template <int AUX = 0> DI bf16x8 bld(rsrc_t rs, unsigned voff, unsigned soff) { return __builtin_bit_cast(bf16x8, __builtin_amdgcn_raw_buffer_load_b128(rs, voff, soff, AUX)); }
template <int AUX = 0> DI u32x4 bld4(rsrc_t rs, unsigned voff, unsigned soff) { return __builtin_amdgcn_raw_buffer_load_b128(rs, voff, soff, AUX); }
template <int AUX = 0> DI f32x4 bld4f(rsrc_t rs, unsigned voff, unsigned soff) { return __builtin_bit_cast(f32x4, __builtin_amdgcn_raw_buffer_load_b128(rs, voff, soff, AUX)); }
template <int AUX = 0> DI unsigned bld1(rsrc_t rs, unsigned voff, unsigned soff) { return __builtin_amdgcn_raw_buffer_load_b32(rs, voff, soff, AUX); }
template <int AUX = 0> DI void bst4(u32x4 v, rsrc_t rs, unsigned voff, unsigned soff) {
  __builtin_amdgcn_raw_buffer_store_b128(v, rs, voff, soff, AUX);
  asm volatile("s_nop 1" :: "v"(v) : "memory");
}
template <int AUX = 0> DI void bst2(u32x2 v, rsrc_t rs, unsigned voff, unsigned soff) { __builtin_amdgcn_raw_buffer_store_b64(v, rs, voff, soff, AUX); }
template <int AUX = 0> DI void bst1(unsigned v, rsrc_t rs, unsigned voff, unsigned soff) { __builtin_amdgcn_raw_buffer_store_b32(v, rs, voff, soff, AUX); }
constexpr unsigned OOB = 0x7ffffff0u;
DI void dma(rsrc_t rs, unsigned voff, unsigned soff, unsigned lds_dst) {
  unsigned sv;
  asm volatile("s_mov_b32 %0, m0\n\ts_mov_b32 m0, %1\n\ts_nop 0\n\tbuffer_load_dwordx4 %2, %3, %4 offen lds\n\ts_mov_b32 m0, %0"
               : "=&s"(sv) : "s"(lds_dst), "v"(voff), "s"(rs), "s"(soff) : "memory");
}
DI void wg_signal(char* ws, unsigned foff, int tid) {
  asm volatile("s_waitcnt vmcnt(0)" ::: "memory");
  raw_barrier();
  if (tid == 0) __hip_atomic_store((gu32*)(ws + foff), 1u, __ATOMIC_RELAXED, __HIP_MEMORY_SCOPE_AGENT);
}
DI void wave_wait(char* ws, unsigned foff, unsigned lane_off) {
  gu32* f = (gu32*)(ws + foff + lane_off);
#pragma unroll 1
  for (unsigned spins = 0; spins < (1u << 22); ++spins) {
    const unsigned v = __hip_atomic_load(f, __ATOMIC_RELAXED, __HIP_MEMORY_SCOPE_AGENT);
    if (__all(v != 0u)) break;
#define POLL_SLEEP 1
    __builtin_amdgcn_s_sleep(POLL_SLEEP);
  }
  asm volatile("" ::: "memory");
}

constexpr int LDS_BYTES = 163840;
constexpr int AL_RO = 0;
constexpr int AL_RL = AL_RO + 32768;
constexpr int AL_O  = AL_RL + 2048;
constexpr int AL_Q  = AL_O + 4096;
constexpr int AL_FL = AL_Q + 8192;
constexpr int AL_JUNK = AL_FL + 256;
constexpr int AL_W1 = 49152;
constexpr int AL_MX = AL_W1;
static_assert(AL_JUNK + 1024 <= AL_W1 && AL_W1 + 112 * 1024 <= LDS_BYTES, "lds");
constexpr float SHIFT = 32.f;

DI void dma_w1(rsrc_t rs, unsigned lds0, unsigned l16, int nt0, int w, int t, int kk) {
  dma(rs, l16, OFF_W1 + ((nt0 + t) * 16 + 2 * w + kk) * KB, lds0 + AL_W1 + ((2 * w + kk) * 7 + t) * 1024);
}

DI void attn_phase(char* lds, char* ws, rsrc_t rs, const u16* xin, int layer, int tid) {
  const int lane = tid & 63, r = lane & 31, h = lane >> 5;
  const int w = __builtin_amdgcn_readfirstlane(tid >> 6);
  const int xcd = blockIdx.x & 7, idx = blockIdx.x >> 3;
  const int t0 = xcd * 256 + idx * 8;
  const unsigned lds0 = (unsigned)(uintptr_t)lds;
  const unsigned qkv = OFF_QKV + layer * QKV_BYTES;
  const unsigned l16 = lane * 16;
  const int f1_nt0 = (idx & 15) * 7;
  STAMP(1, 0);
  const unsigned qs = qkv + QO_Q + t0 * 1024;
  const unsigned qo = (r & 7) * 1024 + (r >> 3) * 32 + 16 * h;
  bf16x8 qf = bld(rs, qo, qs);
  SB();
  bf16x8 kf[8], vf[16];
  auto load_kv = [&](unsigned base) {
    const unsigned kb = base + QO_K + w * 8192, vb = base + QO_VT + w * 8192;
    const unsigned ko = r * 32 + 16 * h, vo = (r >> 4) * 512 + h * 256 + (r & 15) * 16;
    kf[0] = bld(rs, ko, kb);
    SB();
#pragma unroll
    for (int t = 0; t < 8; ++t) {
      if (t < 7) kf[t + 1] = bld(rs, ko + ((t + 1) & 3) * 1024, kb + ((t + 1) >> 2) * 4096);
      vf[2 * t] = bld(rs, vo + (t & 3) * 1024, vb + (t >> 2) * 4096);
      SB();
    }
  };
  load_kv(qkv);
  bf16x8 qn = bld(rs, qo + 128, qs);
  const unsigned kn_a = bld1(rs, lane * 4, qkv + QO_KN2), kn_b = bld1(rs, lane * 4 + 256, qkv + QO_KN2);
  const unsigned qn_a = bld1(rs, (lane & 15) * 4, qkv + QO_QN2 + (t0 >> 4) * 64);
  dma(rs, qo, qs + w * 128, lds0 + AL_Q + w * 1024);
  *(u32x2*)(lds + AL_O + tid * 8) = (u32x2){0u, 0u};
  ((unsigned*)(lds + AL_FL))[lane] = h;
  const unsigned vconst = (lane & 15) == 0 ? 0x3F803F80u : 0u;
  auto vsplit = [&](int t) {
    u32x4 x = __builtin_bit_cast(u32x4, vf[2 * t]), y;
#pragma unroll
    for (int i = 0; i < 4; ++i) { auto sw = __builtin_amdgcn_permlane16_swap(x[i], vconst, false, false); x[i] = sw[0]; y[i] = sw[1]; }
    vf[2 * t] = __builtin_bit_cast(bf16x8, x); vf[2 * t + 1] = __builtin_bit_cast(bf16x8, y);
  };
  const unsigned wos = OFF_WO + (4 * w * 16) * KB;
  f32x4 oacc[4] = {};
  f32x16 o;
  char* const ro_w = lds + AL_RO + w * 2048 + lane * 8;
  const int lf_w0 = h == 0 ? AL_RL + (r * 8 + w) * 4 : AL_FL + (w * 4 + (r & 3)) * 4, lf_step = h == 0 ? 1024 : 0;
  const int o_wr = AL_O + (r >> 4) * 1024 + ((2 * ((w & 3) >> 1) + h) * 16 + (w >> 2) * 8 + (r & 7)) * 16 + ((r >> 3) & 1) * 8 + (w & 1) * 4;
  auto publish = [&](int g) {
    char* ro = ro_w + (g & 1) * 16384;
    *(f32x2*)(ro) = (f32x2){o[0], o[1]}; *(f32x2*)(ro + 512) = (f32x2){o[2], o[3]};
    *(f32x2*)(ro + 1024) = (f32x2){o[4], o[5]}; *(f32x2*)(ro + 1536) = (f32x2){o[6], o[7]};
    asm volatile("" ::: "memory");
    *(volatile LAS unsigned*)((LAS char*)lds + lf_w0 + (g & 1) * lf_step) = h == 0 ? __float_as_uint(o[8]) : (unsigned)(g + 1);
  };
  f32x2 ra[4]; f32x4 rl0, rl1;
  const char* const red_r = lds + AL_RO + (w >> 2) * 8192 + (w & 3) * 512 + lane * 8;
  auto reduce_ld = [&](int g) {
#pragma unroll
    for (int i = 0; i < 4; ++i) ra[i] = *(const f32x2*)(red_r + (g & 1) * 16384 + i * 2048);
    rl0 = *(const f32x4*)(lds + AL_RL + (g & 1) * 1024 + r * 32); rl1 = *(const f32x4*)(lds + AL_RL + (g & 1) * 1024 + r * 32 + 16);
  };
  auto reduce_fin = [&](int g) {
    const f32x4 l4 = rl0 + rl1;
    const f32x2 l2 = (f32x2){l4[0], l4[1]} + (f32x2){l4[2], l4[3]};
    const float inv = __builtin_amdgcn_rcpf(l2[0] + l2[1]);
    const f32x2 sa = (ra[0] + ra[1]) + (ra[2] + ra[3]);
    *(unsigned*)(lds + o_wr + (g & 1) * 2048) = pk2(sa[0] * inv, sa[1] * inv);
    asm volatile("" ::: "memory");
    *(volatile LAS unsigned*)((LAS char*)lds + AL_FL + 128 + w * 16 + (lane & 3) * 4) = (unsigned)(g + 2);
  };
  auto reduce = [&](int g) { reduce_ld(g); reduce_fin(g); };
  const int g4 = lane >> 4, mm = lane & 15;
  const unsigned fs = ((t0 >> 4) * 16 + 2 * w) * KB, fv = (mm >> 3) * 1024 + (g4 * 16 + (t0 & 15) + (mm & 7)) * 16;
  u32x4 xr;
  auto load_res = [&]() {
    if (layer == 0) {
      const u16* xp = xin + (size_t)(t0 + (mm & 7)) * D_ + 64 * w + 32 * (mm >> 3) + 4 * g4;
      const u32x2 lo = *(const u32x2*)xp, hi = *(const u32x2*)(xp + 16);
      xr = (u32x4){lo[0], lo[1], hi[0], hi[1]};
    } else xr = bld4(rs, fv, OFF_X2 + fs);
  };
  f32x16 cinit;
#pragma unroll
  for (int i = 0; i < 16; ++i) cinit[i] = -SHIFT;
  PIN1(cinit);
  bf16x8 wof[8], of0, of1;
  u32x4 p0, p1;
  unsigned flg;
  const unsigned fl_addr = lds0 + AL_FL + lane * 4;
  STAMP(1, 1);
  f32x16 sc = mfma32(kf[0], qf, cinit);
  auto group = [&](auto first_tag, const int g) {
    constexpr bool FIRST = decltype(first_tag)::value;
#pragma unroll
    for (int t = 0; t < 8; ++t) {
      if (t == 3) __builtin_amdgcn_s_setprio(3); if (t == 5) __builtin_amdgcn_s_setprio(2); if (t == 7) __builtin_amdgcn_s_setprio(1); if (t == 1) __builtin_amdgcn_s_setprio(0);
      const f32x16 sn = mfma32(kf[(t + 1) & 7], t < 7 ? qf : qn, cinit);
      SB();
      if (FIRST) { vsplit(t); SB(); }
      float e0 = EX(sc[0]), e1 = EX(sc[1]), e2 = EX(sc[2]), e3 = EX(sc[3]), e4 = EX(sc[4]), e5 = EX(sc[5]);
      PIN4(e0, e1, e2, e3); PIN2(e4, e5); SB();
      if (t > 0 || !FIRST) o = mfma32(vf[(2 * t + 15) & 15], __builtin_bit_cast(bf16x8, p1), o);
      SB();
      float e6 = EX(sc[6]), e7 = EX(sc[7]);
      PIN2(e6, e7); SB();
      p0[0] = pk2(e0, e1); p0[1] = pk2(e2, e3); p0[2] = pk2(e4, e5); p0[3] = pk2(e6, e7);
      PIN1(p0); SB();
      float e8 = EX(sc[8]), e9 = EX(sc[9]);
      PIN2(e8, e9); SB();
      float e10, e11, e12, e13;
      if (t == 0) {
        if (!FIRST) { e10 = EX(sc[10]); e11 = EX(sc[11]); e12 = EX(sc[12]); e13 = EX(sc[13]); PIN4(e10, e11, e12, e13); PIN1(o); SB(); publish(g - 1); SB(); }
        f32x16 z = {}; o = mfma32(vf[0], __builtin_bit_cast(bf16x8, p0), z);
      } else o = mfma32(vf[2 * t], __builtin_bit_cast(bf16x8, p0), o);
      SB();
      if (t > 0 || FIRST) { e10 = EX(sc[10]); e11 = EX(sc[11]); e12 = EX(sc[12]); e13 = EX(sc[13]); PIN4(e10, e11, e12, e13); SB(); }
      if (!FIRST && t >= 4) { mfma16_ip(oacc[t - 4], wof[2 * (t - 4)], of0); SB(); }
      float e14 = EX(sc[14]), e15 = EX(sc[15]);
      PIN2(e14, e15); SB();
      if (!FIRST && t >= 4) { mfma16_ip(oacc[t - 4], wof[2 * (t - 4) + 1], of1); SB(); }
      p1[0] = pk2(e8, e9); p1[1] = pk2(e10, e11); p1[2] = pk2(e12, e13); p1[3] = pk2(e14, e15);
      PIN1(p1); SB();
      if (t >= 4) {
        const int gw = FIRST ? 0 : g - 1;
        wof[2 * (t - 4)] = bld(rs, l16, wos + ((t - 4) * 16 + 2 * gw) * 1024);
        wof[2 * (t - 4) + 1] = bld(rs, l16, wos + ((t - 4) * 16 + 2 * gw + 1) * 1024);
        SB();
      }
      if (!FIRST && (t == 1 || t == 2)) { dma_w1(rs, lds0, l16, f1_nt0, w, g - 1, t - 1); SB(); }
      if (!FIRST && t == 0) { dma(rs, l16, OFF_W2 + ((idx * 7 + g - 1) * 8 + w) * KB, lds0 + AL_JUNK); SB(); }
      if (!FIRST && t == 1) { flg = *(volatile LAS unsigned*)((LAS char*)lds + AL_FL + lane * 4); SB(); }
      if (!FIRST && t == 2) {
        asm volatile("1:\n\tv_cmp_gt_u32 vcc, %2, %0\n\ts_cbranch_vccz 2f\n\ts_sleep 1\n\tds_read_b32 %0, %1\n\ts_waitcnt lgkmcnt(0)\n\ts_branch 1b\n2:"
                     : "+v"(flg) : "v"(fl_addr), "s"(g) : "vcc", "memory");
        reduce_ld(g - 1);
        SB();
      }
      if (!FIRST && t == 3) {
        reduce_fin(g - 1);
        SB();
        of0 = lds_frag(lds, AL_O + (g & 1) * 2048 + l16);
        of1 = lds_frag(lds, AL_O + (g & 1) * 2048 + 1024 + l16);
        SB();
      }
      if (!FIRST && t == 5) { qn = lds_frag(lds, AL_Q + (g < 7 ? g + 1 : 7) * 1024 + l16); SB(); }
      sc = sn;
    }
    qf = qn;
  };
  group(std::true_type{}, 0);
  const float kq2 = __uint_as_float(xrow_umax(row16_umax(umax(kn_a, kn_b)))) * __uint_as_float(row16_umax(qn_a));
  const bool slow = __builtin_amdgcn_readfirstlane(kq2 <= SHIFT * SHIFT * 0.998f ? 0 : 1) != 0;
  wait_vm<8>();
  raw_barrier();
  STAMP(1, 2);
  if (!slow) {
#pragma unroll 1
    for (int g = 1; g < 8; ++g) group(std::false_type{}, g);
    __builtin_amdgcn_s_setprio(0);
    o = mfma32(vf[15], __builtin_bit_cast(bf16x8, p1), o);
    bf16x8 wl[8];
#pragma unroll
    for (int t = 0; t < 4; ++t) {
      wl[2 * t] = bld(rs, l16, wos + (t * 16 + 14) * 1024);
      wl[2 * t + 1] = bld(rs, l16, wos + (t * 16 + 15) * 1024);
    }
    load_res();
    SB();
    publish(7);
    raw_barrier();
    STAMP(1, 9);
    reduce_ld(7);
    of0 = lds_frag(lds, AL_O + l16); of1 = lds_frag(lds, AL_O + 1024 + l16);
    SB();
    reduce_fin(7);
    SB();
#pragma unroll
    for (int t = 0; t < 4; ++t) { mfma16_ip(oacc[t], wof[2 * t], of0); }
#pragma unroll
    for (int t = 0; t < 4; ++t) { mfma16_ip(oacc[t], wof[2 * t + 1], of1); }
    raw_barrier();
    STAMP(1, 10);
    of0 = lds_frag(lds, AL_O + 2048 + l16); of1 = lds_frag(lds, AL_O + 3072 + l16);
#pragma unroll
    for (int t = 0; t < 4; ++t) { mfma16_ip(oacc[t], wl[2 * t], of0); }
#pragma unroll
    for (int t = 0; t < 4; ++t) { mfma16_ip(oacc[t], wl[2 * t + 1], of1); }
    asm volatile("s_nop 7" : "+v"(oacc[0]), "+v"(oacc[1]), "+v"(oacc[2]), "+v"(oacc[3]));
  } else {
    __builtin_amdgcn_s_setprio(0);
    float* mx = (float*)(lds + AL_MX);
    const f32x16 z = {};
#pragma unroll 1
    for (int g = 0; g < 8; ++g) {
      const bf16x8 q = lds_frag(lds, AL_Q + g * 1024 + l16);
      float m = -INFINITY;
#pragma unroll
      for (int t = 0; t < 8; ++t) {
        const f32x16 s = mfma32(kf[t], q, z);
#pragma unroll
        for (int i = 0; i < 16; ++i) m = fmaxf(m, s[i]);
        SB();
      }
      m = fmaxf(m, __shfl_xor(m, 32));
      if (h == 0) mx[w * 256 + g * 32 + r] = m;
    }
    __syncthreads();
#pragma unroll 1
    for (int g = 0; g < 8; ++g) {
      const bf16x8 q = lds_frag(lds, AL_Q + g * 1024 + l16);
      float m = mx[g * 32 + r];
#pragma unroll
      for (int i = 1; i < 8; ++i) m = fmaxf(m, mx[i * 256 + g * 32 + r]);
      o = z;
#pragma unroll
      for (int t = 0; t < 8; ++t) {
        const f32x16 s = mfma32(kf[t], q, z);
        u32x4 a0, a1;
#pragma unroll
        for (int i = 0; i < 4; ++i) {
          a0[i] = pk2(EX(s[2 * i] - m), EX(s[2 * i + 1] - m));
          a1[i] = pk2(EX(s[8 + 2 * i] - m), EX(s[9 + 2 * i] - m));
        }
        o = mfma32(vf[2 * t], __builtin_bit_cast(bf16x8, a0), o);
        o = mfma32(vf[2 * t + 1], __builtin_bit_cast(bf16x8, a1), o);
        SB();
      }
      publish(g);
      __syncthreads();
      reduce(g);
      __syncthreads();
      const bf16x8 b0 = lds_frag(lds, AL_O + (g & 1) * 2048 + l16), b1 = lds_frag(lds, AL_O + (g & 1) * 2048 + 1024 + l16);
#pragma unroll
      for (int t = 0; t < 4; ++t) {
        oacc[t] = mfma16(bld(rs, l16, wos + (t * 16 + 2 * g) * 1024), b0, oacc[t]);
        oacc[t] = mfma16(bld(rs, l16, wos + (t * 16 + 2 * g + 1) * 1024), b1, oacc[t]);
      }
    }
    load_res();
#pragma unroll 1
    for (int i = 0; i < 14; ++i) dma_w1(rs, lds0, l16, f1_nt0, w, i >> 1, i & 1);
    asm volatile("s_nop 7" : "+v"(oacc[0]), "+v"(oacc[1]), "+v"(oacc[2]), "+v"(oacc[3]));
  }
  float xv[8];
  asm volatile("s_nop 1\n\t"
               "v_add_f32_dpp %0, %8, %8 row_ror:8 row_mask:0xf bank_mask:0x3\n\t"
               "v_add_f32_dpp %1, %9, %9 row_ror:8 row_mask:0xf bank_mask:0x3\n\t"
               "v_add_f32_dpp %2, %10, %10 row_ror:8 row_mask:0xf bank_mask:0x3\n\t"
               "v_add_f32_dpp %3, %11, %11 row_ror:8 row_mask:0xf bank_mask:0x3\n\t"
               "v_add_f32_dpp %4, %12, %12 row_ror:8 row_mask:0xf bank_mask:0x3\n\t"
               "v_add_f32_dpp %5, %13, %13 row_ror:8 row_mask:0xf bank_mask:0x3\n\t"
               "v_add_f32_dpp %6, %14, %14 row_ror:8 row_mask:0xf bank_mask:0x3\n\t"
               "v_add_f32_dpp %7, %15, %15 row_ror:8 row_mask:0xf bank_mask:0x3\n\t"
               "v_add_f32_dpp %0, %16, %16 row_ror:8 row_mask:0xf bank_mask:0xc\n\t"
               "v_add_f32_dpp %1, %17, %17 row_ror:8 row_mask:0xf bank_mask:0xc\n\t"
               "v_add_f32_dpp %2, %18, %18 row_ror:8 row_mask:0xf bank_mask:0xc\n\t"
               "v_add_f32_dpp %3, %19, %19 row_ror:8 row_mask:0xf bank_mask:0xc\n\t"
               "v_add_f32_dpp %4, %20, %20 row_ror:8 row_mask:0xf bank_mask:0xc\n\t"
               "v_add_f32_dpp %5, %21, %21 row_ror:8 row_mask:0xf bank_mask:0xc\n\t"
               "v_add_f32_dpp %6, %22, %22 row_ror:8 row_mask:0xf bank_mask:0xc\n\t"
               "v_add_f32_dpp %7, %23, %23 row_ror:8 row_mask:0xf bank_mask:0xc\n\t"
               : "=&v"(xv[0]), "=&v"(xv[1]), "=&v"(xv[2]), "=&v"(xv[3]), "=&v"(xv[4]), "=&v"(xv[5]), "=&v"(xv[6]), "=&v"(xv[7])
               : "v"(oacc[0][0]), "v"(oacc[0][1]), "v"(oacc[0][2]), "v"(oacc[0][3]), "v"(oacc[1][0]), "v"(oacc[1][1]), "v"(oacc[1][2]), "v"(oacc[1][3]), "v"(oacc[2][0]), "v"(oacc[2][1]), "v"(oacc[2][2]), "v"(oacc[2][3]), "v"(oacc[3][0]), "v"(oacc[3][1]), "v"(oacc[3][2]), "v"(oacc[3][3]));
  u32x4 ov;
  float sm = 0.f, sq = 0.f;
#pragma unroll
  for (int q = 0; q < 4; ++q) {
    const float a = xv[2 * q] + bflo(xr[q]), b = xv[2 * q + 1] + bfhi(xr[q]);
    ov[q] = pk2(a, b);
    sm += a + b; sq = fmaf(a, a, fmaf(b, b, sq));
  }
  bst4<SC1>(ov, rs, fv, OFF_X1 + fs);
  sm += dpp<0x128>(sm); sq += dpp<0x128>(sq);
  xrow_sum2(sm, sq);
  bst2<SC1>((u32x2){__float_as_uint(sm), __float_as_uint(sq)}, rs, lane < 8 ? lane * 64 : OOB, OFF_ST + t0 * 64 + w * 8);
  STAMP(1, 11);
  wg_signal(ws, flag_off(layer, 0, t0 >> 4) + (t0 & 8) / 2, tid);
}

DI void ffn2_prefetch(rsrc_t rs, unsigned lds0, unsigned l16, int nt0, int w, bf16x8 (&stg)[20], int i) {
  const int mp = w & 1, kq = w >> 1;
  if (i < 4) dma(rs, l16, OFF_W2 + ((nt0 + 2 * mp + (i & 1)) * 56 + 14 * kq + (i >> 1)) * KB, lds0 + (2 * kq + (i >> 1)) * 4096 + (2 * mp + (i & 1)) * 1024);
  else { const int q = i - 4, ti = q & 3;
    stg[q] = bld(rs, l16, OFF_W2 + ((nt0 + ((ti & 2) ^ (2 * (kq & 1))) + (ti & 1)) * 56 + 14 * kq + 9 + (q >> 2)) * KB); }
}

DI void ffn1_phase(char* lds, char* ws, rsrc_t rs, int layer, int tid, bool standalone, bf16x8 (&stg)[20]) {
  const int lane = tid & 63, g = lane >> 4, mm = lane & 15;
  const int w = __builtin_amdgcn_readfirstlane(tid >> 6);
  const int xcd = blockIdx.x & 7, idx = blockIdx.x >> 3, rb = idx >> 4, cb = idx & 15;
  const int mt = xcd * 16 + rb * 8 + w, nt0 = cb * 7;
  const unsigned lds0 = (unsigned)(uintptr_t)lds, l16 = lane * 16;
  STAMP(2, 0);
  if (standalone) {
#pragma unroll 1
    for (int i = 0; i < 14; ++i) dma_w1(rs, lds0, l16, nt0, w, i >> 1, i & 1);
    wait_vm<0>();
    raw_barrier();
  }
#define PRE_EARLY 12
#pragma unroll
  for (int i = 0; i < PRE_EARLY; ++i) ffn2_prefetch(rs, lds0, l16, (idx & 7) * 4, w, stg, i);
  {
    bf16x8 ones; for (int i = 0; i < 8; ++i) ones[i] = 0x3F80;
    const int cbase = AL_W1 + (w < 7 ? w : 6) * 1024 + l16;
    f32x4 c0 = {}, c1 = {};
#pragma unroll
    for (int ks = 0; ks < 16; ks += 2) { c0 = mfma16(lds_frag(lds, cbase + ks * 7168), ones, c0); c1 = mfma16(lds_frag(lds, cbase + (ks + 1) * 7168), ones, c1); }
    if (mm == 0) *(f32x4*)(lds + AL_RL + w * 64 + g * 16) = c0 + c1;
    raw_barrier();
  }
  bf16x8 a[2][7];
#pragma unroll
  for (int t = 0; t < 7; ++t) a[0][t] = lds_frag(lds, AL_W1 + t * 1024 + l16);
  wave_wait(ws, flag_off(layer, 0, mt), (lane & 1) * 4);
  STAMP(2, 1);
  constexpr int PD = 6;
  bf16x8 b[16];
  const unsigned bs = OFF_X1 + mt * 16 * KB;
#pragma unroll
  for (int ks = 0; ks < PD; ++ks) b[ks] = bld<SC1>(rs, l16, bs + ks * KB);
  const f32x4 stp = bld4f<SC1>(rs, mm * 64 + g * 16, OFF_ST + mt * 1024);
  SB();
  STAMP(2, 2);
  f32x4 acc[7] = {}, cs[7];
  float rstd, nmr;
#pragma unroll
  for (int ks = 0; ks < 16; ++ks) {
    if (ks < 15) {
#pragma unroll
      for (int t = 0; t < 7; ++t) a[(ks + 1) & 1][t] = lds_frag(lds, AL_W1 + ((ks + 1) * 7 + t) * 1024 + l16);
    }
    if (ks + PD < 16) b[ks + PD] = bld<SC1>(rs, l16, bs + (ks + PD) * KB);
    if (ks >= 6) {
#pragma unroll
      for (int i = PRE_EARLY + 2 * (ks - 6); i < PRE_EARLY + 2 * (ks - 5); ++i) if (i < 24) ffn2_prefetch(rs, lds0, l16, (idx & 7) * 4, w, stg, i);
    }
    if (ks == 14) {
#pragma unroll
      for (int t = 0; t < 7; ++t) cs[t] = *(const f32x4*)(lds + AL_RL + t * 64 + g * 16);
    }
    if (ks == 8) {
      float sm = stp[0] + stp[2], sq = stp[1] + stp[3];
      xrow_sum2(sm, sq);
      const float mean = sm * (1.f / D_);
      rstd = __builtin_amdgcn_rsqf(fmaxf(sq * (1.f / D_) - mean * mean, 0.f) + LN_EPS); nmr = -mean * rstd;
      PIN2(rstd, nmr);
    }
    if (ks == 15) {
#pragma unroll
      for (int t = 0; t < 7; ++t) { cs[t] *= nmr; PIN1(cs[t]); }
    }
    SB();
#pragma unroll
    for (int t = 0; t < 7; ++t) acc[t] = mfma16(a[ks & 1][t], b[ks], acc[t]);
    SB();
    if (ks == 0) STAMP(2, 3);
    if (ks == 7) STAMP(2, 4);
  }
  STAMP(2, 10);
  u32x2 hv[7];
#pragma unroll
  for (int t = 0; t < 7; ++t) {
    const f32x4 v = acc[t] * rstd + cs[t];
    hv[t] = (u32x2){relu_pk(pk2(v[0], v[1])), relu_pk(pk2(v[2], v[3]))};
  }
  const unsigned hs = OFF_H + (mt * 56 + (nt0 >> 1)) * KB;
  if ((cb & 1) == 0) {
#pragma unroll
    for (int p = 0; p < 3; ++p) bst4<SC1>((u32x4){hv[2 * p][0], hv[2 * p][1], hv[2 * p + 1][0], hv[2 * p + 1][1]}, rs, l16, hs + p * KB);
    bst2<SC1>(hv[6], rs, l16, hs + 3 * KB);
  } else {
    bst2<SC1>(hv[0], rs, l16 + 8, hs);
#pragma unroll
    for (int p = 0; p < 3; ++p) bst4<SC1>((u32x4){hv[2 * p + 1][0], hv[2 * p + 1][1], hv[2 * p + 2][0], hv[2 * p + 2][1]}, rs, l16, hs + (p + 1) * KB);
  }
  STAMP(2, 11);
  wg_signal(ws, flag_off(layer, 1, (xcd * 2 + rb) * 4 + (cb >> 2)) + (cb & 3) * 4, tid);
}

DI void ffn2_phase(char* lds, char* ws, rsrc_t rs, u16* out, int layer, int tid, bool standalone, bf16x8 (&stg)[20]) {
  const int lane = tid & 63, g = lane >> 4, mm = lane & 15;
  const int w = __builtin_amdgcn_readfirstlane(tid >> 6);
  const int xcd = blockIdx.x & 7, idx = blockIdx.x >> 3, rb = idx >> 3, cb = idx & 7;
  const int mp = w & 1, kq = w >> 1, mt0 = xcd * 16 + rb * 4, nt0 = cb * 4;
  const unsigned lds0 = (unsigned)(uintptr_t)lds, l16 = lane * 16;
  STAMP(3, 0);
  if (standalone) {
#pragma unroll
    for (int i = 0; i < 24; ++i) ffn2_prefetch(rs, lds0, l16, nt0, w, stg, i);
  }
  const unsigned w2s = OFF_W2 + ((nt0 + 2 * mp) * 56 + 14 * kq) * KB;
#pragma unroll
  for (int i = 0; i < 14; ++i) dma(rs, l16, w2s + ((i & 1) * 56 + 2 + (i >> 1)) * KB, lds0 + AL_W1 + (7 * kq + (i >> 1)) * 4096 + (2 * mp + (i & 1)) * 1024);
  const int fmt = mt0 + 2 * mp + (kq >> 1);
  const unsigned fs = (fmt * 16 + (nt0 >> 1) + (kq & 1)) * KB;
  const u32x4 xr = bld4<SC1>(rs, l16, OFF_X1 + fs);
  bf16x8 a[2][4];
  const int nlo = (kq & 1) * 2048 + l16, nhi = (1 - (kq & 1)) * 2048 + l16;
  const int e0 = 2 * kq * 4096, m0 = AL_W1 + 7 * kq * 4096;
  auto kstep = [&](int s) { return s < 5 ? 9 + s : s - 5; };
  auto lda = [&](int s) {
    const int j = s - 5, off = j < 2 ? e0 + j * 4096 : m0 + (j - 2) * 4096;
    a[s & 1][0] = lds_frag(lds, off + nlo); a[s & 1][1] = lds_frag(lds, off + nlo + 1024);
    a[s & 1][2] = lds_frag(lds, off + nhi); a[s & 1][3] = lds_frag(lds, off + nhi + 1024);
  };
  wave_wait(ws, flag_off(layer, 1, (xcd * 2 + (rb >> 1)) * 4 + kq), (lane & 3) * 4);
  STAMP(3, 1);
  constexpr int PD = 6;
  bf16x8 b[14][2];
  const unsigned bs0 = OFF_H + (fmt * 56 + 14 * kq) * KB, bs1 = OFF_H + ((fmt ^ 1) * 56 + 14 * kq) * KB;
  auto ldb = [&](int s) { b[s][0] = bld<SC1>(rs, l16, bs0 + kstep(s) * KB); b[s][1] = bld<SC1>(rs, l16, bs1 + kstep(s) * KB); };
#pragma unroll
  for (int s = 0; s < PD; ++s) ldb(s);
  SB();
  f32x4 acc[2][4] = {};
  STAMP(3, 2);
#pragma unroll
  for (int s = 0; s < 14; ++s) {
    if (s == 4) { STAMP(3, 4); raw_barrier(); STAMP(3, 5); }
    if (s == 8) raw_barrier();
    if (s >= 4 && s < 13) lda(s + 1);
    if (s + PD < 14) ldb(s + PD);
    SB();
#pragma unroll
    for (int t = 0; t < 4; ++t) {
      const bf16x8 af = s < 5 ? stg[4 * s + t] : a[s & 1][t];
      acc[0][t] = mfma16(af, b[s][0], acc[0][t]); acc[1][t] = mfma16(af, b[s][1], acc[1][t]);
    }
    SB();
  }
  STAMP(3, 9);
  f32x4 s0 = acc[0][0] + (f32x4){bflo(xr[0]), bfhi(xr[0]), bflo(xr[1]), bfhi(xr[1])};
  f32x4 s1 = acc[0][1] + (f32x4){bflo(xr[2]), bfhi(xr[2]), bflo(xr[3]), bfhi(xr[3])};
#pragma unroll
  for (int d = 1; d < 4; ++d) {
    char* xb = lds + (3 * (kq ^ d) + (d - 1)) * 4096 + mp * 2048 + l16;
    *(f32x4*)(xb) = acc[d >> 1][2 * (d & 1)]; *(f32x4*)(xb + 1024) = acc[d >> 1][2 * (d & 1) + 1];
  }
  raw_barrier();
#pragma unroll
  for (int d = 0; d < 3; ++d) {
    const char* xb = lds + (3 * kq + d) * 4096 + mp * 2048 + l16;
    s0 += *(const f32x4*)(xb); s1 += *(const f32x4*)(xb + 1024);
  }
  STAMP(3, 10);
  const u32x4 o = {pk2(s0[0], s0[1]), pk2(s0[2], s0[3]), pk2(s1[0], s1[1]), pk2(s1[2], s1[3])};
  if (layer == 1) {
    u16* op = out + (size_t)(fmt * 16 + mm) * D_ + (nt0 + 2 * (kq & 1)) * 16 + 4 * g;
    *(u32x2*)op = (u32x2){o[0], o[1]};
    *(u32x2*)(op + 16) = (u32x2){o[2], o[3]};
    STAMP(3, 11);
  } else {
    bst4<SC1>(o, rs, l16, OFF_X2 + fs);
    STAMP(3, 11);
    wg_signal(ws, flag_off(0, 2, (xcd * 4 + rb) * 2 + (cb >> 2)) + (cb & 3) * 4, tid);
  }
}

typedef short v4i16_t __attribute__((ext_vector_type(4)));
DI bf16x8 tr_frag(const char* lds, int off0, int off1) {
  const v4i16_t lo = __builtin_amdgcn_ds_read_tr16_b64_v4i16((LAS v4i16_t*)((LAS char*)lds + off0));
  const v4i16_t hi = __builtin_amdgcn_ds_read_tr16_b64_v4i16((LAS v4i16_t*)((LAS char*)lds + off1));
  return __builtin_shufflevector(lo, hi, 0, 1, 2, 3, 4, 5, 6, 7);
}
constexpr int QL_KV = 65536, QL_X = 81920, QL_ST = QL_X + 32768, QL_CS = QL_ST + 1024, QL_TR = QL_CS + 1024;
template <bool FIRST, class H1, class H2>
DI void qkv_phase(char* lds, char* ws, rsrc_t rs, int tid, const u16* x, const u16* wq, const u16* wkv, H1 hook1, H2 hook2) {
  const int lane = tid & 63, g = lane >> 4, mm = lane & 15;
  const int w = __builtin_amdgcn_readfirstlane(tid >> 6);
  const int xcd = blockIdx.x & 7, idx = blockIdx.x >> 3, rb = idx >> 3, cb = idx & 7;
  const int ml = w & 3, hf = w >> 2, mt = xcd * 16 + rb * 4 + ml;
  const int kvml = cb >> 1;
  const unsigned lds0 = (unsigned)(uintptr_t)lds, l16 = lane * 16;
  const unsigned qkv = OFF_QKV + (FIRST ? 0 : QKV_BYTES);
  STAMP(FIRST ? 5 : 0, 0);
  bf16x8 b[8];
  if (FIRST) {
    const rsrc_t rw = __builtin_amdgcn_make_buffer_rsrc((void*)wq, 0, D_ * D_ * 2, 0x00020000);
    const rsrc_t rk = __builtin_amdgcn_make_buffer_rsrc((void*)wkv, 0, D_ * 16 * 2, 0x00020000);
    const rsrc_t rx = __builtin_amdgcn_make_buffer_rsrc((void*)x, 0, S_ * D_ * 2, 0x00020000);
    const unsigned v0 = (lane >> 3) * 1024 + (((lane & 7) ^ ((lane >> 3) & 2)) << 4), v1 = v0 ^ 64;
#pragma unroll
    for (int i = 0; i < 8; ++i) dma(rw, (i & 1) ? v1 : v0, (8 * w + i) * 8192 + cb * 128, lds0 + (8 * w + i) * 1024);
#pragma unroll
    for (int i = 0; i < 2; ++i) dma(rk, l16, (2 * w + i) * 1024, lds0 + QL_KV + (2 * w + i) * 1024);
#pragma unroll
    for (int j = 0; j < 8; ++j) b[j] = bld(rx, mm * 1024 + g * 16 + j * 64, mt * 16384 + hf * 512);
    wait_vm<8>();
  } else {
#pragma unroll
    for (int kk = 0; kk < 2; ++kk) {
      const int ks = 2 * w + kk;
#pragma unroll
      for (int t = 0; t < 4; ++t) dma(rs, l16, OFF_WQKV + ((4 * cb + t) * 16 + ks) * KB, lds0 + (ks * 4 + t) * 1024);
      dma(rs, l16, OFF_WQKV + ((32 + (cb & 1)) * 16 + ks) * KB, lds0 + QL_KV + ks * 1024);
    }
    wait_vm<0>();
  }
  raw_barrier();
  STAMP(FIRST ? 5 : 0, 1);
  hook1();
  const int q4 = (lane >> 2) & 3, p4 = lane & 3;
  const int fsw = ((q4 >> 1) & 1) | ((g & 1) << 1);
  const int abase = FIRST ? hf * 32768 + (8 * g + q4) * 128 + p4 * 8 : hf * 32768 + l16;
  auto aq = [&](int t  ) { return FIRST ? abase + ((t ^ fsw) << 5) : abase + t * 1024; };
  auto ldq = [&](int base, int j) { return FIRST ? tr_frag(lds, base + j * 4096, base + j * 4096 + 512) : lds_frag(lds, base + j * 4096); };
  const int kvb = QL_KV + hf * 8192 + (FIRST ? (8 * g + q4) * 32 + p4 * 8 : l16);
  auto ldkv = [&](int j) { return FIRST ? tr_frag(lds, kvb + j * 1024, kvb + j * 1024 + 128) : lds_frag(lds, kvb + j * 1024); };
  bf16x8 ones; for (int i = 0; i < 8; ++i) ones[i] = 0x3F80;
  {
    const int cbs = aq(ml);
    f32x4 c0 = {}, c1 = {}, ck = {};
#pragma unroll
    for (int j = 0; j < 8; j += 2) { c0 = mfma16(ldq(cbs, j), ones, c0); c1 = mfma16(ldq(cbs, j + 1), ones, c1); }
    const int kv2 = kvb + ml * 2048;
#pragma unroll
    for (int j = 0; j < 2; ++j) ck = mfma16(FIRST ? tr_frag(lds, kv2 + j * 1024, kv2 + j * 1024 + 128) : lds_frag(lds, kv2 + j * 1024), ones, ck);
    if (mm == 0) { *(f32x4*)(lds + QL_CS + (ml * 2 + hf) * 64 + g * 16) = c0 + c1; *(f32x4*)(lds + QL_CS + 512 + w * 64 + g * 16) = ck; }
  }
  if (!FIRST) {
    wave_wait(ws, flag_off(0, 2, (xcd * 4 + rb) * 2 + hf), (lane & 3) * 4);
#pragma unroll
    for (int j = 0; j < 8; ++j) b[j] = bld<SC1>(rs, l16, OFF_X2 + (mt * 16 + 8 * hf + j) * KB);
  }
  STAMP(FIRST ? 5 : 0, 2);
  f32x4 acc[4] = {}, accs = {}, accg = {};
  bf16x8 a[2][4];
  const int ab0 = aq(2 * hf), ab1 = aq(2 * hf + 1), ab2 = aq(2 - 2 * hf), ab3 = aq(3 - 2 * hf);
  auto lda = [&](int j) { a[j & 1][0] = ldq(ab0, j); a[j & 1][1] = ldq(ab1, j); a[j & 1][2] = ldq(ab2, j); a[j & 1][3] = ldq(ab3, j); };
  lda(0);
#pragma unroll
  for (int j = 0; j < 8; ++j) {
    if (j < 7) lda(j + 1);
    SB();
    accs = mfma16(ones, b[j], accs); accg = mfma16(b[j], b[j], accg);
#pragma unroll
    for (int t = 0; t < 4; ++t) acc[t] = mfma16(a[j & 1][t], b[j], acc[t]);
    SB();
  }
  char* xb = lds + QL_X + ml * 8192 + l16;
  if (ml == kvml) {
    f32x4 akv = {};
    if (cb & 1) {
#pragma unroll
      for (int j = 0; j < 8; ++j) akv = mfma16(b[j], ldkv(j), akv);
    } else {
#pragma unroll
      for (int j = 0; j < 8; ++j) akv = mfma16(ldkv(j), b[j], akv);
    }
    *(f32x4*)(xb + 4096 + hf * 1024) = akv;
  }
  STAMP(FIRST ? 5 : 0, 9);
  *(f32x4*)(xb + (1 - hf) * 2048) = acc[2];
  *(f32x4*)(xb + (1 - hf) * 2048 + 1024) = acc[3];
  f32x2* st = (f32x2*)(lds + QL_ST);
  {
    const float s01 = (mm & 1) ? accg[1] : accg[0], s23 = (mm & 1) ? accg[3] : accg[2];
    if (g == (mm >> 2)) st[(ml * 16 + mm) * 2 + hf] = (f32x2){accs[0], (mm & 2) ? s23 : s01};
  }
  hook2();
  raw_barrier();
  STAMP(FIRST ? 5 : 0, 10);
  auto stats = [&](int tokl, float& mean, float& rstd) {
    const f32x4 s = *(const f32x4*)(st + (ml * 16 + tokl) * 2);
    mean = (s[0] + s[2]) * (1.f / D_);
    rstd = __builtin_amdgcn_rsqf(fmaxf((s[1] + s[3]) * (1.f / D_) - mean * mean, 0.f) + LN_EPS);
  };
  float mean, rstd; stats(mm, mean, rstd);
  const int nt_ = 4 * cb + 2 * hf;
  const char* csp = lds + QL_CS + hf * 256 + g * 16;
  const f32x4 cs0 = *(const f32x4*)(csp) + *(const f32x4*)(csp + 64), cs1 = *(const f32x4*)(csp + 128) + *(const f32x4*)(csp + 192);
  const f32x4 s0 = acc[0] + *(const f32x4*)(xb + hf * 2048), s1 = acc[1] + *(const f32x4*)(xb + hf * 2048 + 1024);
  const float rq = rstd * QSCALE, nmq = -mean * rq;
  const f32x4 q0 = s0 * rq + cs0 * nmq, q1 = s1 * rq + cs1 * nmq;
  const u32x2 o0 = {pk2(q0[0], q0[1]), pk2(q0[2], q0[3])}, o1 = {pk2(q1[0], q1[1]), pk2(q1[2], q1[3])};
  bst2(o0, rs, mm * 1024 + g * 8, qkv + QO_Q + mt * 16384 + nt_ * 32);
  bst2(o1, rs, mm * 1024 + g * 8 + 32, qkv + QO_Q + mt * 16384 + nt_ * 32);
  float n0 = sq4(o0), n1 = sq4(o1);
  xrow_sum2(n0, n1);
  const unsigned qn = row16_umax(umax(__float_as_uint(n0), __float_as_uint(n1)));
  bst1(qn, rs, lane == 0 ? 0u : OOB, qkv + QO_QN2 + mt * 64 + (cb * 2 + hf) * 4);
  if (hf == 0 && ml == kvml) {
    const f32x4 skv = *(const f32x4*)(xb + 4096) + *(const f32x4*)(xb + 5120);
    if (cb & 1) {
      float csv = 0.f;
#pragma unroll
      for (int i = 0; i < 8; ++i) csv += ((const float*)(lds + QL_CS + 512))[i * 16 + mm];
      float v[4];
#pragma unroll
      for (int i = 0; i < 4; ++i) { float mn, rsd; stats(4 * g + i, mn, rsd); v[i] = (skv[i] - mn * csv) * rsd; }
      const u32x2 o = {pk2(v[0], v[1]), pk2(v[2], v[3])};
      bst2(o, rs, (g & 1) * 256 + mm * 16 + (g >> 1) * 8, qkv + QO_VT + mt * 512);
    } else {
      f32x4 csk = {};
#pragma unroll
      for (int i = 0; i < 8; ++i) csk += *(const f32x4*)(lds + QL_CS + 512 + i * 64 + g * 16);
      const f32x4 k = (skv - csk * mean) * rstd;
      const u32x2 o = {pk2(k[0], k[1]), pk2(k[2], k[3])};
      bst2(o, rs, mm * 32 + g * 8, qkv + QO_K + mt * 512);
      const unsigned n2 = row16_umax(__float_as_uint(xrow_sum(sq4(o))));
      bst1(n2, rs, lane == 0 ? 0u : OOB, qkv + QO_KN2 + mt * 4);
    }
  }
  STAMP(FIRST ? 5 : 0, 11);
}

__global__ __launch_bounds__(512, 2) void k_first(const u16* __restrict__ x, const u16* __restrict__ wq, const u16* __restrict__ wk, const u16* __restrict__ wv,
                                                  const u16* __restrict__ wo, const u16* __restrict__ w1, const u16* __restrict__ w2, char* __restrict__ ws) {
  extern __shared__ __attribute__((aligned(16))) char lds[];
  const int tid = threadIdx.x, lane = tid & 63, g = lane >> 4;
  const int w = __builtin_amdgcn_readfirstlane(tid >> 6);
  const rsrc_t rs = __builtin_amdgcn_make_buffer_rsrc((void*)ws, 0, WS_NEED, 0x00020000);
  const unsigned lds0 = (unsigned)(uintptr_t)lds, l16 = lane * 16;
  const int xcd = blockIdx.x & 7, idx = blockIdx.x >> 3, cb = idx & 7;
  const u16* tw; int tpitch, tks, tnb; unsigned tdst; int tkst;
  {
    const int bi = blockIdx.x * 4 + (w & 3);
    if (bi < 128) { tw = wo; tpitch = 1024; tks = bi >> 3; tnb = bi & 7; tdst = OFF_WO; tkst = 16; }
    else if (bi < 576) { const int c = bi - 128; tw = w1; tpitch = 3584; tks = c / 28; tnb = c - 28 * tks; tdst = OFF_W1; tkst = 16; }
    else { const int c = bi - 576; tw = w2; tpitch = 1024; tks = c >> 3; tnb = c & 7; tdst = OFF_W2; tkst = 56; }
  }
  auto fetch = [&]() {
    if (w < 4) {
      const rsrc_t rt = __builtin_amdgcn_make_buffer_rsrc((void*)tw, 0, D_ * DFF * 2, 0x00020000);
      const unsigned v = (lane >> 3) * tpitch + (lane & 7) * 16;
#pragma unroll
      for (int i = 0; i < 4; ++i) dma(rt, v, (32 * tks + 8 * i) * tpitch + tnb * 128, lds0 + QL_TR + w * 4096 + i * 1024);
    }
  };
  auto convert = [&]() {
    const int q4 = (lane >> 2) & 3, p4 = lane & 3;
    if (w < 4) {
      wait_vm<0>();
      const int base = QL_TR + w * 4096 + (4 * g + q4) * 128 + p4 * 8;
#pragma unroll
      for (int t = 0; t < 4; ++t)
        bst4(__builtin_bit_cast(u32x4, tr_frag(lds, base + t * 32, base + t * 32 + 2048)), rs, l16, tdst + ((4 * tnb + t) * tkst + tks) * KB);
    } else if (w < 6) {
      const int c = (xcd * 4 + (idx >> 3)) * 2 + (w & 1), ks = c >> 2, t = c & 3;
      const int r0 = 4 * g + q4, f0 = ((r0 >> 1) & 1) | (((r0 >> 3) & 1) << 1);
      const int base = (32 * ks + r0) * 128 + ((t ^ f0) << 5) + p4 * 8;
      bst4(__builtin_bit_cast(u32x4, tr_frag(lds, base, base + 2048)), rs, l16, OFF_WQKV + ((4 * cb + t) * 16 + ks) * KB);
    } else if (w == 6) {
      const int ks = xcd * 4 + (idx >> 3);
      if (cb < 2 && ks < 16) {
        const int base = QL_KV + (32 * ks + 4 * g + q4) * 32 + p4 * 8;
        bst4(__builtin_bit_cast(u32x4, tr_frag(lds, base, base + 512)), rs, l16, OFF_WQKV + ((32 + cb) * 16 + ks) * KB);
      }
    } else {
      bst4((u32x4){0u, 0u, 0u, 0u}, rs, lane < 24 ? l16 : OOB, OFF_FLAG + blockIdx.x * 384);
    }
  };
  qkv_phase<true>(lds, ws, rs, tid, x, wq, (cb & 1) ? wv : wk, fetch, convert);
}

__global__ __launch_bounds__(512, 2) void k_main(char* __restrict__ ws, u16* __restrict__ out, const u16* __restrict__ xin, int layer, int pbeg, int pend) {
  extern __shared__ __attribute__((aligned(16))) char lds[];
  const int tid = threadIdx.x;
  const rsrc_t rs = __builtin_amdgcn_make_buffer_rsrc((void*)ws, 0, WS_NEED, 0x00020000);
  bf16x8 stg[20];
  if (pbeg <= 0 && pend > 0) attn_phase(lds, ws, rs, xin, layer, tid);
  if (pbeg <= 1 && pend > 1) ffn1_phase(lds, ws, rs, layer, tid, pbeg == 1, stg);
  if (pbeg <= 2 && pend > 2) ffn2_phase(lds, ws, rs, out, layer, tid, pbeg == 2, stg);
  if (pbeg <= 3 && pend > 3) qkv_phase<false>(lds, ws, rs, tid, nullptr, nullptr, nullptr, []() {}, []() {});
}

extern "C" void kernel_launch(void* const* d_in, const int* in_sizes, int n_in, void* d_out, int out_size, void* d_ws, size_t ws_size,
                              hipStream_t stream) {
  const u16* x = (const u16*)d_in[0];
  const u16* wq = (const u16*)d_in[1];
  const u16* wk = (const u16*)d_in[2];
  const u16* wv = (const u16*)d_in[3];
  const u16* wo = (const u16*)d_in[4];
  const u16* w1 = (const u16*)d_in[6];
  const u16* w2 = (const u16*)d_in[7];
  char* ws = (char*)d_ws;
  u16* out = (u16*)d_out;
  (void)hipFuncSetAttribute((const void*)k_main, hipFuncAttributeMaxDynamicSharedMemorySize, LDS_BYTES);
  (void)hipFuncSetAttribute((const void*)k_first, hipFuncAttributeMaxDynamicSharedMemorySize, LDS_BYTES);
  k_first<<<256, 512, LDS_BYTES, stream>>>(x, wq, wk, wv, wo, w1, w2, ws);
  k_main<<<256, 512, LDS_BYTES, stream>>>(ws, out, x, 0, 0, 4);
  k_main<<<256, 512, LDS_BYTES, stream>>>(ws, out, x, 1, 0, 3);
}
```

```cpp
#include <hip/hip_runtime.h>
#include <hip/hip_bf16.h>
#include <stdint.h>
#include <type_traits>

typedef __attribute__((ext_vector_type(8))) short bf16x8;
typedef __attribute__((ext_vector_type(2))) float f32x2;
typedef __attribute__((ext_vector_type(4))) float f32x4;
typedef __attribute__((ext_vector_type(16))) float f32x16;
typedef __attribute__((ext_vector_type(4))) unsigned u32x4;
typedef __attribute__((ext_vector_type(2))) unsigned u32x2;
typedef unsigned short u16;
typedef __amdgpu_buffer_rsrc_t rsrc_t;
typedef __attribute__((address_space(1))) unsigned gu32;
#define DI __device__ __forceinline__
#define LAS __attribute__((address_space(3)))

constexpr int S_ = 2048, D_ = 512, DFF = 1792;
constexpr float QSCALE = 0.25f * 1.4426950408889634f;
constexpr float LN_EPS = 1e-5f;

constexpr unsigned KB = 1024;
constexpr unsigned OFF_FLAG = 0;
constexpr unsigned FLAG_BYTES = 128 * KB;
constexpr unsigned OFF_WQKV = OFF_FLAG + FLAG_BYTES;
constexpr unsigned OFF_WO   = OFF_WQKV + 544 * KB;
constexpr unsigned OFF_W1   = OFF_WO + 512 * KB;
constexpr unsigned OFF_W2   = OFF_W1 + 1792 * KB;
constexpr unsigned OFF_CSUM = OFF_W2 + 1792 * KB;
constexpr unsigned OFF_CS1  = OFF_CSUM + 4 * KB;
constexpr unsigned OFF_X2   = OFF_CS1 + 28 * KB;
constexpr unsigned OFF_QKV  = OFF_X2 + 2048 * KB;
constexpr unsigned QO_X1 = 0;
constexpr unsigned QO_ST = QO_X1 + 2048 * KB;
constexpr unsigned QO_H  = QO_ST + 128 * KB;
constexpr unsigned QO_Q  = QO_H + 7168 * KB;
constexpr unsigned QO_K = QO_Q + 2048 * KB;
constexpr unsigned QO_VT = QO_K + 64 * KB;
constexpr unsigned QO_KN2 = QO_VT + 64 * KB;
constexpr unsigned QO_QN2 = QO_KN2 + 1 * KB;
constexpr unsigned QKV_BYTES = QO_QN2 + 8 * KB;
constexpr unsigned WS_NEED  = OFF_QKV + 2 * QKV_BYTES;
DI unsigned flag_off(int layer, int edge, int line) { return OFF_FLAG + ((layer * 4 + edge) * 128 + line) * 128; }

DI float bf2f(u16 v) { return __uint_as_float(((unsigned)v) << 16); }
DI float bflo(unsigned v) { return __uint_as_float(v << 16); }
DI float bfhi(unsigned v) { return __uint_as_float(v & 0xffff0000u); }
DI unsigned pk2(float lo, float hi) {
  typedef __attribute__((ext_vector_type(2))) __bf16 bf16x2_;
  f32x2 v = {lo, hi};
  bf16x2_ r = __builtin_convertvector(v, bf16x2_);
  return __builtin_bit_cast(unsigned, r);
}
DI unsigned relu_pk(unsigned v) {
  typedef __attribute__((ext_vector_type(2))) short s16x2;
  const s16x2 z = {0, 0};
  return __builtin_bit_cast(unsigned, __builtin_elementwise_max(__builtin_bit_cast(s16x2, v), z));
}
DI f32x4 mfma16(bf16x8 a, bf16x8 b, f32x4 c) {
  typedef __attribute__((ext_vector_type(8))) __bf16 b8;
  return __builtin_amdgcn_mfma_f32_16x16x32_bf16(__builtin_bit_cast(b8, a), __builtin_bit_cast(b8, b), c, 0, 0, 0);
}
DI f32x16 mfma32(bf16x8 a, bf16x8 b, f32x16 c) {
  typedef __attribute__((ext_vector_type(8))) __bf16 b8;
  return __builtin_amdgcn_mfma_f32_32x32x16_bf16(__builtin_bit_cast(b8, a), __builtin_bit_cast(b8, b), c, 0, 0, 0);
}
template <int N> DI void wait_vm() { asm volatile("s_waitcnt vmcnt(%0)" :: "i"(N) : "memory"); }
DI void raw_barrier() { asm volatile("s_waitcnt lgkmcnt(0)\n\ts_barrier" ::: "memory"); }
DI bf16x8 lds_frag(const char* lds, int off) { return *(const LAS bf16x8*)((const LAS char*)lds + off); }
#define STAMP(kid, i)
DI int kperm(int g, int j) { return (j < 4) ? 4 * g + j : 16 + 4 * g + (j - 4); }

template <int CTRL> DI float dpp(float x) { return __builtin_bit_cast(float, __builtin_amdgcn_mov_dpp(__builtin_bit_cast(int, x), CTRL, 0xf, 0xf, true)); }
template <int CTRL> DI unsigned dppu(unsigned x) { return (unsigned)__builtin_amdgcn_mov_dpp((int)x, CTRL, 0xf, 0xf, true); }
DI unsigned umax(unsigned a, unsigned b) { return a > b ? a : b; }
DI unsigned row16_umax(unsigned x) { x = umax(x, dppu<0xB1>(x)); x = umax(x, dppu<0x4E>(x)); x = umax(x, dppu<0x141>(x)); x = umax(x, dppu<0x140>(x)); return x; }
DI unsigned xrow_umax(unsigned x) {
  auto s = __builtin_amdgcn_permlane16_swap(x, x, false, false); x = umax(s[0], s[1]);
  auto t = __builtin_amdgcn_permlane32_swap(x, x, false, false); return umax(t[0], t[1]); }
DI float xrow_sum(float x) {
  auto s = __builtin_amdgcn_permlane16_swap(__float_as_uint(x), __float_as_uint(x), false, false);
  x = __uint_as_float(s[0]) + __uint_as_float(s[1]);
  auto t = __builtin_amdgcn_permlane32_swap(__float_as_uint(x), __float_as_uint(x), false, false);
  return __uint_as_float(t[0]) + __uint_as_float(t[1]); }
DI void xrow_sum2(float& a, float& b) {
  auto s = __builtin_amdgcn_permlane16_swap(__float_as_uint(a), __float_as_uint(b), false, false);
  const float x = __uint_as_float(s[0]) + __uint_as_float(s[1]);
  auto t = __builtin_amdgcn_permlane32_swap(__float_as_uint(x), __float_as_uint(x), false, false);
  const float y = __uint_as_float(t[0]) + __uint_as_float(t[1]);
  auto u = __builtin_amdgcn_permlane16_swap(__float_as_uint(y), __float_as_uint(y), false, false);
  a = __uint_as_float(u[0]); b = __uint_as_float(u[1]); }
DI float sq4(u32x2 o) { return bflo(o[0]) * bflo(o[0]) + bfhi(o[0]) * bfhi(o[0]) + bflo(o[1]) * bflo(o[1]) + bfhi(o[1]) * bfhi(o[1]); }
#define SB() __builtin_amdgcn_sched_barrier(0)
#define EX(x) __builtin_amdgcn_exp2f(x)
#define PIN1(a) asm volatile("" : "+v"(a))
#define PIN2(a, b) asm volatile("" : "+v"(a), "+v"(b))
#define PIN4(a, b, c, d) asm volatile("" : "+v"(a), "+v"(b), "+v"(c), "+v"(d))
constexpr int SC1 = 16;
DI void mfma16_ip(f32x4& acc, bf16x8 a, bf16x8 b) { asm volatile("v_mfma_f32_16x16x32_bf16 %0, %1, %2, %0" : "+v"(acc) : "v"(a), "v"(b)); }
DI void bld_asm(bf16x8& d, rsrc_t rs, unsigned voff, unsigned soff) { asm volatile("buffer_load_dwordx4 %0, %1, %2, %3 offen" : "+v"(d) : "v"(voff), "s"(rs), "s"(soff)); }
template <int N> DI void mfma16_w(f32x4& acc, bf16x8& a, bf16x8 b) { asm volatile("s_waitcnt vmcnt(%3)\n\tv_mfma_f32_16x16x32_bf16 %0, %1, %2, %0" : "+v"(acc), "+v"(a) : "v"(b), "i"(N)); }
template <int AUX = 0> DI bf16x8 bld(rsrc_t rs, unsigned voff, unsigned soff) { return __builtin_bit_cast(bf16x8, __builtin_amdgcn_raw_buffer_load_b128(rs, voff, soff, AUX)); }
template <int AUX = 0> DI u32x4 bld4(rsrc_t rs, unsigned voff, unsigned soff) { return __builtin_amdgcn_raw_buffer_load_b128(rs, voff, soff, AUX); }
template <int AUX = 0> DI f32x4 bld4f(rsrc_t rs, unsigned voff, unsigned soff) { return __builtin_bit_cast(f32x4, __builtin_amdgcn_raw_buffer_load_b128(rs, voff, soff, AUX)); }
template <int AUX = 0> DI unsigned bld1(rsrc_t rs, unsigned voff, unsigned soff) { return __builtin_amdgcn_raw_buffer_load_b32(rs, voff, soff, AUX); }
template <int AUX = 0> DI void bst4(u32x4 v, rsrc_t rs, unsigned voff, unsigned soff) {
  __builtin_amdgcn_raw_buffer_store_b128(v, rs, voff, soff, AUX);
  asm volatile("s_nop 1" :: "v"(v) : "memory");
}
template <int AUX = 0> DI void bst2(u32x2 v, rsrc_t rs, unsigned voff, unsigned soff) { __builtin_amdgcn_raw_buffer_store_b64(v, rs, voff, soff, AUX); }
template <int AUX = 0> DI void bst1(unsigned v, rsrc_t rs, unsigned voff, unsigned soff) { __builtin_amdgcn_raw_buffer_store_b32(v, rs, voff, soff, AUX); }
constexpr unsigned OOB = 0x7ffffff0u;
DI void dma(rsrc_t rs, unsigned voff, unsigned soff, unsigned lds_dst) {
  unsigned sv;
  asm volatile("s_mov_b32 %0, m0\n\ts_mov_b32 m0, %1\n\ts_nop 0\n\tbuffer_load_dwordx4 %2, %3, %4 offen lds\n\ts_mov_b32 m0, %0"
               : "=&s"(sv) : "s"(lds_dst), "v"(voff), "s"(rs), "s"(soff) : "memory");
}
DI void pull(rsrc_t rs, unsigned voff, unsigned soff, unsigned lds_junk) {
  unsigned sv;
  asm volatile("s_mov_b32 %0, m0\n\ts_mov_b32 m0, %1\n\ts_nop 0\n\tbuffer_load_dword %2, %3, %4 offen sc1 lds\n\ts_mov_b32 m0, %0"
               : "=&s"(sv) : "s"(lds_junk), "v"(voff), "s"(rs), "s"(soff) : "memory");
}
template <class F>
DI void wg_signal(char* ws, unsigned foff, int tid, F after_drain) {
  asm volatile("s_waitcnt vmcnt(0)" ::: "memory");
  after_drain();
  raw_barrier();
  if (tid == 0) __hip_atomic_store((gu32*)(ws + foff), 1u, __ATOMIC_RELAXED, __HIP_MEMORY_SCOPE_AGENT);
}
DI void wave_wait(char* ws, unsigned foff, unsigned lane_off) {
  gu32* f = (gu32*)(ws + foff + lane_off);
#pragma unroll 1
  for (unsigned spins = 0; spins < (1u << 22); ++spins) {
    const unsigned v = __hip_atomic_load(f, __ATOMIC_RELAXED, __HIP_MEMORY_SCOPE_AGENT);
    if (__all(v != 0u)) break;
#define POLL_SLEEP 1
    __builtin_amdgcn_s_sleep(POLL_SLEEP);
  }
  asm volatile("" ::: "memory");
}

DI int mybid() { int b = blockIdx.x; asm volatile("" : "+s"(b)); return b; }

constexpr int LDS_BYTES = 163840;
constexpr int AL_RO = 0;
constexpr int AL_RL = AL_RO + 32768;
constexpr int AL_O  = AL_RL + 2048;
constexpr int AL_Q  = AL_O + 4096;
constexpr int AL_FL = AL_Q + 8192;
constexpr int AL_BND = AL_FL + 256;
constexpr int AL_CS = AL_BND + 512;
constexpr int AL_JUNK = AL_Q;
constexpr int AL_W1 = 49152;
constexpr int AL_MX = AL_W1;
static_assert(AL_CS + 512 <= AL_W1 && AL_W1 + 112 * 1024 <= LDS_BYTES, "lds");
constexpr float SHIFT = 32.f;

DI void dma_w1(rsrc_t rs, unsigned lds0, unsigned l16, int nt0, int w, int t, int kk) {
  dma(rs, l16, OFF_W1 + ((nt0 + t) * 16 + 2 * w + kk) * KB, lds0 + AL_W1 + ((2 * w + kk) * 7 + t) * 1024);
}

DI void ffn2_prefetch(rsrc_t rs, unsigned l16, int nt0, int w, bf16x8 (&stg)[28], int i) {
  const int ti = i & 3;
  stg[i] = bld(rs, l16, OFF_W2 + ((nt0 + ((ti & 2) ^ (2 * (w & 1))) + (ti & 1)) * 56 + 7 * w + (i >> 2)) * KB);
}

constexpr int PRE_ATT = 8;
DI void attn_phase(char* lds, char* ws, rsrc_t rs, const u16* xin, int layer, int tid, bf16x8 (&stg)[28]) {
  const int lane = tid & 63, r = lane & 31, h = lane >> 5;
  const int w = __builtin_amdgcn_readfirstlane(tid >> 6);
  const int bid = mybid(), xcd = bid & 7, idx = bid >> 3;
  const int t0 = xcd * 256 + idx * 8;
  const unsigned lds0 = (unsigned)(uintptr_t)lds;
  const unsigned qkv = OFF_QKV + layer * QKV_BYTES;
  const unsigned l16 = lane * 16;
  const int f1_nt0 = (idx & 15) * 7;
  STAMP(layer ? 1 : 6, 0);
  if (layer == 1) wave_wait(ws, flag_off(0, 3, 0), (h == 0 ? w * 8 + (lane >> 2) : (xcd * 4 + (idx >> 3)) * 2 + ((lane >> 2) & 1)) * 128 + (lane & 3) * 4);
  STAMP(layer ? 1 : 6, 3);
  const unsigned qs = qkv + QO_Q + t0 * 1024;
  bf16x8 qf = bld<SC1>(rs, l16, qs);
  SB();
  bf16x8 kf[8], vf[16], qn, qw;
  unsigned kn_a, qn_a, c1p;
  const unsigned kb = qkv + QO_K + w * 8192, vb = qkv + QO_VT + w * 8192;
  const unsigned ko = r * 32 + 16 * h, vo = (r >> 4) * 512 + h * 256 + (r & 15) * 16;
  auto load_kv = [&](int t) {
    if (t < 7) kf[t + 1] = bld<SC1>(rs, ko + ((t + 1) & 3) * 1024, kb + ((t + 1) >> 2) * 4096);
    else qn = bld<SC1>(rs, l16 + 1024, qs);
    vf[2 * t] = bld<SC1>(rs, vo + (t & 3) * 1024, vb + (t >> 2) * 4096);
    asm volatile("" ::: "memory");
    SB();
  };
#define KV_PD 3
  kf[0] = bld<SC1>(rs, ko, kb);
  SB();
#pragma unroll
  for (int t = 0; t < KV_PD; ++t) load_kv(t);
  { unsigned z = 0u; PIN1(z); *(u32x2*)(lds + AL_O + tid * 8) = (u32x2){z, z}; }
  ((unsigned*)(lds + AL_FL))[lane] = h;
  const unsigned vconst = (lane & 15) == 0 ? 0x3F803F80u : 0u;
  auto vsplit = [&](int t) {
    u32x4 x = __builtin_bit_cast(u32x4, vf[2 * t]), y;
#pragma unroll
    for (int i = 0; i < 4; ++i) { auto sw = __builtin_amdgcn_permlane16_swap(x[i], vconst, false, false); x[i] = sw[0]; y[i] = sw[1]; }
    vf[2 * t] = __builtin_bit_cast(bf16x8, x); vf[2 * t + 1] = __builtin_bit_cast(bf16x8, y);
  };
  const unsigned wos = OFF_WO + (4 * w * 16) * KB;
  f32x4 oacc[4] = {};
  f32x16 o;
  char* const ro_w = lds + AL_RO + w * 2048 + lane * 8;
  const int lf_w0 = h == 0 ? AL_RL + (r * 8 + w) * 4 : AL_FL + (w * 4 + (r & 3)) * 4, lf_step = h == 0 ? 1024 : 0;
  const int o_wr = AL_O + (r >> 4) * 1024 + ((2 * ((w & 3) >> 1) + h) * 16 + (w >> 2) * 8 + (r & 7)) * 16 + ((r >> 3) & 1) * 8 + (w & 1) * 4;
  auto publish = [&](int g) {
    char* ro = ro_w + (g & 1) * 16384;
    *(f32x2*)(ro) = (f32x2){o[0], o[1]}; *(f32x2*)(ro + 512) = (f32x2){o[2], o[3]};
    *(f32x2*)(ro + 1024) = (f32x2){o[4], o[5]}; *(f32x2*)(ro + 1536) = (f32x2){o[6], o[7]};
    asm volatile("" ::: "memory");
    *(volatile LAS unsigned*)((LAS char*)lds + lf_w0 + (g & 1) * lf_step) = h == 0 ? __float_as_uint(o[8]) : (unsigned)(g + 1);
  };
  f32x2 ra[4]; f32x4 rl0, rl1;
  const char* const red_r = lds + AL_RO + (w >> 2) * 8192 + (w & 3) * 512 + lane * 8;
  auto reduce_ld = [&](int g) {
#pragma unroll
    for (int i = 0; i < 4; ++i) ra[i] = *(const f32x2*)(red_r + (g & 1) * 16384 + i * 2048);
    rl0 = *(const f32x4*)(lds + AL_RL + (g & 1) * 1024 + r * 32); rl1 = *(const f32x4*)(lds + AL_RL + (g & 1) * 1024 + r * 32 + 16);
  };
  auto reduce_fin = [&](int g) {
    const f32x4 l4 = rl0 + rl1;
    const f32x2 l2 = (f32x2){l4[0], l4[1]} + (f32x2){l4[2], l4[3]};
    const float inv = __builtin_amdgcn_rcpf(l2[0] + l2[1]);
    const f32x2 sa = (ra[0] + ra[1]) + (ra[2] + ra[3]);
    *(unsigned*)(lds + o_wr + (g & 1) * 2048) = pk2(sa[0] * inv, sa[1] * inv);
    asm volatile("" ::: "memory");
    *(volatile LAS unsigned*)((LAS char*)lds + AL_FL + 128 + w * 16 + (lane & 3) * 4) = (unsigned)(g + 2);
  };
  auto reduce = [&](int g) { reduce_ld(g); reduce_fin(g); };
  const int g4 = lane >> 4, mm = lane & 15;
  const unsigned fs = ((t0 >> 4) * 16 + 2 * w) * KB, fv = (mm >> 3) * 1024 + (g4 * 16 + (t0 & 15) + (mm & 7)) * 16;
  u32x4 xr;
  auto load_res = [&]() {
    if (layer == 0) {
      const u16* xp = xin + (size_t)(t0 + (mm & 7)) * D_ + 64 * w + 32 * (mm >> 3) + 4 * g4;
      const u32x2 lo = *(const u32x2*)xp, hi = *(const u32x2*)(xp + 16);
      xr = (u32x4){lo[0], lo[1], hi[0], hi[1]};
    } else xr = bld4<SC1>(rs, fv, OFF_X2 + fs);
  };
  f32x16 cinit;
  { float ns = -SHIFT; PIN1(ns);
#pragma unroll
    for (int i = 0; i < 16; ++i) cinit[i] = ns; }
  PIN1(cinit);
  bf16x8 wof[4] = {qf, qf, qf, qf}, of;
  u32x4 p0, p1;
  unsigned flg;
  const unsigned fl_addr = lds0 + AL_FL + lane * 4;
  STAMP(layer ? 1 : 6, 1);
  f32x16 sc = mfma32(kf[0], qf, cinit);
  bf16x8 wl[16];
  auto group = [&](auto mode_tag, const int g) {
    constexpr bool FIRST = decltype(mode_tag)::value == 0, LAST = decltype(mode_tag)::value == 2;
#pragma unroll
    for (int t = 0; t < 8; ++t) {
      if (t == 3) __builtin_amdgcn_s_setprio(3); if (t == 5) __builtin_amdgcn_s_setprio(2); if (t == 7) __builtin_amdgcn_s_setprio(1); if (t == 1) __builtin_amdgcn_s_setprio(0);
      f32x16 sn;
      if (!(LAST && t == 7)) sn = mfma32(kf[(t + 1) & 7], t < 7 ? qf : qn, cinit);
      SB();
      if (FIRST) {
        if (t + KV_PD < 8) load_kv(t + KV_PD);
        if (t == 8 - KV_PD || (KV_PD == 8 && t == 0)) {
          qw = bld<SC1>(rs, l16, qs + w * 1024);
          kn_a = bld1<SC1>(rs, (lane & 15) * 4, qkv + QO_KN2 + w * 64);
          qn_a = bld1<SC1>(rs, (lane & 15) * 4, qkv + QO_QN2 + (t0 >> 4) * 64);
          c1p = bld1(rs, (lane >> 4) * 7168 + (lane & 15) * 4, OFF_CS1 + (f1_nt0 + (w < 7 ? w : 6)) * 64);
          asm volatile("" ::: "memory");
          SB();
        }
        vsplit(t); SB();
      }
      float e0 = EX(sc[0]), e1 = EX(sc[1]), e2 = EX(sc[2]), e3 = EX(sc[3]), e4 = EX(sc[4]), e5 = EX(sc[5]);
      PIN4(e0, e1, e2, e3); PIN2(e4, e5); SB();
      if (t > 0 || !FIRST) o = mfma32(vf[(2 * t + 15) & 15], __builtin_bit_cast(bf16x8, p1), o);
      SB();
      float e6 = EX(sc[6]), e7 = EX(sc[7]);
      PIN2(e6, e7); SB();
      p0[0] = pk2(e0, e1); p0[1] = pk2(e2, e3); p0[2] = pk2(e4, e5); p0[3] = pk2(e6, e7);
      PIN1(p0); SB();
      float e8 = EX(sc[8]), e9 = EX(sc[9]);
      PIN2(e8, e9); SB();
      float e10, e11, e12, e13;
      if (t == 0) {
        if (!FIRST) { e10 = EX(sc[10]); e11 = EX(sc[11]); e12 = EX(sc[12]); e13 = EX(sc[13]); PIN4(e10, e11, e12, e13); PIN1(o); SB(); publish(g - 1); SB(); }
        f32x16 z = {}; o = mfma32(vf[0], __builtin_bit_cast(bf16x8, p0), z);
      } else o = mfma32(vf[2 * t], __builtin_bit_cast(bf16x8, p0), o);
      SB();
      if (t > 0 || FIRST) { e10 = EX(sc[10]); e11 = EX(sc[11]); e12 = EX(sc[12]); e13 = EX(sc[13]); PIN4(e10, e11, e12, e13); SB(); }
      if (!FIRST) {
        constexpr int X = LAST ? 3 : 0;
        if (t == 0) mfma16_w<3>(oacc[0], wof[0], of); else if (t == 1) mfma16_w<4 + X>(oacc[1], wof[1], of);
        else if (t == 2) mfma16_w<5 + 2 * X>(oacc[2], wof[2], of); else if (t == 3) mfma16_w<6 + 3 * X>(oacc[3], wof[3], of);
        else if (t == 4) mfma16_w<6 + 4 * X>(oacc[0], wof[0], of); else if (t == 5) mfma16_w<5 + 4 * X>(oacc[1], wof[1], of);
        else if (t == 6) mfma16_w<4 + 4 * X>(oacc[2], wof[2], of); else mfma16_w<3 + 4 * X>(oacc[3], wof[3], of);
        SB();
      }
      float e14 = EX(sc[14]), e15 = EX(sc[15]);
      PIN2(e14, e15); SB();
      p1[0] = pk2(e8, e9); p1[1] = pk2(e10, e11); p1[2] = pk2(e12, e13); p1[3] = pk2(e14, e15);
      PIN1(p1); SB();
      if (!FIRST || t >= 4) {
        const int gw = FIRST ? 0 : (g > 2 ? g - 2 : 0);
        bld_asm(wof[t & 3], rs, l16, wos + ((t & 3) * 16 + 2 * gw + (t >> 2)) * 1024);
        SB();
      }
      if (LAST) {
#pragma unroll
        for (int i = 2 * t; i < 2 * t + 2; ++i) { wl[i] = bld(rs, l16, wos + ((i & 3) * 16 + 12 + (i >> 2)) * 1024); asm volatile("" ::: "memory"); }
        ffn2_prefetch(rs, l16, (idx & 7) * 4, w, stg, t); asm volatile("" ::: "memory");
        SB();
      }
      if (!FIRST && (t == 1 || t == 2)) { dma_w1(rs, lds0, l16, f1_nt0, w, g - 1, t - 1); SB(); }
      if (!FIRST && t == 0) { dma(rs, l16, OFF_W2 + ((idx * 7 + g - 1) * 8 + w) * KB, lds0 + AL_JUNK); SB(); }
      if (!FIRST && t == 1) { flg = *(volatile LAS unsigned*)((LAS char*)lds + AL_FL + lane * 4); SB(); }
      if (!FIRST && t == 2) {
        asm volatile("1:\n\tv_cmp_gt_u32 vcc, %2, %0\n\ts_cbranch_vccz 2f\n\ts_sleep 1\n\tds_read_b32 %0, %1\n\ts_waitcnt lgkmcnt(0)\n\ts_branch 1b\n2:"
                     : "+v"(flg) : "v"(fl_addr), "s"(g) : "vcc", "memory");
        reduce_ld(g - 1);
        SB();
      }
      if (!FIRST && t == 3) {
        reduce_fin(g - 1);
        SB();
      }
      if (!FIRST && (t & 3) == 3) { of = lds_frag(lds, AL_O + (g & 1) * 2048 + (t >> 2) * 1024 + l16); SB(); }
      if (!FIRST && !LAST && t == 5) { qn = lds_frag(lds, AL_Q + (g + 1) * 1024 + l16); SB(); }
      if (!(LAST && t == 7)) sc = sn;
    }
    qf = qn;
  };
  group(std::integral_constant<int, 0>{}, 0);
  *(LAS bf16x8*)((LAS char*)lds + AL_Q + w * 1024 + l16) = qw;
  ((float*)(lds + AL_BND))[w * 16 + (lane & 15)] = __uint_as_float(kn_a) * __uint_as_float(row16_umax(qn_a));
  { const float c1 = xrow_sum(__uint_as_float(c1p)); if (lane < 16) ((float*)(lds + AL_CS))[w * 16 + lane] = c1; }
  raw_barrier();
  const f32x2 kq2 = *(const f32x2*)(lds + AL_BND + lane * 8);
  const bool slow = !__all(kq2[0] <= SHIFT * SHIFT * 0.998f && kq2[1] <= SHIFT * SHIFT * 0.998f);
  of = lds_frag(lds, AL_O + l16);
  STAMP(layer ? 1 : 6, 2);
  if (!slow) {
#pragma unroll 1
    for (int g = 1; g < 7; ++g) group(std::integral_constant<int, 1>{}, g);
    group(std::integral_constant<int, 2>{}, 7);
    __builtin_amdgcn_s_setprio(0);
    o = mfma32(vf[15], __builtin_bit_cast(bf16x8, p1), o);
    SB();
    load_res();
    SB();
    publish(7);
    raw_barrier();
    STAMP(layer ? 1 : 6, 9);
    reduce_ld(7);
    bf16x8 of0 = lds_frag(lds, AL_O + l16), of1 = lds_frag(lds, AL_O + 1024 + l16);
    SB();
#pragma unroll
    for (int t = 0; t < 4; ++t) mfma16_w<4>(oacc[t], wof[t], of);
    SB();
    reduce_fin(7);
    SB();
#pragma unroll
    for (int t = 0; t < 4; ++t) mfma16_ip(oacc[t], wl[t], of0);
#pragma unroll
    for (int t = 0; t < 4; ++t) mfma16_ip(oacc[t], wl[4 + t], of1);
    raw_barrier();
    STAMP(layer ? 1 : 6, 10);
    of0 = lds_frag(lds, AL_O + 2048 + l16); of1 = lds_frag(lds, AL_O + 3072 + l16);
#pragma unroll
    for (int t = 0; t < 4; ++t) mfma16_ip(oacc[t], wl[8 + t], of0);
#pragma unroll
    for (int t = 0; t < 4; ++t) mfma16_ip(oacc[t], wl[12 + t], of1);
    asm volatile("s_nop 7" : "+v"(oacc[0]), "+v"(oacc[1]), "+v"(oacc[2]), "+v"(oacc[3]));
  } else {
    __builtin_amdgcn_s_setprio(0);
    asm volatile("s_waitcnt vmcnt(0)" : "+v"(wof[0]), "+v"(wof[1]), "+v"(wof[2]), "+v"(wof[3]) :: "memory");
    float* mx = (float*)(lds + AL_MX);
    const f32x16 z = {};
#pragma unroll 1
    for (int g = 0; g < 8; ++g) {
      const bf16x8 q = lds_frag(lds, AL_Q + g * 1024 + l16);
      float m = -INFINITY;
#pragma unroll
      for (int t = 0; t < 8; ++t) {
        const f32x16 s = mfma32(kf[t], q, z);
#pragma unroll
        for (int i = 0; i < 16; ++i) m = fmaxf(m, s[i]);
        SB();
      }
      { auto sw = __builtin_amdgcn_permlane32_swap(__float_as_uint(m), __float_as_uint(m), false, false); m = fmaxf(__uint_as_float(sw[0]), __uint_as_float(sw[1])); }
      if (h == 0) mx[w * 256 + g * 32 + r] = m;
    }
    __syncthreads();
#pragma unroll 1
    for (int g = 0; g < 8; ++g) {
      const bf16x8 q = lds_frag(lds, AL_Q + g * 1024 + l16);
      float m = mx[g * 32 + r];
#pragma unroll
      for (int i = 1; i < 8; ++i) m = fmaxf(m, mx[i * 256 + g * 32 + r]);
      o = z;
#pragma unroll
      for (int t = 0; t < 8; ++t) {
        const f32x16 s = mfma32(kf[t], q, z);
        u32x4 a0, a1;
#pragma unroll
        for (int i = 0; i < 4; ++i) {
          a0[i] = pk2(EX(s[2 * i] - m), EX(s[2 * i + 1] - m));
          a1[i] = pk2(EX(s[8 + 2 * i] - m), EX(s[9 + 2 * i] - m));
        }
        o = mfma32(vf[2 * t], __builtin_bit_cast(bf16x8, a0), o);
        o = mfma32(vf[2 * t + 1], __builtin_bit_cast(bf16x8, a1), o);
        SB();
      }
      publish(g);
      __syncthreads();
      reduce(g);
      __syncthreads();
      const bf16x8 b0 = lds_frag(lds, AL_O + (g & 1) * 2048 + l16), b1 = lds_frag(lds, AL_O + (g & 1) * 2048 + 1024 + l16);
#pragma unroll
      for (int t = 0; t < 4; ++t) {
        oacc[t] = mfma16(bld(rs, l16, wos + (t * 16 + 2 * g) * 1024), b0, oacc[t]);
        oacc[t] = mfma16(bld(rs, l16, wos + (t * 16 + 2 * g + 1) * 1024), b1, oacc[t]);
      }
    }
    load_res();
#pragma unroll
    for (int i = 0; i < PRE_ATT; ++i) ffn2_prefetch(rs, l16, (idx & 7) * 4, w, stg, i);
#pragma unroll 1
    for (int i = 0; i < 14; ++i) dma_w1(rs, lds0, l16, f1_nt0, w, i >> 1, i & 1);
    asm volatile("s_nop 7" : "+v"(oacc[0]), "+v"(oacc[1]), "+v"(oacc[2]), "+v"(oacc[3]));
  }
  float xv[8];
  asm volatile("s_nop 1\n\t"
               "v_add_f32_dpp %0, %8, %8 row_ror:8 row_mask:0xf bank_mask:0x3\n\t"
               "v_add_f32_dpp %1, %9, %9 row_ror:8 row_mask:0xf bank_mask:0x3\n\t"
               "v_add_f32_dpp %2, %10, %10 row_ror:8 row_mask:0xf bank_mask:0x3\n\t"
               "v_add_f32_dpp %3, %11, %11 row_ror:8 row_mask:0xf bank_mask:0x3\n\t"
               "v_add_f32_dpp %4, %12, %12 row_ror:8 row_mask:0xf bank_mask:0x3\n\t"
               "v_add_f32_dpp %5, %13, %13 row_ror:8 row_mask:0xf bank_mask:0x3\n\t"
               "v_add_f32_dpp %6, %14, %14 row_ror:8 row_mask:0xf bank_mask:0x3\n\t"
               "v_add_f32_dpp %7, %15, %15 row_ror:8 row_mask:0xf bank_mask:0x3\n\t"
               "v_add_f32_dpp %0, %16, %16 row_ror:8 row_mask:0xf bank_mask:0xc\n\t"
               "v_add_f32_dpp %1, %17, %17 row_ror:8 row_mask:0xf bank_mask:0xc\n\t"
               "v_add_f32_dpp %2, %18, %18 row_ror:8 row_mask:0xf bank_mask:0xc\n\t"
               "v_add_f32_dpp %3, %19, %19 row_ror:8 row_mask:0xf bank_mask:0xc\n\t"
               "v_add_f32_dpp %4, %20, %20 row_ror:8 row_mask:0xf bank_mask:0xc\n\t"
               "v_add_f32_dpp %5, %21, %21 row_ror:8 row_mask:0xf bank_mask:0xc\n\t"
               "v_add_f32_dpp %6, %22, %22 row_ror:8 row_mask:0xf bank_mask:0xc\n\t"
               "v_add_f32_dpp %7, %23, %23 row_ror:8 row_mask:0xf bank_mask:0xc\n\t"
               : "=&v"(xv[0]), "=&v"(xv[1]), "=&v"(xv[2]), "=&v"(xv[3]), "=&v"(xv[4]), "=&v"(xv[5]), "=&v"(xv[6]), "=&v"(xv[7])
               : "v"(oacc[0][0]), "v"(oacc[0][1]), "v"(oacc[0][2]), "v"(oacc[0][3]), "v"(oacc[1][0]), "v"(oacc[1][1]), "v"(oacc[1][2]), "v"(oacc[1][3]), "v"(oacc[2][0]), "v"(oacc[2][1]), "v"(oacc[2][2]), "v"(oacc[2][3]), "v"(oacc[3][0]), "v"(oacc[3][1]), "v"(oacc[3][2]), "v"(oacc[3][3]));
  u32x4 ov;
  float sm = 0.f, sq = 0.f;
#pragma unroll
  for (int q = 0; q < 4; ++q) {
    const float a = xv[2 * q] + bflo(xr[q]), b = xv[2 * q + 1] + bfhi(xr[q]);
    ov[q] = pk2(a, b);
    sm += a + b; sq = fmaf(a, a, fmaf(b, b, sq));
  }
  bst4<SC1>(ov, rs, fv, qkv + QO_X1 + fs);
  sm += dpp<0x128>(sm); sq += dpp<0x128>(sq);
  xrow_sum2(sm, sq);
  bst2<SC1>((u32x2){__float_as_uint(sm), __float_as_uint(sq)}, rs, lane < 8 ? lane * 64 : OOB, qkv + QO_ST + t0 * 64 + w * 8);
  STAMP(layer ? 1 : 6, 11);
  wg_signal(ws, flag_off(layer, 0, t0 >> 4) + (t0 & 8) / 2, tid, [&]() {});
}

DI void ffn1_phase(char* lds, char* ws, rsrc_t rs, int layer, int tid, bf16x8 (&stg)[28]) {
  const int lane = tid & 63, g = lane >> 4, mm = lane & 15;
  const int w = __builtin_amdgcn_readfirstlane(tid >> 6);
  const int bid = mybid(), xcd = bid & 7, idx = bid >> 3, rb = idx >> 4, cb = idx & 15;
  const int mt = xcd * 16 + rb * 8 + w, nt0 = cb * 7;
  const unsigned l16 = lane * 16;
  STAMP(2, 0);
#define PRE_EARLY 14
#define PRE_LATE 4
#pragma unroll
  for (int i = PRE_ATT; i < PRE_EARLY; ++i) ffn2_prefetch(rs, l16, (idx & 7) * 4, w, stg, i);
  bf16x8 a[2][7];
#pragma unroll
  for (int t = 0; t < 7; ++t) a[0][t] = lds_frag(lds, AL_W1 + t * 1024 + l16);
  wave_wait(ws, flag_off(layer, 0, mt), (lane & 1) * 4);
  STAMP(2, 1);
  constexpr int PD = 5;
  bf16x8 b[16];
  const unsigned lb = OFF_QKV + layer * QKV_BYTES, bs = lb + QO_X1 + mt * 16 * KB;
#pragma unroll
  for (int ks = 0; ks < PD; ++ks) b[ks] = bld<SC1>(rs, l16, bs + ks * KB);
  const f32x4 stp = bld4f<SC1>(rs, mm * 64 + g * 16, lb + QO_ST + mt * 1024);
  SB();
  STAMP(2, 2);
  f32x4 acc[7] = {};
  float rstd, nmr;
#pragma unroll
  for (int ks = 0; ks < 16; ++ks) {
    if (ks < 15) {
#pragma unroll
      for (int t = 0; t < 7; ++t) a[(ks + 1) & 1][t] = lds_frag(lds, AL_W1 + ((ks + 1) * 7 + t) * 1024 + l16);
    }
    if (ks + PD < 16) b[ks + PD] = bld<SC1>(rs, l16, bs + (ks + PD) * KB);
    {
      constexpr int NMID = 28 - PRE_EARLY - PRE_LATE;
      const int lo = ks <= 10 ? (ks * 6) / 11 : 6 + (ks - 11) * 2, hi = ks + 1 <= 10 ? ((ks + 1) * 6) / 11 : 6 + (ks + 1 - 11) * 2;
#pragma unroll
      for (int i = lo; i < hi; ++i) if (i < NMID) ffn2_prefetch(rs, l16, (idx & 7) * 4, w, stg, PRE_EARLY + i);
    }
    if (ks == 8) {
      float sm = stp[0] + stp[2], sq = stp[1] + stp[3];
      xrow_sum2(sm, sq);
      const float mean = sm * (1.f / D_);
      rstd = __builtin_amdgcn_rsqf(fmaxf(sq * (1.f / D_) - mean * mean, 0.f) + LN_EPS); nmr = -mean * rstd;
      PIN2(rstd, nmr);
    }
    SB();
#pragma unroll
    for (int t = 0; t < 7; ++t) acc[t] = mfma16(a[ks & 1][t], b[ks], acc[t]);
    SB();
    if (ks == 0) STAMP(2, 3);
    if (ks == 7) STAMP(2, 4);
  }
  STAMP(2, 10);
  u32x2 hv[7];
#pragma unroll
  for (int t = 0; t < 7; ++t) {
    const f32x4 v = acc[t] * rstd + *(const f32x4*)(lds + AL_CS + t * 64 + g * 16) * nmr;
    hv[t] = (u32x2){relu_pk(pk2(v[0], v[1])), relu_pk(pk2(v[2], v[3]))};
  }
  const unsigned hs = lb + QO_H + (mt * 56 + (nt0 >> 1)) * KB;
  if ((cb & 1) == 0) {
#pragma unroll
    for (int p = 0; p < 3; ++p) bst4<SC1>((u32x4){hv[2 * p][0], hv[2 * p][1], hv[2 * p + 1][0], hv[2 * p + 1][1]}, rs, l16, hs + p * KB);
    bst2<SC1>(hv[6], rs, l16, hs + 3 * KB);
  } else {
    bst2<SC1>(hv[0], rs, l16 + 8, hs);
#pragma unroll
    for (int p = 0; p < 3; ++p) bst4<SC1>((u32x4){hv[2 * p + 1][0], hv[2 * p + 1][1], hv[2 * p + 2][0], hv[2 * p + 2][1]}, rs, l16, hs + (p + 1) * KB);
  }
  STAMP(2, 11);
  wg_signal(ws, flag_off(layer, 1, (xcd * 2 + rb) * 8 + (cb >> 1)) + (cb & 1) * 4, tid,
            [&]() {});
}

#define QW_EARLY
DI void ffn2_phase(char* lds, char* ws, rsrc_t rs, u16* out, int layer, int tid, bf16x8 (&stg)[28]) {
  const int lane = tid & 63, g = lane >> 4, mm = lane & 15;
  const int w = __builtin_amdgcn_readfirstlane(tid >> 6);
  const int bid = mybid(), xcd = bid & 7, idx = bid >> 3, rb = idx >> 3, cb = idx & 7;
  const int fm = w >> 1, fp = w & 1, mt0 = xcd * 16 + rb * 4, nt0 = cb * 4;
  const unsigned lds0 = (unsigned)(uintptr_t)lds, l16 = lane * 16;
  STAMP(3, 0);
#pragma unroll
  for (int i = 28 - PRE_LATE; i < 28; ++i) ffn2_prefetch(rs, l16, nt0, w, stg, i);
  const unsigned fs = ((mt0 + fm) * 16 + (nt0 >> 1) + fp) * KB;
  const unsigned lb = OFF_QKV + layer * QKV_BYTES;
  const u32x4 xr = bld4<SC1>(rs, l16, lb + QO_X1 + fs);
  if (layer == 0) {
#pragma unroll
    for (int i = 0; i < 6; ++i) { const int c = 6 * w + i; dma(rs, l16, OFF_WQKV + ((4 * cb + (c & 3)) * 16 + (c >> 2)) * KB, lds0 + c * 1024); }
  }
  wave_wait(ws, flag_off(layer, 1, (xcd * 2 + (rb >> 1)) * 8 + w), (lane & 1) * 4);
  STAMP(3, 1);
#define F2_PD 6
  constexpr int PD = F2_PD;
  bf16x8 b[28];
  auto ldb = [&](int i) { b[i] = bld<SC1>(rs, l16, lb + QO_H + ((mt0 + ((fm + 1 + i / 7) & 3)) * 56 + 7 * w + i % 7) * KB); };
#pragma unroll
  for (int i = 0; i < PD; ++i) ldb(i);
  SB();
  STAMP(3, 2);
  f32x4 acc[2][4];
  auto send = [&](int mi, int pr) {
    const int d = 2 * ((fm + 1 + mi) & 3) + (pr ^ fp);
    char* xb = lds + AL_W1 + (d * 7 + ((w - d - 1) & 7)) * 2048 + l16;
    *(f32x4*)(xb) = acc[mi & 1][2 * pr]; *(f32x4*)(xb + 1024) = acc[mi & 1][2 * pr + 1];
  };
#pragma unroll
  for (int i = 0; i < 28; ++i) {
    const int mi = i / 7, j = i % 7;
    if (i + PD < 28) ldb(i + PD);
    if (false) {
      const int c = 6 * w + (i - (28 - PD));
      dma(rs, l16, OFF_WQKV + ((4 * cb + (c & 3)) * 16 + (c >> 2)) * KB, lds0 + c * 1024);
    }
    if (mi > 0 && j == 3) send(mi - 1, 0);
    if (mi > 0 && j == 5) send(mi - 1, 1);
    SB();
#pragma unroll
    for (int t = 0; t < 4; ++t) {
      if (j == 0) { const f32x4 z = {}; acc[mi & 1][t] = mfma16(stg[t], b[i], z); }
      else acc[mi & 1][t] = mfma16(stg[4 * j + t], b[i], acc[mi & 1][t]);
    }
    SB();
  }
  STAMP(3, 9);
  send(3, 1);
  f32x4 s0 = acc[1][0] + (f32x4){bflo(xr[0]), bfhi(xr[0]), bflo(xr[1]), bfhi(xr[1])};
  f32x4 s1 = acc[1][1] + (f32x4){bflo(xr[2]), bfhi(xr[2]), bflo(xr[3]), bfhi(xr[3])};
  raw_barrier();
#pragma unroll
  for (int d = 0; d < 7; ++d) {
    const char* xb = lds + AL_W1 + (w * 7 + d) * 2048 + l16;
    s0 += *(const f32x4*)(xb); s1 += *(const f32x4*)(xb + 1024);
  }
  STAMP(3, 10);
  const u32x4 o = {pk2(s0[0], s0[1]), pk2(s0[2], s0[3]), pk2(s1[0], s1[1]), pk2(s1[2], s1[3])};
  if (layer == 1) {
    u16* op = out + (size_t)((mt0 + fm) * 16 + mm) * D_ + (nt0 + 2 * fp) * 16 + 4 * g;
    *(u32x2*)op = (u32x2){o[0], o[1]};
    *(u32x2*)(op + 16) = (u32x2){o[2], o[3]};
    STAMP(3, 11);
  } else {
    bst4<SC1>(o, rs, l16, OFF_X2 + fs);
    STAMP(3, 11);
    wg_signal(ws, flag_off(0, 2, (xcd * 4 + rb) * 2 + (cb >> 2)) + (cb & 3) * 4, tid, [&]() {});
  }
}

typedef short v4i16_t __attribute__((ext_vector_type(4)));
DI bf16x8 tr_frag(const char* lds, int off0, int off1) {
  const v4i16_t lo = __builtin_amdgcn_ds_read_tr16_b64_v4i16((LAS v4i16_t*)((LAS char*)lds + off0));
  const v4i16_t hi = __builtin_amdgcn_ds_read_tr16_b64_v4i16((LAS v4i16_t*)((LAS char*)lds + off1));
  return __builtin_shufflevector(lo, hi, 0, 1, 2, 3, 4, 5, 6, 7);
}
constexpr int QL_KV = 65536, QL_P = 81920, QL_TR = QL_P + 65536;
constexpr int QP_T = 0, QP_KV = 2048, QP_CS = 3072, QP_CK = 3136, QP_ST = 3200;
template <bool FIRST, class H1, class H2>
DI void qkv_phase(char* lds, char* ws, rsrc_t rs, int tid, const u16* x, const u16* wq, const u16* wkv, H1 hook1, H2 hook2) {
  const int lane = tid & 63, g = lane >> 4, mm = lane & 15;
  const int w = __builtin_amdgcn_readfirstlane(tid >> 6);
  const int bid = mybid(), xcd = bid & 7, idx = bid >> 3, rb = idx >> 3, cb = idx & 7;
  const int ml = w & 3, hf = w >> 2, mt = xcd * 16 + rb * 4 + ml;
  const int kvml = cb >> 1;
  const unsigned lds0 = (unsigned)(uintptr_t)lds, l16 = lane * 16;
  const unsigned qkv = OFF_QKV + (FIRST ? 0 : QKV_BYTES);
  char* const mine = lds + QL_P + w * 8192;
  const char* const other = lds + QL_P + (w ^ 4) * 8192;
  STAMP(FIRST ? 5 : 0, 0);
  bf16x8 b[8];
  f32x4 cs0, cs1, csk; float csv;
  if (FIRST) {
    const rsrc_t rw = __builtin_amdgcn_make_buffer_rsrc((void*)wq, 0, D_ * D_ * 2, 0x00020000);
    const rsrc_t rk = __builtin_amdgcn_make_buffer_rsrc((void*)wkv, 0, D_ * 16 * 2, 0x00020000);
    const rsrc_t rx = __builtin_amdgcn_make_buffer_rsrc((void*)x, 0, S_ * D_ * 2, 0x00020000);
#pragma unroll
    for (int i = 0; i < 2; ++i) dma(rk, l16, (2 * w + i) * 1024, lds0 + QL_KV + (2 * w + i) * 1024);
    const unsigned vx = (lane >> 5) * 1024 + (((lane & 31) ^ (lane >> 5)) << 4);
#pragma unroll
    for (int i = 0; i < 8; ++i) dma(rx, vx ^ (i << 5), mt * 16384 + i * 2048 + hf * 512, lds0 + QL_P + w * 8192 + i * 1024);
    const unsigned v0 = (lane >> 3) * 1024 + (((lane & 7) ^ ((lane >> 3) & 2)) << 4), v1 = v0 ^ 64;
#pragma unroll
    for (int i = 0; i < 8; ++i) dma(rw, (i & 1) ? v1 : v0, (8 * w + i) * 8192 + cb * 128, lds0 + (8 * w + i) * 1024);
    wait_vm<0>();
#pragma unroll
    for (int j = 0; j < 8; ++j) b[j] = lds_frag(mine, mm * 512 + (((4 * j + g) ^ mm) << 4));
  } else {
#pragma unroll
    for (int i = 0; i < 2; ++i) {
      const int c = 48 + 2 * w + i;
      dma(rs, l16, OFF_WQKV + ((4 * cb + (c & 3)) * 16 + (c >> 2)) * KB, lds0 + c * 1024);
      dma(rs, l16, OFF_WQKV + ((32 + (cb & 1)) * 16 + 2 * w + i) * KB, lds0 + QL_KV + (2 * w + i) * 1024);
    }
    cs0 = bld4f(rs, g * 16, OFF_CSUM + (4 * cb + 2 * hf) * 64); cs1 = bld4f(rs, g * 16 + 64, OFF_CSUM + (4 * cb + 2 * hf) * 64);
    csk = bld4f(rs, g * 16, OFF_CSUM + 32 * 64);
    csv = __uint_as_float(bld1(rs, mm * 4, OFF_CSUM + 33 * 64));
    wave_wait(ws, flag_off(0, 2, (xcd * 4 + rb) * 2 + hf), (lane & 3) * 4);
#pragma unroll
    for (int j = 0; j < 8; ++j) b[j] = bld<SC1>(rs, l16, OFF_X2 + (mt * 16 + 8 * hf + j) * KB);
  }
  raw_barrier();
  STAMP(FIRST ? 5 : 0, 1);
  hook1();
  const int q4 = (lane >> 2) & 3, p4 = lane & 3;
  const int fsw = ((q4 >> 1) & 1) | ((g & 1) << 1);
  const int abase = FIRST ? hf * 32768 + (8 * g + q4) * 128 + p4 * 8 : hf * 32768 + l16;
  auto aq = [&](int t  ) { return FIRST ? abase + ((t ^ fsw) << 5) : abase + t * 1024; };
  auto ldq = [&](int base, int j) { return FIRST ? tr_frag(lds, base + j * 4096, base + j * 4096 + 512) : lds_frag(lds, base + j * 4096); };
  const int kvb = QL_KV + hf * 8192 + (FIRST ? (8 * g + q4) * 32 + p4 * 8 : l16);
  auto ldkv = [&](int j) { return FIRST ? tr_frag(lds, kvb + j * 1024, kvb + j * 1024 + 128) : lds_frag(lds, kvb + j * 1024); };
  bf16x8 ones; for (int i = 0; i < 8; ++i) ones[i] = 0x3F80;
  if (FIRST) {
    const int cbs = aq(ml);
    f32x4 c0 = {}, c1 = {}, ck = {};
#pragma unroll
    for (int j = 0; j < 8; j += 2) { c0 = mfma16(ldq(cbs, j), ones, c0); c1 = mfma16(ldq(cbs, j + 1), ones, c1); }
    const int kv2 = kvb + ml * 2048;
#pragma unroll
    for (int j = 0; j < 2; ++j) ck = mfma16(FIRST ? tr_frag(lds, kv2 + j * 1024, kv2 + j * 1024 + 128) : lds_frag(lds, kv2 + j * 1024), ones, ck);
    if (mm == 0) { *(f32x4*)(mine + QP_CS + g * 16) = c0 + c1; *(f32x4*)(mine + QP_CK + g * 16) = ck; }
  }
  STAMP(FIRST ? 5 : 0, 2);
  f32x4 acc[4] = {}, accs = {}, accg = {};
  bf16x8 a[2][4];
  const int ab0 = aq(2 * hf), ab1 = aq(2 * hf + 1), ab2 = aq(2 - 2 * hf), ab3 = aq(3 - 2 * hf);
  auto lda = [&](int j) { a[j & 1][0] = ldq(ab0, j); a[j & 1][1] = ldq(ab1, j); a[j & 1][2] = ldq(ab2, j); a[j & 1][3] = ldq(ab3, j); };
  lda(0);
#pragma unroll
  for (int j = 0; j < 8; ++j) {
    if (j < 7) lda(j + 1);
    SB();
    accs = mfma16(ones, b[j], accs); accg = mfma16(b[j], b[j], accg);
#pragma unroll
    for (int t = 0; t < 4; ++t) acc[t] = mfma16(a[j & 1][t], b[j], acc[t]);
    SB();
  }
  f32x4 akv = {};
  if (ml == kvml) {
    if (cb & 1) {
#pragma unroll
      for (int j = 0; j < 8; ++j) akv = mfma16(b[j], ldkv(j), akv);
    } else {
#pragma unroll
      for (int j = 0; j < 8; ++j) akv = mfma16(ldkv(j), b[j], akv);
    }
    *(f32x4*)(mine + QP_KV + l16) = akv;
  }
  STAMP(FIRST ? 5 : 0, 9);
  *(f32x4*)(mine + QP_T + l16) = acc[2];
  *(f32x4*)(mine + QP_T + 1024 + l16) = acc[3];
  {
    const float s01 = (mm & 1) ? accg[1] : accg[0], s23 = (mm & 1) ? accg[3] : accg[2];
    if (g == (mm >> 2)) *(f32x2*)(mine + QP_ST + mm * 8) = (f32x2){accs[0], (mm & 2) ? s23 : s01};
  }
  hook2();
  raw_barrier();
  STAMP(FIRST ? 5 : 0, 10);
  float invd = 1.f / D_; PIN1(invd);
  auto stats = [&](int tokl, float& mean, float& rstd) {
    const f32x2 s = *(const f32x2*)(mine + QP_ST + tokl * 8) + *(const f32x2*)(other + QP_ST + tokl * 8);
    mean = s[0] * invd;
    rstd = __builtin_amdgcn_rsqf(fmaxf(s[1] * invd - mean * mean, 0.f) + LN_EPS);
  };
  float mean, rstd; stats(mm, mean, rstd);
  const char* csp = lds + QL_P + 2 * hf * 8192 + QP_CS + g * 16;
  if (FIRST) {
    cs0 = *(const f32x4*)(csp) + *(const f32x4*)(csp + 32768); cs1 = *(const f32x4*)(csp + 8192) + *(const f32x4*)(csp + 8192 + 32768);
    if (mt == 0) {
      bst4(__builtin_bit_cast(u32x4, cs0), rs, mm == 0 ? g * 16 : OOB, OFF_CSUM + (4 * cb + 2 * hf) * 64);
      bst4(__builtin_bit_cast(u32x4, cs1), rs, mm == 0 ? g * 16 + 64 : OOB, OFF_CSUM + (4 * cb + 2 * hf) * 64);
    }
  }
  const f32x4 s0 = acc[0] + *(const f32x4*)(other + QP_T + l16), s1 = acc[1] + *(const f32x4*)(other + QP_T + 1024 + l16);
  const float rq = rstd * QSCALE, nmq = -mean * rq;
  const f32x4 q0 = s0 * rq + cs0 * nmq, q1 = s1 * rq + cs1 * nmq;
  const u32x2 o0 = {pk2(q0[0], q0[1]), pk2(q0[2], q0[3])}, o1 = {pk2(q1[0], q1[1]), pk2(q1[2], q1[3])};
  {
    auto s0_ = __builtin_amdgcn_permlane16_swap(o0[0], o1[0], false, false);
    auto s1_ = __builtin_amdgcn_permlane16_swap(o0[1], o1[1], false, false);
    bst4<SC1>((u32x4){s0_[0], s1_[0], s0_[1], s1_[1]}, rs, (mm >> 3) * 8192 + ((g >> 1) * 32 + (2 * hf + (g & 1)) * 8 + (mm & 7)) * 16, qkv + QO_Q + mt * 16384 + cb * 1024);
  }
  float n0 = sq4(o0), n1 = sq4(o1);
  xrow_sum2(n0, n1);
  const unsigned qn = row16_umax(umax(__float_as_uint(n0), __float_as_uint(n1)));
  bst1<SC1>(qn, rs, lane == 0 ? 0u : OOB, qkv + QO_QN2 + mt * 64 + (cb * 2 + hf) * 4);
  if (hf == 0 && ml == kvml) {
    const f32x4 skv = akv + *(const f32x4*)(other + QP_KV + l16);
    if (cb & 1) {
      if (FIRST) {
        csv = 0.f;
#pragma unroll
        for (int i = 0; i < 8; ++i) csv += *(const float*)(lds + QL_P + i * 8192 + QP_CK + mm * 4);
        if (mt == 0) bst1(__float_as_uint(csv), rs, g == 0 ? mm * 4 : OOB, OFF_CSUM + 33 * 64);
      }
      float v[4];
#pragma unroll
      for (int i = 0; i < 4; ++i) { float mn, rsd; stats(4 * g + i, mn, rsd); v[i] = (skv[i] - mn * csv) * rsd; }
      const u32x2 o = {pk2(v[0], v[1]), pk2(v[2], v[3])};
      bst2<SC1>(o, rs, (g & 1) * 256 + mm * 16 + (g >> 1) * 8, qkv + QO_VT + mt * 512);
    } else {
      if (FIRST) {
        csk = (f32x4){};
#pragma unroll
        for (int i = 0; i < 8; ++i) csk += *(const f32x4*)(lds + QL_P + i * 8192 + QP_CK + g * 16);
        if (mt == 0) bst4(__builtin_bit_cast(u32x4, csk), rs, mm == 0 ? g * 16 : OOB, OFF_CSUM + 32 * 64);
      }
      const f32x4 k = (skv - csk * mean) * rstd;
      const u32x2 o = {pk2(k[0], k[1]), pk2(k[2], k[3])};
      bst2<SC1>(o, rs, mm * 32 + g * 8, qkv + QO_K + mt * 512);
      const unsigned n2 = row16_umax(__float_as_uint(xrow_sum(sq4(o))));
      bst1<SC1>(n2, rs, lane == 0 ? 0u : OOB, qkv + QO_KN2 + mt * 4);
    }
  }
  STAMP(FIRST ? 5 : 0, 11);
  if (!FIRST) wg_signal(ws, flag_off(0, 3, (xcd * 4 + rb) * 2 + (cb >> 2)) + (cb & 3) * 4, tid, [&]() {});
}

__global__ __launch_bounds__(512, 2) void k_first(const u16* __restrict__ x, const u16* __restrict__ wq, const u16* __restrict__ wk, const u16* __restrict__ wv,
                                                  const u16* __restrict__ wo, const u16* __restrict__ w1, const u16* __restrict__ w2, char* __restrict__ ws) {
  extern __shared__ __attribute__((aligned(16))) char lds[];
  const int tid = threadIdx.x, lane = tid & 63, g = lane >> 4;
  const int w = __builtin_amdgcn_readfirstlane(tid >> 6);
  const rsrc_t rs = __builtin_amdgcn_make_buffer_rsrc((void*)ws, 0, WS_NEED, 0x00020000);
  const unsigned lds0 = (unsigned)(uintptr_t)lds, l16 = lane * 16;
  const int xcd = blockIdx.x & 7, idx = blockIdx.x >> 3, cb = idx & 7;
  const u16* tw; int tpitch, tks, tnb; unsigned tdst; int tkst;
  {
    const int bi = blockIdx.x * 4 + (w & 3);
    if (bi < 128) { tw = wo; tpitch = 1024; tks = bi >> 3; tnb = bi & 7; tdst = OFF_WO; tkst = 16; }
    else if (bi < 576) { const int cw = (bi - 128) >> 2, kq = cw / 28; tw = w1; tpitch = 3584; tks = 4 * kq + (w & 3); tnb = cw - 28 * kq; tdst = OFF_W1; tkst = 16; }
    else { const int c = bi - 576; tw = w2; tpitch = 1024; tks = c >> 3; tnb = c & 7; tdst = OFF_W2; tkst = 56; }
  }
  auto fetch = [&]() {
    if (w < 4) {
      const rsrc_t rt = __builtin_amdgcn_make_buffer_rsrc((void*)tw, 0, D_ * DFF * 2, 0x00020000);
      const unsigned v = (lane >> 3) * tpitch + (lane & 7) * 16;
#pragma unroll
      for (int i = 0; i < 4; ++i) dma(rt, v, (32 * tks + 8 * i) * tpitch + tnb * 128, lds0 + QL_TR + w * 4096 + i * 1024);
    }
  };
#define CVT_AUX 16
  auto convert = [&]() {
    const int q4 = (lane >> 2) & 3, p4 = lane & 3;
    if (w < 4) {
      wait_vm<0>();
      const int base = QL_TR + w * 4096 + (4 * g + q4) * 128 + p4 * 8;
      bf16x8 ones; for (int i = 0; i < 8; ++i) ones[i] = 0x3F80;
#pragma unroll
      for (int t = 0; t < 4; ++t) {
        const bf16x8 f = tr_frag(lds, base + t * 32, base + t * 32 + 2048);
        bst4<CVT_AUX>(__builtin_bit_cast(u32x4, f), rs, l16, tdst + ((4 * tnb + t) * tkst + tks) * KB);
        const f32x4 z = {};
        const f32x4 c = mfma16(f, ones, z);
        if ((lane & 15) == 0) *(f32x4*)(lds + QL_P + w * 8192 + 4096 + t * 64 + g * 16) = c;
      }
    } else if (w < 6) {
      const int c = (xcd * 4 + (idx >> 3)) * 2 + (w & 1), ks = c >> 2, t = c & 3;
      const int r0 = 4 * g + q4, f0 = ((r0 >> 1) & 1) | (((r0 >> 3) & 1) << 1);
      const int base = (32 * ks + r0) * 128 + ((t ^ f0) << 5) + p4 * 8;
      bst4<CVT_AUX>(__builtin_bit_cast(u32x4, tr_frag(lds, base, base + 2048)), rs, l16, OFF_WQKV + ((4 * cb + t) * 16 + ks) * KB);
    } else if (w == 6) {
      const int ks = xcd * 4 + (idx >> 3);
      if (cb < 2 && ks < 16) {
        const int base = QL_KV + (32 * ks + 4 * g + q4) * 32 + p4 * 8;
        bst4<CVT_AUX>(__builtin_bit_cast(u32x4, tr_frag(lds, base, base + 512)), rs, l16, OFF_WQKV + ((32 + cb) * 16 + ks) * KB);
      }
    } else {
      bst4<CVT_AUX>((u32x4){0u, 0u, 0u, 0u}, rs, lane < 32 ? l16 : OOB, OFF_FLAG + blockIdx.x * 512);
    }
  };
  qkv_phase<true>(lds, ws, rs, tid, x, wq, (cb & 1) ? wv : wk, fetch, convert);
  if (w == 7 && tdst == OFF_W1) {
    const float* c = (const float*)(lds + QL_P + 4096) + lane;
    bst1(__float_as_uint((c[0] + c[2048]) + (c[4096] + c[6144])), rs, lane * 4, OFF_CS1 + (tks >> 2) * 7168 + tnb * 256);
  }
}

DI int mytid(int w64) { int t; asm volatile("v_mbcnt_lo_u32_b32 %0, -1, %1\n\tv_mbcnt_hi_u32_b32 %0, -1, %0" : "=v"(t) : "s"(w64)); return t; }
__global__ __launch_bounds__(512, 2) void k_main(char* __restrict__ ws, u16* __restrict__ out, const u16* __restrict__ xin) {
  extern __shared__ __attribute__((aligned(16))) char lds[];
  const int w64 = __builtin_amdgcn_readfirstlane(threadIdx.x & ~63);
  const rsrc_t rs = __builtin_amdgcn_make_buffer_rsrc((void*)ws, 0, WS_NEED, 0x00020000);
#pragma unroll 1
  for (int layer = 0; layer < 2; ++layer) {
    bf16x8 stg[28];
    attn_phase(lds, ws, rs, xin, layer, mytid(w64), stg);
    ffn1_phase(lds, ws, rs, layer, mytid(w64), stg);
    ffn2_phase(lds, ws, rs, out, layer, mytid(w64), stg);
    if (layer == 0) qkv_phase<false>(lds, ws, rs, mytid(w64), nullptr, nullptr, nullptr, []() {}, []() {});
  }
}

extern "C" void kernel_launch(void* const* d_in, const int* in_sizes, int n_in, void* d_out, int out_size, void* d_ws, size_t ws_size,
                              hipStream_t stream) {
  const u16* x = (const u16*)d_in[0];
  const u16* wq = (const u16*)d_in[1];
  const u16* wk = (const u16*)d_in[2];
  const u16* wv = (const u16*)d_in[3];
  const u16* wo = (const u16*)d_in[4];
  const u16* w1 = (const u16*)d_in[6];
  const u16* w2 = (const u16*)d_in[7];
  char* ws = (char*)d_ws;
  u16* out = (u16*)d_out;
  (void)hipFuncSetAttribute((const void*)k_main, hipFuncAttributeMaxDynamicSharedMemorySize, LDS_BYTES);
  (void)hipFuncSetAttribute((const void*)k_first, hipFuncAttributeMaxDynamicSharedMemorySize, LDS_BYTES);
  k_first<<<256, 512, LDS_BYTES, stream>>>(x, wq, wk, wv, wo, w1, w2, ws);
  k_main<<<256, 512, LDS_BYTES, stream>>>(ws, out, x);
}
```

```cpp
#include <hip/hip_runtime.h>
#include <hip/hip_bf16.h>
#include <stdint.h>
#include <type_traits>

typedef __attribute__((ext_vector_type(8))) short bf16x8;
typedef __attribute__((ext_vector_type(2))) float f32x2;
typedef __attribute__((ext_vector_type(4))) float f32x4;
typedef __attribute__((ext_vector_type(16))) float f32x16;
typedef __attribute__((ext_vector_type(4))) unsigned u32x4;
typedef __attribute__((ext_vector_type(2))) unsigned u32x2;
typedef unsigned short u16;
typedef __amdgpu_buffer_rsrc_t rsrc_t;
typedef __attribute__((address_space(1))) unsigned gu32;
#define DI __device__ __forceinline__
#define LAS __attribute__((address_space(3)))

constexpr int S_ = 2048, D_ = 512, DFF = 1792;
constexpr float QSCALE = 0.25f * 1.4426950408889634f;
constexpr float LN_EPS = 1e-5f;

constexpr unsigned KB = 1024;
constexpr unsigned OFF_FLAG = 0;
constexpr unsigned FLAG_BYTES = 128 * KB;
constexpr unsigned OFF_WQKV = OFF_FLAG + FLAG_BYTES;
constexpr unsigned OFF_WO   = OFF_WQKV + 544 * KB;
constexpr unsigned OFF_W1   = OFF_WO + 512 * KB;
constexpr unsigned OFF_W2   = OFF_W1 + 1792 * KB;
constexpr unsigned OFF_CSUM = OFF_W2 + 1792 * KB;
constexpr unsigned OFF_CS1  = OFF_CSUM + 4 * KB;
constexpr unsigned OFF_X2   = OFF_CS1 + 28 * KB;
constexpr unsigned OFF_QKV  = OFF_X2 + 2048 * KB;
constexpr unsigned QO_X1 = 0;
constexpr unsigned QO_ST = QO_X1 + 2048 * KB;
constexpr unsigned QO_H  = QO_ST + 128 * KB;
constexpr unsigned QO_Q  = QO_H + 7168 * KB;
constexpr unsigned QO_K = QO_Q + 2048 * KB;
constexpr unsigned QO_VT = QO_K + 64 * KB;
constexpr unsigned QO_KN2 = QO_VT + 64 * KB;
constexpr unsigned QO_QN2 = QO_KN2 + 1 * KB;
constexpr unsigned QKV_BYTES = QO_QN2 + 8 * KB;
constexpr unsigned WS_NEED  = OFF_QKV + 2 * QKV_BYTES;
DI unsigned flag_off(int layer, int edge, int line) { return OFF_FLAG + ((layer * 4 + edge) * 128 + line) * 128; }

DI float bf2f(u16 v) { return __uint_as_float(((unsigned)v) << 16); }
DI float bflo(unsigned v) { return __uint_as_float(v << 16); }
DI float bfhi(unsigned v) { return __uint_as_float(v & 0xffff0000u); }
DI unsigned pk2(float lo, float hi) {
  typedef __attribute__((ext_vector_type(2))) __bf16 bf16x2_;
  f32x2 v = {lo, hi};
  bf16x2_ r = __builtin_convertvector(v, bf16x2_);
  return __builtin_bit_cast(unsigned, r);
}
DI unsigned relu_pk(unsigned v) {
  typedef __attribute__((ext_vector_type(2))) short s16x2;
  const s16x2 z = {0, 0};
  return __builtin_bit_cast(unsigned, __builtin_elementwise_max(__builtin_bit_cast(s16x2, v), z));
}
DI f32x4 mfma16(bf16x8 a, bf16x8 b, f32x4 c) {
  typedef __attribute__((ext_vector_type(8))) __bf16 b8;
  return __builtin_amdgcn_mfma_f32_16x16x32_bf16(__builtin_bit_cast(b8, a), __builtin_bit_cast(b8, b), c, 0, 0, 0);
}
DI f32x16 mfma32(bf16x8 a, bf16x8 b, f32x16 c) {
  typedef __attribute__((ext_vector_type(8))) __bf16 b8;
  return __builtin_amdgcn_mfma_f32_32x32x16_bf16(__builtin_bit_cast(b8, a), __builtin_bit_cast(b8, b), c, 0, 0, 0);
}
template <int N> DI void wait_vm() { asm volatile("s_waitcnt vmcnt(%0)" :: "i"(N) : "memory"); }
DI void raw_barrier() { asm volatile("s_waitcnt lgkmcnt(0)\n\ts_barrier" ::: "memory"); }
DI bf16x8 lds_frag(const char* lds, int off) { return *(const LAS bf16x8*)((const LAS char*)lds + off); }
#define STAMP(kid, i)
DI int kperm(int g, int j) { return (j < 4) ? 4 * g + j : 16 + 4 * g + (j - 4); }

template <int CTRL> DI float dpp(float x) { return __builtin_bit_cast(float, __builtin_amdgcn_mov_dpp(__builtin_bit_cast(int, x), CTRL, 0xf, 0xf, true)); }
template <int CTRL> DI unsigned dppu(unsigned x) { return (unsigned)__builtin_amdgcn_mov_dpp((int)x, CTRL, 0xf, 0xf, true); }
DI unsigned umax(unsigned a, unsigned b) { return a > b ? a : b; }
DI unsigned row16_umax(unsigned x) { x = umax(x, dppu<0xB1>(x)); x = umax(x, dppu<0x4E>(x)); x = umax(x, dppu<0x141>(x)); x = umax(x, dppu<0x140>(x)); return x; }
DI unsigned xrow_umax(unsigned x) {
  auto s = __builtin_amdgcn_permlane16_swap(x, x, false, false); x = umax(s[0], s[1]);
  auto t = __builtin_amdgcn_permlane32_swap(x, x, false, false); return umax(t[0], t[1]); }
DI float xrow_sum(float x) {
  auto s = __builtin_amdgcn_permlane16_swap(__float_as_uint(x), __float_as_uint(x), false, false);
  x = __uint_as_float(s[0]) + __uint_as_float(s[1]);
  auto t = __builtin_amdgcn_permlane32_swap(__float_as_uint(x), __float_as_uint(x), false, false);
  return __uint_as_float(t[0]) + __uint_as_float(t[1]); }
DI void xrow_sum2(float& a, float& b) {
  auto s = __builtin_amdgcn_permlane16_swap(__float_as_uint(a), __float_as_uint(b), false, false);
  const float x = __uint_as_float(s[0]) + __uint_as_float(s[1]);
  auto t = __builtin_amdgcn_permlane32_swap(__float_as_uint(x), __float_as_uint(x), false, false);
  const float y = __uint_as_float(t[0]) + __uint_as_float(t[1]);
  auto u = __builtin_amdgcn_permlane16_swap(__float_as_uint(y), __float_as_uint(y), false, false);
  a = __uint_as_float(u[0]); b = __uint_as_float(u[1]); }
DI float sq4(u32x2 o) { return bflo(o[0]) * bflo(o[0]) + bfhi(o[0]) * bfhi(o[0]) + bflo(o[1]) * bflo(o[1]) + bfhi(o[1]) * bfhi(o[1]); }
#define SB() __builtin_amdgcn_sched_barrier(0)
#define EX(x) __builtin_amdgcn_exp2f(x)
#define PIN1(a) asm volatile("" : "+v"(a))
#define PIN2(a, b) asm volatile("" : "+v"(a), "+v"(b))
#define PIN4(a, b, c, d) asm volatile("" : "+v"(a), "+v"(b), "+v"(c), "+v"(d))
constexpr int SC1 = 16;
DI void mfma16_ip(f32x4& acc, bf16x8 a, bf16x8 b) { asm volatile("v_mfma_f32_16x16x32_bf16 %0, %1, %2, %0" : "+v"(acc) : "v"(a), "v"(b)); }
DI void bld_asm(bf16x8& d, rsrc_t rs, unsigned voff, unsigned soff) { asm volatile("buffer_load_dwordx4 %0, %1, %2, %3 offen" : "+v"(d) : "v"(voff), "s"(rs), "s"(soff)); }
template <int N> DI void mfma16_w(f32x4& acc, bf16x8& a, bf16x8 b) { asm volatile("s_waitcnt vmcnt(%3)\n\tv_mfma_f32_16x16x32_bf16 %0, %1, %2, %0" : "+v"(acc), "+v"(a) : "v"(b), "i"(N)); }
template <int AUX = 0> DI bf16x8 bld(rsrc_t rs, unsigned voff, unsigned soff) { return __builtin_bit_cast(bf16x8, __builtin_amdgcn_raw_buffer_load_b128(rs, voff, soff, AUX)); }
template <int AUX = 0> DI u32x4 bld4(rsrc_t rs, unsigned voff, unsigned soff) { return __builtin_amdgcn_raw_buffer_load_b128(rs, voff, soff, AUX); }
template <int AUX = 0> DI f32x4 bld4f(rsrc_t rs, unsigned voff, unsigned soff) { return __builtin_bit_cast(f32x4, __builtin_amdgcn_raw_buffer_load_b128(rs, voff, soff, AUX)); }
template <int AUX = 0> DI unsigned bld1(rsrc_t rs, unsigned voff, unsigned soff) { return __builtin_amdgcn_raw_buffer_load_b32(rs, voff, soff, AUX); }
template <int AUX = 0> DI void bst4(u32x4 v, rsrc_t rs, unsigned voff, unsigned soff) {
  __builtin_amdgcn_raw_buffer_store_b128(v, rs, voff, soff, AUX);
  asm volatile("s_nop 1" :: "v"(v) : "memory");
}
template <int AUX = 0> DI void bst2(u32x2 v, rsrc_t rs, unsigned voff, unsigned soff) { __builtin_amdgcn_raw_buffer_store_b64(v, rs, voff, soff, AUX); }
template <int AUX = 0> DI void bst1(unsigned v, rsrc_t rs, unsigned voff, unsigned soff) { __builtin_amdgcn_raw_buffer_store_b32(v, rs, voff, soff, AUX); }
constexpr unsigned OOB = 0x7ffffff0u;
DI void dma(rsrc_t rs, unsigned voff, unsigned soff, unsigned lds_dst) {
  unsigned sv;
  asm volatile("s_mov_b32 %0, m0\n\ts_mov_b32 m0, %1\n\ts_nop 0\n\tbuffer_load_dwordx4 %2, %3, %4 offen lds\n\ts_mov_b32 m0, %0"
               : "=&s"(sv) : "s"(lds_dst), "v"(voff), "s"(rs), "s"(soff) : "memory");
}
DI void pull(rsrc_t rs, unsigned voff, unsigned soff, unsigned lds_junk) {
  unsigned sv;
  asm volatile("s_mov_b32 %0, m0\n\ts_mov_b32 m0, %1\n\ts_nop 0\n\tbuffer_load_dword %2, %3, %4 offen sc1 lds\n\ts_mov_b32 m0, %0"
               : "=&s"(sv) : "s"(lds_junk), "v"(voff), "s"(rs), "s"(soff) : "memory");
}
template <class F>
DI void wg_signal(char* ws, unsigned foff, int tid, F after_drain) {
  asm volatile("s_waitcnt vmcnt(0)" ::: "memory");
  after_drain();
  raw_barrier();
  if (tid == 0) __hip_atomic_store((gu32*)(ws + foff), 1u, __ATOMIC_RELAXED, __HIP_MEMORY_SCOPE_AGENT);
}
DI void wave_wait(char* ws, unsigned foff, unsigned lane_off) {
  gu32* f = (gu32*)(ws + foff + lane_off);
#pragma unroll 1
  for (unsigned spins = 0; spins < (1u << 22); ++spins) {
    const unsigned v = __hip_atomic_load(f, __ATOMIC_RELAXED, __HIP_MEMORY_SCOPE_AGENT);
    if (__all(v != 0u)) break;
#define POLL_SLEEP 1
    __builtin_amdgcn_s_sleep(POLL_SLEEP);
  }
  asm volatile("" ::: "memory");
}

DI int mybid() { int b = blockIdx.x; asm volatile("" : "+s"(b)); return b; }

constexpr int LDS_BYTES = 163840;
constexpr int AL_RO = 0;
constexpr int AL_RL = AL_RO + 32768;
constexpr int AL_O  = AL_RL + 2048;
constexpr int AL_Q  = AL_O + 4096;
constexpr int AL_FL = AL_Q + 8192;
constexpr int AL_BND = AL_FL + 256;
constexpr int AL_CS = AL_BND + 512;
constexpr int AL_JUNK = AL_Q;
constexpr int AL_W1 = 49152;
constexpr int AL_MX = AL_W1;
static_assert(AL_CS + 512 <= AL_W1 && AL_W1 + 112 * 1024 <= LDS_BYTES, "lds");
constexpr float SHIFT = 32.f;

DI void dma_w1(rsrc_t rs, unsigned lds0, unsigned l16, int nt0, int w, int t, int kk) {
  dma(rs, l16, OFF_W1 + ((nt0 + t) * 16 + 2 * w + kk) * KB, lds0 + AL_W1 + ((2 * w + kk) * 7 + t) * 1024);
}

DI void ffn2_prefetch(rsrc_t rs, unsigned l16, int nt0, int w, bf16x8 (&stg)[28], int i) {
  const int ti = i & 3;
  stg[i] = bld(rs, l16, OFF_W2 + ((nt0 + ((ti & 2) ^ (2 * (w & 1))) + (ti & 1)) * 56 + 7 * w + (i >> 2)) * KB);
}

constexpr int PRE_ATT = 8;
DI void attn_phase(char* lds, char* ws, rsrc_t rs, const u16* xin, int layer, int tid, bf16x8 (&stg)[28]) {
  const int lane = tid & 63, r = lane & 31, h = lane >> 5;
  const int w = __builtin_amdgcn_readfirstlane(tid >> 6);
  const int bid = mybid(), xcd = bid & 7, idx = bid >> 3;
  const int t0 = xcd * 256 + idx * 8;
  const unsigned lds0 = (unsigned)(uintptr_t)lds;
  const unsigned qkv = OFF_QKV + layer * QKV_BYTES;
  const unsigned l16 = lane * 16;
  const int f1_nt0 = (idx & 15) * 7;
  STAMP(layer ? 1 : 6, 0);
  if (layer == 1) wave_wait(ws, flag_off(0, 3, 0), (h == 0 ? w * 8 + (lane >> 2) : (xcd * 4 + (idx >> 3)) * 2 + ((lane >> 2) & 1)) * 128 + (lane & 3) * 4);
  STAMP(layer ? 1 : 6, 3);
  const unsigned qs = qkv + QO_Q + t0 * 1024;
  bf16x8 qf = bld<SC1>(rs, l16, qs);
  SB();
  bf16x8 kf[8], vf[16], qn, qw;
  unsigned kn_a, qn_a, c1p;
  const unsigned kb = qkv + QO_K + w * 8192, vb = qkv + QO_VT + w * 8192;
  const unsigned ko = r * 32 + 16 * h, vo = (r >> 4) * 512 + h * 256 + (r & 15) * 16;
  auto load_kv = [&](int t) {
    if (t < 7) kf[t + 1] = bld<SC1>(rs, ko + ((t + 1) & 3) * 1024, kb + ((t + 1) >> 2) * 4096);
    else qn = bld<SC1>(rs, l16 + 1024, qs);
    vf[2 * t] = bld<SC1>(rs, vo + (t & 3) * 1024, vb + (t >> 2) * 4096);
    asm volatile("" ::: "memory");
    SB();
  };
#define KV_PD 3
  kf[0] = bld<SC1>(rs, ko, kb);
  SB();
#pragma unroll
  for (int t = 0; t < KV_PD; ++t) load_kv(t);
  { unsigned z = 0u; PIN1(z); *(u32x2*)(lds + AL_O + tid * 8) = (u32x2){z, z}; }
  ((unsigned*)(lds + AL_FL))[lane] = h;
  const unsigned vconst = (lane & 15) == 0 ? 0x3F803F80u : 0u;
  auto vsplit = [&](int t) {
    u32x4 x = __builtin_bit_cast(u32x4, vf[2 * t]), y;
#pragma unroll
    for (int i = 0; i < 4; ++i) { auto sw = __builtin_amdgcn_permlane16_swap(x[i], vconst, false, false); x[i] = sw[0]; y[i] = sw[1]; }
    vf[2 * t] = __builtin_bit_cast(bf16x8, x); vf[2 * t + 1] = __builtin_bit_cast(bf16x8, y);
  };
  const unsigned wos = OFF_WO + (4 * w * 16) * KB;
  f32x4 oacc[4] = {};
  f32x16 o;
  char* const ro_w = lds + AL_RO + w * 2048 + lane * 8;
  const int lf_w0 = h == 0 ? AL_RL + (r * 8 + w) * 4 : AL_FL + (w * 4 + (r & 3)) * 4, lf_step = h == 0 ? 1024 : 0;
  const int o_wr = AL_O + (r >> 4) * 1024 + ((2 * ((w & 3) >> 1) + h) * 16 + (w >> 2) * 8 + (r & 7)) * 16 + ((r >> 3) & 1) * 8 + (w & 1) * 4;
  auto publish = [&](int g) {
    char* ro = ro_w + (g & 1) * 16384;
    *(f32x2*)(ro) = (f32x2){o[0], o[1]}; *(f32x2*)(ro + 512) = (f32x2){o[2], o[3]};
    *(f32x2*)(ro + 1024) = (f32x2){o[4], o[5]}; *(f32x2*)(ro + 1536) = (f32x2){o[6], o[7]};
    asm volatile("" ::: "memory");
    *(volatile LAS unsigned*)((LAS char*)lds + lf_w0 + (g & 1) * lf_step) = h == 0 ? __float_as_uint(o[8]) : (unsigned)(g + 1);
  };
  f32x2 ra[4]; f32x4 rl0, rl1;
  const char* const red_r = lds + AL_RO + (w >> 2) * 8192 + (w & 3) * 512 + lane * 8;
  auto reduce_ld = [&](int g) {
#pragma unroll
    for (int i = 0; i < 4; ++i) ra[i] = *(const f32x2*)(red_r + (g & 1) * 16384 + i * 2048);
    rl0 = *(const f32x4*)(lds + AL_RL + (g & 1) * 1024 + r * 32); rl1 = *(const f32x4*)(lds + AL_RL + (g & 1) * 1024 + r * 32 + 16);
  };
  auto reduce_fin = [&](int g) {
    const f32x4 l4 = rl0 + rl1;
    const f32x2 l2 = (f32x2){l4[0], l4[1]} + (f32x2){l4[2], l4[3]};
    const float inv = __builtin_amdgcn_rcpf(l2[0] + l2[1]);
    const f32x2 sa = (ra[0] + ra[1]) + (ra[2] + ra[3]);
    *(unsigned*)(lds + o_wr + (g & 1) * 2048) = pk2(sa[0] * inv, sa[1] * inv);
    asm volatile("" ::: "memory");
    *(volatile LAS unsigned*)((LAS char*)lds + AL_FL + 128 + w * 16 + (lane & 3) * 4) = (unsigned)(g + 2);
  };
  auto reduce = [&](int g) { reduce_ld(g); reduce_fin(g); };
  const int g4 = lane >> 4, mm = lane & 15;
  const unsigned fs = ((t0 >> 4) * 16 + 2 * w) * KB, fv = (mm >> 3) * 1024 + (g4 * 16 + (t0 & 15) + (mm & 7)) * 16;
  u32x4 xr;
  auto load_res = [&]() {
    if (layer == 0) {
      const u16* xp = xin + (size_t)(t0 + (mm & 7)) * D_ + 64 * w + 32 * (mm >> 3) + 4 * g4;
      const u32x2 lo = *(const u32x2*)xp, hi = *(const u32x2*)(xp + 16);
      xr = (u32x4){lo[0], lo[1], hi[0], hi[1]};
    } else xr = bld4<SC1>(rs, fv, OFF_X2 + fs);
  };
  f32x16 cinit;
  { float ns = -SHIFT; PIN1(ns);
#pragma unroll
    for (int i = 0; i < 16; ++i) cinit[i] = ns; }
  PIN1(cinit);
  bf16x8 wof[4] = {qf, qf, qf, qf}, of;
  u32x4 p0, p1;
  unsigned flg;
  const unsigned fl_addr = lds0 + AL_FL + lane * 4;
  STAMP(layer ? 1 : 6, 1);
  f32x16 sc = mfma32(kf[0], qf, cinit);
  bf16x8 wl[16];
  auto group = [&](auto mode_tag, const int g) {
    constexpr bool FIRST = decltype(mode_tag)::value == 0, LAST = decltype(mode_tag)::value == 2;
#pragma unroll
    for (int t = 0; t < 8; ++t) {
      if (t == 3) __builtin_amdgcn_s_setprio(3); if (t == 5) __builtin_amdgcn_s_setprio(2); if (t == 7) __builtin_amdgcn_s_setprio(1); if (t == 1) __builtin_amdgcn_s_setprio(0);
      f32x16 sn;
      if (!(LAST && t == 7)) sn = mfma32(kf[(t + 1) & 7], t < 7 ? qf : qn, cinit);
      SB();
      if (FIRST) {
        if (t + KV_PD < 8) load_kv(t + KV_PD);
        if (t == 8 - KV_PD || (KV_PD == 8 && t == 0)) {
          qw = bld<SC1>(rs, l16, qs + w * 1024);
          kn_a = bld1<SC1>(rs, (lane & 15) * 4, qkv + QO_KN2 + w * 64);
          qn_a = bld1<SC1>(rs, (lane & 15) * 4, qkv + QO_QN2 + (t0 >> 4) * 64);
          c1p = bld1(rs, (lane >> 4) * 7168 + (lane & 15) * 4, OFF_CS1 + (f1_nt0 + (w < 7 ? w : 6)) * 64);
          asm volatile("" ::: "memory");
          SB();
        }
        vsplit(t); SB();
      }
      float e0 = EX(sc[0]), e1 = EX(sc[1]), e2 = EX(sc[2]), e3 = EX(sc[3]), e4 = EX(sc[4]), e5 = EX(sc[5]);
      PIN4(e0, e1, e2, e3); PIN2(e4, e5); SB();
      if (t > 0 || !FIRST) o = mfma32(vf[(2 * t + 15) & 15], __builtin_bit_cast(bf16x8, p1), o);
      SB();
      float e6 = EX(sc[6]), e7 = EX(sc[7]);
      PIN2(e6, e7); SB();
      p0[0] = pk2(e0, e1); p0[1] = pk2(e2, e3); p0[2] = pk2(e4, e5); p0[3] = pk2(e6, e7);
      PIN1(p0); SB();
      float e8 = EX(sc[8]), e9 = EX(sc[9]);
      PIN2(e8, e9); SB();
      float e10, e11, e12, e13;
      if (t == 0) {
        if (!FIRST) { e10 = EX(sc[10]); e11 = EX(sc[11]); e12 = EX(sc[12]); e13 = EX(sc[13]); PIN4(e10, e11, e12, e13); PIN1(o); SB(); publish(g - 1); SB(); }
        f32x16 z = {}; o = mfma32(vf[0], __builtin_bit_cast(bf16x8, p0), z);
      } else o = mfma32(vf[2 * t], __builtin_bit_cast(bf16x8, p0), o);
      SB();
      if (t > 0 || FIRST) { e10 = EX(sc[10]); e11 = EX(sc[11]); e12 = EX(sc[12]); e13 = EX(sc[13]); PIN4(e10, e11, e12, e13); SB(); }
      if (!FIRST) {
        constexpr int X = LAST ? 3 : 0;
        if (t == 0) mfma16_w<3>(oacc[0], wof[0], of); else if (t == 1) mfma16_w<4 + X>(oacc[1], wof[1], of);
        else if (t == 2) mfma16_w<5 + 2 * X>(oacc[2], wof[2], of); else if (t == 3) mfma16_w<6 + 3 * X>(oacc[3], wof[3], of);
        else if (t == 4) mfma16_w<6 + 4 * X>(oacc[0], wof[0], of); else if (t == 5) mfma16_w<5 + 4 * X>(oacc[1], wof[1], of);
        else if (t == 6) mfma16_w<4 + 4 * X>(oacc[2], wof[2], of); else mfma16_w<3 + 4 * X>(oacc[3], wof[3], of);
        SB();
      }
      float e14 = EX(sc[14]), e15 = EX(sc[15]);
      PIN2(e14, e15); SB();
      p1[0] = pk2(e8, e9); p1[1] = pk2(e10, e11); p1[2] = pk2(e12, e13); p1[3] = pk2(e14, e15);
      PIN1(p1); SB();
      if (!FIRST || t >= 4) {
        const int gw = FIRST ? 0 : (g > 2 ? g - 2 : 0);
        bld_asm(wof[t & 3], rs, l16, wos + ((t & 3) * 16 + 2 * gw + (t >> 2)) * 1024);
        SB();
      }
      if (LAST) {
#pragma unroll
        for (int i = 2 * t; i < 2 * t + 2; ++i) { wl[i] = bld(rs, l16, wos + ((i & 3) * 16 + 12 + (i >> 2)) * 1024); asm volatile("" ::: "memory"); }
        ffn2_prefetch(rs, l16, (idx & 7) * 4, w, stg, t); asm volatile("" ::: "memory");
        SB();
      }
      if (!FIRST && (t == 1 || t == 2)) { dma_w1(rs, lds0, l16, f1_nt0, w, g - 1, t - 1); SB(); }
      if (!FIRST && t == 0) { dma(rs, l16, OFF_W2 + ((idx * 7 + g - 1) * 8 + w) * KB, lds0 + AL_JUNK); SB(); }
      if (!FIRST && t == 1) { flg = *(volatile LAS unsigned*)((LAS char*)lds + AL_FL + lane * 4); SB(); }
      if (!FIRST && t == 2) {
        asm volatile("1:\n\tv_cmp_gt_u32 vcc, %2, %0\n\ts_cbranch_vccz 2f\n\ts_sleep 1\n\tds_read_b32 %0, %1\n\ts_waitcnt lgkmcnt(0)\n\ts_branch 1b\n2:"
                     : "+v"(flg) : "v"(fl_addr), "s"(g) : "vcc", "memory");
        reduce_ld(g - 1);
        SB();
      }
      if (!FIRST && t == 3) {
        reduce_fin(g - 1);
        SB();
      }
      if (!FIRST && (t & 3) == 3) { of = lds_frag(lds, AL_O + (g & 1) * 2048 + (t >> 2) * 1024 + l16); SB(); }
      if (!FIRST && !LAST && t == 5) { qn = lds_frag(lds, AL_Q + (g + 1) * 1024 + l16); SB(); }
      if (!(LAST && t == 7)) sc = sn;
    }
    qf = qn;
  };
  group(std::integral_constant<int, 0>{}, 0);
  *(LAS bf16x8*)((LAS char*)lds + AL_Q + w * 1024 + l16) = qw;
  ((float*)(lds + AL_BND))[w * 16 + (lane & 15)] = __uint_as_float(kn_a) * __uint_as_float(row16_umax(qn_a));
  { const float c1 = xrow_sum(__uint_as_float(c1p)); if (lane < 16) ((float*)(lds + AL_CS))[w * 16 + lane] = c1; }
  raw_barrier();
  const f32x2 kq2 = *(const f32x2*)(lds + AL_BND + lane * 8);
  const bool slow = !__all(kq2[0] <= SHIFT * SHIFT * 0.998f && kq2[1] <= SHIFT * SHIFT * 0.998f);
  of = lds_frag(lds, AL_O + l16);
  STAMP(layer ? 1 : 6, 2);
  if (!slow) {
#pragma unroll 1
    for (int g = 1; g < 7; ++g) group(std::integral_constant<int, 1>{}, g);
    group(std::integral_constant<int, 2>{}, 7);
    __builtin_amdgcn_s_setprio(0);
    o = mfma32(vf[15], __builtin_bit_cast(bf16x8, p1), o);
    SB();
    load_res();
    SB();
    publish(7);
    raw_barrier();
    STAMP(layer ? 1 : 6, 9);
    reduce_ld(7);
    bf16x8 of0 = lds_frag(lds, AL_O + l16), of1 = lds_frag(lds, AL_O + 1024 + l16);
    SB();
#pragma unroll
    for (int t = 0; t < 4; ++t) mfma16_w<4>(oacc[t], wof[t], of);
    SB();
    reduce_fin(7);
    SB();
#pragma unroll
    for (int t = 0; t < 4; ++t) mfma16_ip(oacc[t], wl[t], of0);
#pragma unroll
    for (int t = 0; t < 4; ++t) mfma16_ip(oacc[t], wl[4 + t], of1);
    raw_barrier();
    STAMP(layer ? 1 : 6, 10);
    of0 = lds_frag(lds, AL_O + 2048 + l16); of1 = lds_frag(lds, AL_O + 3072 + l16);
#pragma unroll
    for (int t = 0; t < 4; ++t) mfma16_ip(oacc[t], wl[8 + t], of0);
#pragma unroll
    for (int t = 0; t < 4; ++t) mfma16_ip(oacc[t], wl[12 + t], of1);
    asm volatile("s_nop 7" : "+v"(oacc[0]), "+v"(oacc[1]), "+v"(oacc[2]), "+v"(oacc[3]));
  } else {
    __builtin_amdgcn_s_setprio(0);
    asm volatile("s_waitcnt vmcnt(0)" : "+v"(wof[0]), "+v"(wof[1]), "+v"(wof[2]), "+v"(wof[3]) :: "memory");
    float* mx = (float*)(lds + AL_MX);
    const f32x16 z = {};
#pragma unroll 1
    for (int g = 0; g < 8; ++g) {
      const bf16x8 q = lds_frag(lds, AL_Q + g * 1024 + l16);
      float m = -INFINITY;
#pragma unroll
      for (int t = 0; t < 8; ++t) {
        const f32x16 s = mfma32(kf[t], q, z);
#pragma unroll
        for (int i = 0; i < 16; ++i) m = fmaxf(m, s[i]);
        SB();
      }
      { auto sw = __builtin_amdgcn_permlane32_swap(__float_as_uint(m), __float_as_uint(m), false, false); m = fmaxf(__uint_as_float(sw[0]), __uint_as_float(sw[1])); }
      if (h == 0) mx[w * 256 + g * 32 + r] = m;
    }
    __syncthreads();
#pragma unroll 1
    for (int g = 0; g < 8; ++g) {
      const bf16x8 q = lds_frag(lds, AL_Q + g * 1024 + l16);
      float m = mx[g * 32 + r];
#pragma unroll
      for (int i = 1; i < 8; ++i) m = fmaxf(m, mx[i * 256 + g * 32 + r]);
      o = z;
#pragma unroll
      for (int t = 0; t < 8; ++t) {
        const f32x16 s = mfma32(kf[t], q, z);
        u32x4 a0, a1;
#pragma unroll
        for (int i = 0; i < 4; ++i) {
          a0[i] = pk2(EX(s[2 * i] - m), EX(s[2 * i + 1] - m));
          a1[i] = pk2(EX(s[8 + 2 * i] - m), EX(s[9 + 2 * i] - m));
        }
        o = mfma32(vf[2 * t], __builtin_bit_cast(bf16x8, a0), o);
        o = mfma32(vf[2 * t + 1], __builtin_bit_cast(bf16x8, a1), o);
        SB();
      }
      publish(g);
      __syncthreads();
      reduce(g);
      __syncthreads();
      const bf16x8 b0 = lds_frag(lds, AL_O + (g & 1) * 2048 + l16), b1 = lds_frag(lds, AL_O + (g & 1) * 2048 + 1024 + l16);
#pragma unroll
      for (int t = 0; t < 4; ++t) {
        oacc[t] = mfma16(bld(rs, l16, wos + (t * 16 + 2 * g) * 1024), b0, oacc[t]);
        oacc[t] = mfma16(bld(rs, l16, wos + (t * 16 + 2 * g + 1) * 1024), b1, oacc[t]);
      }
    }
    load_res();
#pragma unroll
    for (int i = 0; i < PRE_ATT; ++i) ffn2_prefetch(rs, l16, (idx & 7) * 4, w, stg, i);
#pragma unroll 1
    for (int i = 0; i < 14; ++i) dma_w1(rs, lds0, l16, f1_nt0, w, i >> 1, i & 1);
    asm volatile("s_nop 7" : "+v"(oacc[0]), "+v"(oacc[1]), "+v"(oacc[2]), "+v"(oacc[3]));
  }
  float xv[8];
  asm volatile("s_nop 1\n\t"
               "v_add_f32_dpp %0, %8, %8 row_ror:8 row_mask:0xf bank_mask:0x3\n\t"
               "v_add_f32_dpp %1, %9, %9 row_ror:8 row_mask:0xf bank_mask:0x3\n\t"
               "v_add_f32_dpp %2, %10, %10 row_ror:8 row_mask:0xf bank_mask:0x3\n\t"
               "v_add_f32_dpp %3, %11, %11 row_ror:8 row_mask:0xf bank_mask:0x3\n\t"
               "v_add_f32_dpp %4, %12, %12 row_ror:8 row_mask:0xf bank_mask:0x3\n\t"
               "v_add_f32_dpp %5, %13, %13 row_ror:8 row_mask:0xf bank_mask:0x3\n\t"
               "v_add_f32_dpp %6, %14, %14 row_ror:8 row_mask:0xf bank_mask:0x3\n\t"
               "v_add_f32_dpp %7, %15, %15 row_ror:8 row_mask:0xf bank_mask:0x3\n\t"
               "v_add_f32_dpp %0, %16, %16 row_ror:8 row_mask:0xf bank_mask:0xc\n\t"
               "v_add_f32_dpp %1, %17, %17 row_ror:8 row_mask:0xf bank_mask:0xc\n\t"
               "v_add_f32_dpp %2, %18, %18 row_ror:8 row_mask:0xf bank_mask:0xc\n\t"
               "v_add_f32_dpp %3, %19, %19 row_ror:8 row_mask:0xf bank_mask:0xc\n\t"
               "v_add_f32_dpp %4, %20, %20 row_ror:8 row_mask:0xf bank_mask:0xc\n\t"
               "v_add_f32_dpp %5, %21, %21 row_ror:8 row_mask:0xf bank_mask:0xc\n\t"
               "v_add_f32_dpp %6, %22, %22 row_ror:8 row_mask:0xf bank_mask:0xc\n\t"
               "v_add_f32_dpp %7, %23, %23 row_ror:8 row_mask:0xf bank_mask:0xc\n\t"
               : "=&v"(xv[0]), "=&v"(xv[1]), "=&v"(xv[2]), "=&v"(xv[3]), "=&v"(xv[4]), "=&v"(xv[5]), "=&v"(xv[6]), "=&v"(xv[7])
               : "v"(oacc[0][0]), "v"(oacc[0][1]), "v"(oacc[0][2]), "v"(oacc[0][3]), "v"(oacc[1][0]), "v"(oacc[1][1]), "v"(oacc[1][2]), "v"(oacc[1][3]), "v"(oacc[2][0]), "v"(oacc[2][1]), "v"(oacc[2][2]), "v"(oacc[2][3]), "v"(oacc[3][0]), "v"(oacc[3][1]), "v"(oacc[3][2]), "v"(oacc[3][3]));
  u32x4 ov;
  float sm = 0.f, sq = 0.f;
#pragma unroll
  for (int q = 0; q < 4; ++q) {
    const float a = xv[2 * q] + bflo(xr[q]), b = xv[2 * q + 1] + bfhi(xr[q]);
    ov[q] = pk2(a, b);
    sm += a + b; sq = fmaf(a, a, fmaf(b, b, sq));
  }
  bst4<SC1>(ov, rs, fv, qkv + QO_X1 + fs);
  sm += dpp<0x128>(sm); sq += dpp<0x128>(sq);
  xrow_sum2(sm, sq);
  bst2<SC1>((u32x2){__float_as_uint(sm), __float_as_uint(sq)}, rs, lane < 8 ? lane * 64 : OOB, qkv + QO_ST + t0 * 64 + w * 8);
  STAMP(layer ? 1 : 6, 11);
  wg_signal(ws, flag_off(layer, 0, t0 >> 4) + (t0 & 8) / 2, tid, [&]() {});
}

DI void ffn1_phase(char* lds, char* ws, rsrc_t rs, int layer, int tid, bf16x8 (&stg)[28]) {
  const int lane = tid & 63, g = lane >> 4, mm = lane & 15;
  const int w = __builtin_amdgcn_readfirstlane(tid >> 6);
  const int bid = mybid(), xcd = bid & 7, idx = bid >> 3, rb = idx >> 4, cb = idx & 15;
  const int mt = xcd * 16 + rb * 8 + w, nt0 = cb * 7;
  const unsigned l16 = lane * 16;
  STAMP(2, 0);
#define PRE_EARLY 14
#define PRE_LATE 4
#pragma unroll
  for (int i = PRE_ATT; i < PRE_EARLY; ++i) ffn2_prefetch(rs, l16, (idx & 7) * 4, w, stg, i);
  bf16x8 a[2][7];
#pragma unroll
  for (int t = 0; t < 7; ++t) a[0][t] = lds_frag(lds, AL_W1 + t * 1024 + l16);
  wave_wait(ws, flag_off(layer, 0, mt), (lane & 1) * 4);
  STAMP(2, 1);
  constexpr int PD = 5;
  bf16x8 b[16];
  const unsigned lb = OFF_QKV + layer * QKV_BYTES, bs = lb + QO_X1 + mt * 16 * KB;
#pragma unroll
  for (int ks = 0; ks < PD; ++ks) b[ks] = bld<SC1>(rs, l16, bs + ks * KB);
  const f32x4 stp = bld4f<SC1>(rs, mm * 64 + g * 16, lb + QO_ST + mt * 1024);
  SB();
  STAMP(2, 2);
  f32x4 acc[7] = {};
  float rstd, nmr;
#pragma unroll
  for (int ks = 0; ks < 16; ++ks) {
    if (ks < 15) {
#pragma unroll
      for (int t = 0; t < 7; ++t) a[(ks + 1) & 1][t] = lds_frag(lds, AL_W1 + ((ks + 1) * 7 + t) * 1024 + l16);
    }
    if (ks + PD < 16) b[ks + PD] = bld<SC1>(rs, l16, bs + (ks + PD) * KB);
    {
      constexpr int NMID = 28 - PRE_EARLY - PRE_LATE;
      const int lo = ks <= 10 ? (ks * 6) / 11 : 6 + (ks - 11) * 2, hi = ks + 1 <= 10 ? ((ks + 1) * 6) / 11 : 6 + (ks + 1 - 11) * 2;
#pragma unroll
      for (int i = lo; i < hi; ++i) if (i < NMID) ffn2_prefetch(rs, l16, (idx & 7) * 4, w, stg, PRE_EARLY + i);
    }
    if (ks == 8) {
      float sm = stp[0] + stp[2], sq = stp[1] + stp[3];
      xrow_sum2(sm, sq);
      const float mean = sm * (1.f / D_);
      rstd = __builtin_amdgcn_rsqf(fmaxf(sq * (1.f / D_) - mean * mean, 0.f) + LN_EPS); nmr = -mean * rstd;
      PIN2(rstd, nmr);
    }
    SB();
#pragma unroll
    for (int t = 0; t < 7; ++t) acc[t] = mfma16(a[ks & 1][t], b[ks], acc[t]);
    SB();
    if (ks == 0) STAMP(2, 3);
    if (ks == 7) STAMP(2, 4);
  }
  STAMP(2, 10);
  u32x2 hv[7];
#pragma unroll
  for (int t = 0; t < 7; ++t) {
    const f32x4 v = acc[t] * rstd + *(const f32x4*)(lds + AL_CS + t * 64 + g * 16) * nmr;
    hv[t] = (u32x2){relu_pk(pk2(v[0], v[1])), relu_pk(pk2(v[2], v[3]))};
  }
  const unsigned hs = lb + QO_H + (mt * 56 + (nt0 >> 1)) * KB;
  if ((cb & 1) == 0) {
#pragma unroll
    for (int p = 0; p < 3; ++p) bst4<SC1>((u32x4){hv[2 * p][0], hv[2 * p][1], hv[2 * p + 1][0], hv[2 * p + 1][1]}, rs, l16, hs + p * KB);
    bst2<SC1>(hv[6], rs, l16, hs + 3 * KB);
  } else {
    bst2<SC1>(hv[0], rs, l16 + 8, hs);
#pragma unroll
    for (int p = 0; p < 3; ++p) bst4<SC1>((u32x4){hv[2 * p + 1][0], hv[2 * p + 1][1], hv[2 * p + 2][0], hv[2 * p + 2][1]}, rs, l16, hs + (p + 1) * KB);
  }
  STAMP(2, 11);
  wg_signal(ws, flag_off(layer, 1, (xcd * 2 + rb) * 8 + (cb >> 1)) + (cb & 1) * 4, tid,
            [&]() {});
}

#define QW_EARLY
#define TLB_WARM 65536
DI void ffn2_phase(char* lds, char* ws, rsrc_t rs, u16* out, int layer, int tid, bf16x8 (&stg)[28]) {
  const int lane = tid & 63, g = lane >> 4, mm = lane & 15;
  const int w = __builtin_amdgcn_readfirstlane(tid >> 6);
  const int bid = mybid(), xcd = bid & 7, idx = bid >> 3, rb = idx >> 3, cb = idx & 7;
  const int fm = w >> 1, fp = w & 1, mt0 = xcd * 16 + rb * 4, nt0 = cb * 4;
  const unsigned lds0 = (unsigned)(uintptr_t)lds, l16 = lane * 16;
  STAMP(3, 0);
#pragma unroll
  for (int i = 28 - PRE_LATE; i < 28; ++i) ffn2_prefetch(rs, l16, nt0, w, stg, i);
  const unsigned fs = ((mt0 + fm) * 16 + (nt0 >> 1) + fp) * KB;
  const unsigned lb = OFF_QKV + layer * QKV_BYTES;
  const u32x4 xr = bld4<SC1>(rs, l16, lb + QO_X1 + fs);
  if (layer == 0) {
#pragma unroll
    for (int i = 0; i < 6; ++i) { const int c = 6 * w + i; dma(rs, l16, OFF_WQKV + ((4 * cb + (c & 3)) * 16 + (c >> 2)) * KB, lds0 + c * 1024); }
  }
  wave_wait(ws, flag_off(layer, 1, (xcd * 2 + (rb >> 1)) * 8 + w), (lane & 1) * 4);
  STAMP(3, 1);
#define F2_PD 6
  constexpr int PD = F2_PD;
  bf16x8 b[28];
  auto ldb = [&](int i) { b[i] = bld<SC1>(rs, l16, lb + QO_H + ((mt0 + ((fm + 1 + i / 7) & 3)) * 56 + 7 * w + i % 7) * KB); };
#pragma unroll
  for (int i = 0; i < PD; ++i) ldb(i);
  SB();
  STAMP(3, 2);
  f32x4 acc[2][4];
  auto send = [&](int mi, int pr) {
    const int d = 2 * ((fm + 1 + mi) & 3) + (pr ^ fp);
    char* xb = lds + AL_W1 + (d * 7 + ((w - d - 1) & 7)) * 2048 + l16;
    *(f32x4*)(xb) = acc[mi & 1][2 * pr]; *(f32x4*)(xb + 1024) = acc[mi & 1][2 * pr + 1];
  };
#pragma unroll
  for (int i = 0; i < 28; ++i) {
    const int mi = i / 7, j = i % 7;
    if (i + PD < 28) ldb(i + PD);
    if (false) {
      const int c = 6 * w + (i - (28 - PD));
      dma(rs, l16, OFF_WQKV + ((4 * cb + (c & 3)) * 16 + (c >> 2)) * KB, lds0 + c * 1024);
    }
    if (mi > 0 && j == 3) send(mi - 1, 0);
    if (mi > 0 && j == 5) send(mi - 1, 1);
    SB();
#pragma unroll
    for (int t = 0; t < 4; ++t) {
      if (j == 0) { const f32x4 z = {}; acc[mi & 1][t] = mfma16(stg[t], b[i], z); }
      else acc[mi & 1][t] = mfma16(stg[4 * j + t], b[i], acc[mi & 1][t]);
    }
    SB();
  }
  STAMP(3, 9);
  send(3, 1);
  f32x4 s0 = acc[1][0] + (f32x4){bflo(xr[0]), bfhi(xr[0]), bflo(xr[1]), bfhi(xr[1])};
  f32x4 s1 = acc[1][1] + (f32x4){bflo(xr[2]), bfhi(xr[2]), bflo(xr[3]), bfhi(xr[3])};
  raw_barrier();
#pragma unroll
  for (int d = 0; d < 7; ++d) {
    const char* xb = lds + AL_W1 + (w * 7 + d) * 2048 + l16;
    s0 += *(const f32x4*)(xb); s1 += *(const f32x4*)(xb + 1024);
  }
  STAMP(3, 10);
  const u32x4 o = {pk2(s0[0], s0[1]), pk2(s0[2], s0[3]), pk2(s1[0], s1[1]), pk2(s1[2], s1[3])};
  if (layer == 1) {
    u16* op = out + (size_t)((mt0 + fm) * 16 + mm) * D_ + (nt0 + 2 * fp) * 16 + 4 * g;
    *(u32x2*)op = (u32x2){o[0], o[1]};
    *(u32x2*)(op + 16) = (u32x2){o[2], o[3]};
    STAMP(3, 11);
  } else {
    bst4<SC1>(o, rs, l16, OFF_X2 + fs);
    STAMP(3, 11);
    wg_signal(ws, flag_off(0, 2, (xcd * 4 + rb) * 2 + (cb >> 2)) + (cb & 3) * 4, tid, [&]() {});
  }
}

typedef short v4i16_t __attribute__((ext_vector_type(4)));
DI bf16x8 tr_frag(const char* lds, int off0, int off1) {
  const v4i16_t lo = __builtin_amdgcn_ds_read_tr16_b64_v4i16((LAS v4i16_t*)((LAS char*)lds + off0));
  const v4i16_t hi = __builtin_amdgcn_ds_read_tr16_b64_v4i16((LAS v4i16_t*)((LAS char*)lds + off1));
  return __builtin_shufflevector(lo, hi, 0, 1, 2, 3, 4, 5, 6, 7);
}
constexpr int QL_KV = 65536, QL_P = 81920, QL_TR = QL_P + 65536;
constexpr int QP_T = 0, QP_KV = 2048, QP_CS = 3072, QP_CK = 3136, QP_ST = 3200;
template <bool FIRST, class H1, class H2>
DI void qkv_phase(char* lds, char* ws, rsrc_t rs, int tid, const u16* x, const u16* wq, const u16* wkv, H1 hook1, H2 hook2) {
  const int lane = tid & 63, g = lane >> 4, mm = lane & 15;
  const int w = __builtin_amdgcn_readfirstlane(tid >> 6);
  const int bid = mybid(), xcd = bid & 7, idx = bid >> 3, rb = idx >> 3, cb = idx & 7;
  const int ml = w & 3, hf = w >> 2, mt = xcd * 16 + rb * 4 + ml;
  const int kvml = cb >> 1;
  const unsigned lds0 = (unsigned)(uintptr_t)lds, l16 = lane * 16;
  const unsigned qkv = OFF_QKV + (FIRST ? 0 : QKV_BYTES);
  char* const mine = lds + QL_P + w * 8192;
  const char* const other = lds + QL_P + (w ^ 4) * 8192;
  STAMP(FIRST ? 5 : 0, 0);
  bf16x8 b[8];
  f32x4 cs0, cs1, csk; float csv;
  if (FIRST) {
    const rsrc_t rw = __builtin_amdgcn_make_buffer_rsrc((void*)wq, 0, D_ * D_ * 2, 0x00020000);
    const rsrc_t rk = __builtin_amdgcn_make_buffer_rsrc((void*)wkv, 0, D_ * 16 * 2, 0x00020000);
    const rsrc_t rx = __builtin_amdgcn_make_buffer_rsrc((void*)x, 0, S_ * D_ * 2, 0x00020000);
#pragma unroll
    for (int i = 0; i < 2; ++i) dma(rk, l16, (2 * w + i) * 1024, lds0 + QL_KV + (2 * w + i) * 1024);
    const unsigned vx = (lane >> 5) * 1024 + (((lane & 31) ^ (lane >> 5)) << 4);
#pragma unroll
    for (int i = 0; i < 8; ++i) dma(rx, vx ^ (i << 5), mt * 16384 + i * 2048 + hf * 512, lds0 + QL_P + w * 8192 + i * 1024);
    const unsigned v0 = (lane >> 3) * 1024 + (((lane & 7) ^ ((lane >> 3) & 2)) << 4), v1 = v0 ^ 64;
#pragma unroll
    for (int i = 0; i < 8; ++i) dma(rw, (i & 1) ? v1 : v0, (8 * w + i) * 8192 + cb * 128, lds0 + (8 * w + i) * 1024);
    wait_vm<0>();
#pragma unroll
    for (int j = 0; j < 8; ++j) b[j] = lds_frag(mine, mm * 512 + (((4 * j + g) ^ mm) << 4));
  } else {
#pragma unroll
    for (int i = 0; i < 2; ++i) {
      const int c = 48 + 2 * w + i;
      dma(rs, l16, OFF_WQKV + ((4 * cb + (c & 3)) * 16 + (c >> 2)) * KB, lds0 + c * 1024);
      dma(rs, l16, OFF_WQKV + ((32 + (cb & 1)) * 16 + 2 * w + i) * KB, lds0 + QL_KV + (2 * w + i) * 1024);
    }
    cs0 = bld4f(rs, g * 16, OFF_CSUM + (4 * cb + 2 * hf) * 64); cs1 = bld4f(rs, g * 16 + 64, OFF_CSUM + (4 * cb + 2 * hf) * 64);
    csk = bld4f(rs, g * 16, OFF_CSUM + 32 * 64);
    csv = __uint_as_float(bld1(rs, mm * 4, OFF_CSUM + 33 * 64));
    wave_wait(ws, flag_off(0, 2, (xcd * 4 + rb) * 2 + hf), (lane & 3) * 4);
#pragma unroll
    for (int j = 0; j < 8; ++j) b[j] = bld<SC1>(rs, l16, OFF_X2 + (mt * 16 + 8 * hf + j) * KB);
  }
  raw_barrier();
  STAMP(FIRST ? 5 : 0, 1);
  hook1();
  const int q4 = (lane >> 2) & 3, p4 = lane & 3;
  const int fsw = ((q4 >> 1) & 1) | ((g & 1) << 1);
  const int abase = FIRST ? hf * 32768 + (8 * g + q4) * 128 + p4 * 8 : hf * 32768 + l16;
  auto aq = [&](int t  ) { return FIRST ? abase + ((t ^ fsw) << 5) : abase + t * 1024; };
  auto ldq = [&](int base, int j) { return FIRST ? tr_frag(lds, base + j * 4096, base + j * 4096 + 512) : lds_frag(lds, base + j * 4096); };
  const int kvb = QL_KV + hf * 8192 + (FIRST ? (8 * g + q4) * 32 + p4 * 8 : l16);
  auto ldkv = [&](int j) { return FIRST ? tr_frag(lds, kvb + j * 1024, kvb + j * 1024 + 128) : lds_frag(lds, kvb + j * 1024); };
  bf16x8 ones; for (int i = 0; i < 8; ++i) ones[i] = 0x3F80;
  if (FIRST) {
    const int cbs = aq(ml);
    f32x4 c0 = {}, c1 = {}, ck = {};
#pragma unroll
    for (int j = 0; j < 8; j += 2) { c0 = mfma16(ldq(cbs, j), ones, c0); c1 = mfma16(ldq(cbs, j + 1), ones, c1); }
    const int kv2 = kvb + ml * 2048;
#pragma unroll
    for (int j = 0; j < 2; ++j) ck = mfma16(FIRST ? tr_frag(lds, kv2 + j * 1024, kv2 + j * 1024 + 128) : lds_frag(lds, kv2 + j * 1024), ones, ck);
    if (mm == 0) { *(f32x4*)(mine + QP_CS + g * 16) = c0 + c1; *(f32x4*)(mine + QP_CK + g * 16) = ck; }
  }
  STAMP(FIRST ? 5 : 0, 2);
  f32x4 acc[4] = {}, accs = {}, accg = {};
  bf16x8 a[2][4];
  const int ab0 = aq(2 * hf), ab1 = aq(2 * hf + 1), ab2 = aq(2 - 2 * hf), ab3 = aq(3 - 2 * hf);
  auto lda = [&](int j) { a[j & 1][0] = ldq(ab0, j); a[j & 1][1] = ldq(ab1, j); a[j & 1][2] = ldq(ab2, j); a[j & 1][3] = ldq(ab3, j); };
  lda(0);
#pragma unroll
  for (int j = 0; j < 8; ++j) {
    if (j < 7) lda(j + 1);
    SB();
    accs = mfma16(ones, b[j], accs); accg = mfma16(b[j], b[j], accg);
#pragma unroll
    for (int t = 0; t < 4; ++t) acc[t] = mfma16(a[j & 1][t], b[j], acc[t]);
    SB();
  }
  f32x4 akv = {};
  if (ml == kvml) {
    if (cb & 1) {
#pragma unroll
      for (int j = 0; j < 8; ++j) akv = mfma16(b[j], ldkv(j), akv);
    } else {
#pragma unroll
      for (int j = 0; j < 8; ++j) akv = mfma16(ldkv(j), b[j], akv);
    }
    *(f32x4*)(mine + QP_KV + l16) = akv;
  }
  STAMP(FIRST ? 5 : 0, 9);
  *(f32x4*)(mine + QP_T + l16) = acc[2];
  *(f32x4*)(mine + QP_T + 1024 + l16) = acc[3];
  {
    const float s01 = (mm & 1) ? accg[1] : accg[0], s23 = (mm & 1) ? accg[3] : accg[2];
    if (g == (mm >> 2)) *(f32x2*)(mine + QP_ST + mm * 8) = (f32x2){accs[0], (mm & 2) ? s23 : s01};
  }
  hook2();
  raw_barrier();
  STAMP(FIRST ? 5 : 0, 10);
  float invd = 1.f / D_; PIN1(invd);
  auto stats = [&](int tokl, float& mean, float& rstd) {
    const f32x2 s = *(const f32x2*)(mine + QP_ST + tokl * 8) + *(const f32x2*)(other + QP_ST + tokl * 8);
    mean = s[0] * invd;
    rstd = __builtin_amdgcn_rsqf(fmaxf(s[1] * invd - mean * mean, 0.f) + LN_EPS);
  };
  float mean, rstd; stats(mm, mean, rstd);
  const char* csp = lds + QL_P + 2 * hf * 8192 + QP_CS + g * 16;
  if (FIRST) {
    cs0 = *(const f32x4*)(csp) + *(const f32x4*)(csp + 32768); cs1 = *(const f32x4*)(csp + 8192) + *(const f32x4*)(csp + 8192 + 32768);
    if (mt == 0) {
      bst4(__builtin_bit_cast(u32x4, cs0), rs, mm == 0 ? g * 16 : OOB, OFF_CSUM + (4 * cb + 2 * hf) * 64);
      bst4(__builtin_bit_cast(u32x4, cs1), rs, mm == 0 ? g * 16 + 64 : OOB, OFF_CSUM + (4 * cb + 2 * hf) * 64);
    }
  }
  const f32x4 s0 = acc[0] + *(const f32x4*)(other + QP_T + l16), s1 = acc[1] + *(const f32x4*)(other + QP_T + 1024 + l16);
  const float rq = rstd * QSCALE, nmq = -mean * rq;
  const f32x4 q0 = s0 * rq + cs0 * nmq, q1 = s1 * rq + cs1 * nmq;
  const u32x2 o0 = {pk2(q0[0], q0[1]), pk2(q0[2], q0[3])}, o1 = {pk2(q1[0], q1[1]), pk2(q1[2], q1[3])};
  {
    auto s0_ = __builtin_amdgcn_permlane16_swap(o0[0], o1[0], false, false);
    auto s1_ = __builtin_amdgcn_permlane16_swap(o0[1], o1[1], false, false);
    bst4<SC1>((u32x4){s0_[0], s1_[0], s0_[1], s1_[1]}, rs, (mm >> 3) * 8192 + ((g >> 1) * 32 + (2 * hf + (g & 1)) * 8 + (mm & 7)) * 16, qkv + QO_Q + mt * 16384 + cb * 1024);
  }
  float n0 = sq4(o0), n1 = sq4(o1);
  xrow_sum2(n0, n1);
  const unsigned qn = row16_umax(umax(__float_as_uint(n0), __float_as_uint(n1)));
  bst1<SC1>(qn, rs, lane == 0 ? 0u : OOB, qkv + QO_QN2 + mt * 64 + (cb * 2 + hf) * 4);
  if (hf == 0 && ml == kvml) {
    const f32x4 skv = akv + *(const f32x4*)(other + QP_KV + l16);
    if (cb & 1) {
      if (FIRST) {
        csv = 0.f;
#pragma unroll
        for (int i = 0; i < 8; ++i) csv += *(const float*)(lds + QL_P + i * 8192 + QP_CK + mm * 4);
        if (mt == 0) bst1(__float_as_uint(csv), rs, g == 0 ? mm * 4 : OOB, OFF_CSUM + 33 * 64);
      }
      float v[4];
#pragma unroll
      for (int i = 0; i < 4; ++i) { float mn, rsd; stats(4 * g + i, mn, rsd); v[i] = (skv[i] - mn * csv) * rsd; }
      const u32x2 o = {pk2(v[0], v[1]), pk2(v[2], v[3])};
      bst2<SC1>(o, rs, (g & 1) * 256 + mm * 16 + (g >> 1) * 8, qkv + QO_VT + mt * 512);
    } else {
      if (FIRST) {
        csk = (f32x4){};
#pragma unroll
        for (int i = 0; i < 8; ++i) csk += *(const f32x4*)(lds + QL_P + i * 8192 + QP_CK + g * 16);
        if (mt == 0) bst4(__builtin_bit_cast(u32x4, csk), rs, mm == 0 ? g * 16 : OOB, OFF_CSUM + 32 * 64);
      }
      const f32x4 k = (skv - csk * mean) * rstd;
      const u32x2 o = {pk2(k[0], k[1]), pk2(k[2], k[3])};
      bst2<SC1>(o, rs, mm * 32 + g * 8, qkv + QO_K + mt * 512);
      const unsigned n2 = row16_umax(__float_as_uint(xrow_sum(sq4(o))));
      bst1<SC1>(n2, rs, lane == 0 ? 0u : OOB, qkv + QO_KN2 + mt * 4);
    }
  }
  STAMP(FIRST ? 5 : 0, 11);
  if (!FIRST) wg_signal(ws, flag_off(0, 3, (xcd * 4 + rb) * 2 + (cb >> 2)) + (cb & 3) * 4, tid, [&]() {});
}

__global__ __launch_bounds__(512, 2) void k_first(const u16* __restrict__ x, const u16* __restrict__ wq, const u16* __restrict__ wk, const u16* __restrict__ wv,
                                                  const u16* __restrict__ wo, const u16* __restrict__ w1, const u16* __restrict__ w2, char* __restrict__ ws, const u16* outp) {
  extern __shared__ __attribute__((aligned(16))) char lds[];
  const int tid = threadIdx.x, lane = tid & 63, g = lane >> 4;
  const int w = __builtin_amdgcn_readfirstlane(tid >> 6);
  const rsrc_t rs = __builtin_amdgcn_make_buffer_rsrc((void*)ws, 0, WS_NEED, 0x00020000);
  const unsigned lds0 = (unsigned)(uintptr_t)lds, l16 = lane * 16;
  const int xcd = blockIdx.x & 7, idx = blockIdx.x >> 3, cb = idx & 7;
  const u16* tw; int tpitch, tks, tnb; unsigned tdst; int tkst;
  {
    const int bi = blockIdx.x * 4 + (w & 3);
    if (bi < 128) { tw = wo; tpitch = 1024; tks = bi >> 3; tnb = bi & 7; tdst = OFF_WO; tkst = 16; }
    else if (bi < 576) { const int cw = (bi - 128) >> 2, kq = cw / 28; tw = w1; tpitch = 3584; tks = 4 * kq + (w & 3); tnb = cw - 28 * kq; tdst = OFF_W1; tkst = 16; }
    else { const int c = bi - 576; tw = w2; tpitch = 1024; tks = c >> 3; tnb = c & 7; tdst = OFF_W2; tkst = 56; }
  }
  auto fetch = [&]() {
    if (w == 7) {
#pragma unroll
      for (int i = 0; i < (WS_NEED / TLB_WARM + 63) / 64; ++i) pull(rs, (unsigned)lane * TLB_WARM, i * 64 * TLB_WARM, lds0 + QL_P + 7 * 8192 + 6144);
      const rsrc_t ro = __builtin_amdgcn_make_buffer_rsrc((void*)outp, 0, S_ * D_ * 2, 0x00020000);
      pull(ro, (unsigned)lane * 32768, 0, lds0 + QL_P + 7 * 8192 + 6144);
    }
    if (w < 4) {
      const rsrc_t rt = __builtin_amdgcn_make_buffer_rsrc((void*)tw, 0, D_ * DFF * 2, 0x00020000);
      const unsigned v = (lane >> 3) * tpitch + (lane & 7) * 16;
#pragma unroll
      for (int i = 0; i < 4; ++i) dma(rt, v, (32 * tks + 8 * i) * tpitch + tnb * 128, lds0 + QL_TR + w * 4096 + i * 1024);
    }
  };
#define CVT_AUX 16
  auto convert = [&]() {
    const int q4 = (lane >> 2) & 3, p4 = lane & 3;
    if (w < 4) {
      wait_vm<0>();
      const int base = QL_TR + w * 4096 + (4 * g + q4) * 128 + p4 * 8;
      bf16x8 ones; for (int i = 0; i < 8; ++i) ones[i] = 0x3F80;
#pragma unroll
      for (int t = 0; t < 4; ++t) {
        const bf16x8 f = tr_frag(lds, base + t * 32, base + t * 32 + 2048);
        bst4<CVT_AUX>(__builtin_bit_cast(u32x4, f), rs, l16, tdst + ((4 * tnb + t) * tkst + tks) * KB);
        const f32x4 z = {};
        const f32x4 c = mfma16(f, ones, z);
        if ((lane & 15) == 0) *(f32x4*)(lds + QL_P + w * 8192 + 4096 + t * 64 + g * 16) = c;
      }
    } else if (w < 6) {
      const int c = (xcd * 4 + (idx >> 3)) * 2 + (w & 1), ks = c >> 2, t = c & 3;
      const int r0 = 4 * g + q4, f0 = ((r0 >> 1) & 1) | (((r0 >> 3) & 1) << 1);
      const int base = (32 * ks + r0) * 128 + ((t ^ f0) << 5) + p4 * 8;
      bst4<CVT_AUX>(__builtin_bit_cast(u32x4, tr_frag(lds, base, base + 2048)), rs, l16, OFF_WQKV + ((4 * cb + t) * 16 + ks) * KB);
    } else if (w == 6) {
      const int ks = xcd * 4 + (idx >> 3);
      if (cb < 2 && ks < 16) {
        const int base = QL_KV + (32 * ks + 4 * g + q4) * 32 + p4 * 8;
        bst4<CVT_AUX>(__builtin_bit_cast(u32x4, tr_frag(lds, base, base + 512)), rs, l16, OFF_WQKV + ((32 + cb) * 16 + ks) * KB);
      }
    } else {
      bst4<CVT_AUX>((u32x4){0u, 0u, 0u, 0u}, rs, lane < 32 ? l16 : OOB, OFF_FLAG + blockIdx.x * 512);
    }
  };
  qkv_phase<true>(lds, ws, rs, tid, x, wq, (cb & 1) ? wv : wk, fetch, convert);
  if (w == 7 && tdst == OFF_W1) {
    const float* c = (const float*)(lds + QL_P + 4096) + lane;
    bst1(__float_as_uint((c[0] + c[2048]) + (c[4096] + c[6144])), rs, lane * 4, OFF_CS1 + (tks >> 2) * 7168 + tnb * 256);
  }
}

DI int mytid(int w64) { int t; asm volatile("v_mbcnt_lo_u32_b32 %0, -1, %1\n\tv_mbcnt_hi_u32_b32 %0, -1, %0" : "=v"(t) : "s"(w64)); return t; }
__global__ __launch_bounds__(512, 2) void k_main(char* __restrict__ ws, u16* __restrict__ out, const u16* __restrict__ xin) {
  extern __shared__ __attribute__((aligned(16))) char lds[];
  const int w64 = __builtin_amdgcn_readfirstlane(threadIdx.x & ~63);
  const rsrc_t rs = __builtin_amdgcn_make_buffer_rsrc((void*)ws, 0, WS_NEED, 0x00020000);
#pragma unroll 1
  for (int layer = 0; layer < 2; ++layer) {
    bf16x8 stg[28];
    attn_phase(lds, ws, rs, xin, layer, mytid(w64), stg);
    ffn1_phase(lds, ws, rs, layer, mytid(w64), stg);
    ffn2_phase(lds, ws, rs, out, layer, mytid(w64), stg);
    if (layer == 0) qkv_phase<false>(lds, ws, rs, mytid(w64), nullptr, nullptr, nullptr, []() {}, []() {});
  }
}

extern "C" void kernel_launch(void* const* d_in, const int* in_sizes, int n_in, void* d_out, int out_size, void* d_ws, size_t ws_size,
                              hipStream_t stream) {
  const u16* x = (const u16*)d_in[0];
  const u16* wq = (const u16*)d_in[1];
  const u16* wk = (const u16*)d_in[2];
  const u16* wv = (const u16*)d_in[3];
  const u16* wo = (const u16*)d_in[4];
  const u16* w1 = (const u16*)d_in[6];
  const u16* w2 = (const u16*)d_in[7];
  char* ws = (char*)d_ws;
  u16* out = (u16*)d_out;
  (void)hipFuncSetAttribute((const void*)k_main, hipFuncAttributeMaxDynamicSharedMemorySize, LDS_BYTES);
  (void)hipFuncSetAttribute((const void*)k_first, hipFuncAttributeMaxDynamicSharedMemorySize, LDS_BYTES);
  k_first<<<256, 512, LDS_BYTES, stream>>>(x, wq, wk, wv, wo, w1, w2, ws, out);
  k_main<<<256, 512, LDS_BYTES, stream>>>(ws, out, x);
}
```

```cpp
#include <hip/hip_runtime.h>
#include <hip/hip_bf16.h>
#include <stdint.h>
#include <type_traits>

typedef __attribute__((ext_vector_type(8))) short bf16x8;
typedef __attribute__((ext_vector_type(2))) float f32x2;
typedef __attribute__((ext_vector_type(4))) float f32x4;
typedef __attribute__((ext_vector_type(16))) float f32x16;
typedef __attribute__((ext_vector_type(4))) unsigned u32x4;
typedef __attribute__((ext_vector_type(2))) unsigned u32x2;
typedef unsigned short u16;
typedef __amdgpu_buffer_rsrc_t rsrc_t;
typedef __attribute__((address_space(1))) unsigned gu32;
#define DI __device__ __forceinline__
#define LAS __attribute__((address_space(3)))

constexpr int S_ = 2048, D_ = 512, DFF = 1792;
constexpr float QSCALE = 0.25f * 1.4426950408889634f;
constexpr float LN_EPS = 1e-5f;

constexpr unsigned KB = 1024;
constexpr unsigned OFF_FLAG = 0;
constexpr unsigned OFF_EPOCH = 128 * KB;
constexpr unsigned FLAG_BYTES = 129 * KB;
constexpr unsigned OFF_WQKV = OFF_FLAG + FLAG_BYTES;
constexpr unsigned OFF_WO   = OFF_WQKV + 544 * KB;
constexpr unsigned OFF_W1   = OFF_WO + 512 * KB;
constexpr unsigned OFF_W2   = OFF_W1 + 1792 * KB;
constexpr unsigned OFF_CSUM = OFF_W2 + 1792 * KB;
constexpr unsigned OFF_CS1  = OFF_CSUM + 4 * KB;
constexpr unsigned OFF_X2   = OFF_CS1 + 28 * KB;
constexpr unsigned OFF_QKV  = OFF_X2 + 2048 * KB;
constexpr unsigned QO_X1 = 0;
constexpr unsigned QO_ST = QO_X1 + 2048 * KB;
constexpr unsigned QO_H  = QO_ST + 128 * KB;
constexpr unsigned QO_Q  = QO_H + 7168 * KB;
constexpr unsigned QO_K = QO_Q + 2048 * KB;
constexpr unsigned QO_VT = QO_K + 64 * KB;
constexpr unsigned QO_KN2 = QO_VT + 68 * KB;
constexpr unsigned QO_QN2 = QO_KN2 + 1 * KB;
constexpr unsigned QKV_BYTES = QO_QN2 + 8 * KB;
constexpr unsigned WS_NEED  = OFF_QKV + 2 * QKV_BYTES;
DI unsigned flag_off(int layer, int edge, int line) { return OFF_FLAG + ((layer * 4 + edge) * 128 + line) * 128; }

DI float bf2f(u16 v) { return __uint_as_float(((unsigned)v) << 16); }
DI float bflo(unsigned v) { return __uint_as_float(v << 16); }
DI float bfhi(unsigned v) { return __uint_as_float(v & 0xffff0000u); }
DI unsigned pk2(float lo, float hi) {
  typedef __attribute__((ext_vector_type(2))) __bf16 bf16x2_;
  f32x2 v = {lo, hi};
  bf16x2_ r = __builtin_convertvector(v, bf16x2_);
  return __builtin_bit_cast(unsigned, r);
}
DI unsigned relu_pk(unsigned v) {
  typedef __attribute__((ext_vector_type(2))) short s16x2;
  const s16x2 z = {0, 0};
  return __builtin_bit_cast(unsigned, __builtin_elementwise_max(__builtin_bit_cast(s16x2, v), z));
}
DI f32x4 mfma16(bf16x8 a, bf16x8 b, f32x4 c) {
  typedef __attribute__((ext_vector_type(8))) __bf16 b8;
  return __builtin_amdgcn_mfma_f32_16x16x32_bf16(__builtin_bit_cast(b8, a), __builtin_bit_cast(b8, b), c, 0, 0, 0);
}
DI f32x16 mfma32(bf16x8 a, bf16x8 b, f32x16 c) {
  typedef __attribute__((ext_vector_type(8))) __bf16 b8;
  return __builtin_amdgcn_mfma_f32_32x32x16_bf16(__builtin_bit_cast(b8, a), __builtin_bit_cast(b8, b), c, 0, 0, 0);
}
template <int N> DI void wait_vm() { asm volatile("s_waitcnt vmcnt(%0)" :: "i"(N) : "memory"); }
DI void raw_barrier() { asm volatile("s_waitcnt lgkmcnt(0)\n\ts_barrier" ::: "memory"); }
DI bf16x8 lds_frag(const char* lds, int off) { return *(const LAS bf16x8*)((const LAS char*)lds + off); }
#define STAMP(kid, i)
DI int kperm(int g, int j) { return (j < 4) ? 4 * g + j : 16 + 4 * g + (j - 4); }

template <int CTRL> DI float dpp(float x) { return __builtin_bit_cast(float, __builtin_amdgcn_mov_dpp(__builtin_bit_cast(int, x), CTRL, 0xf, 0xf, true)); }
template <int CTRL> DI unsigned dppu(unsigned x) { return (unsigned)__builtin_amdgcn_mov_dpp((int)x, CTRL, 0xf, 0xf, true); }
DI unsigned umax(unsigned a, unsigned b) { return a > b ? a : b; }
DI unsigned row16_umax(unsigned x) { x = umax(x, dppu<0xB1>(x)); x = umax(x, dppu<0x4E>(x)); x = umax(x, dppu<0x141>(x)); x = umax(x, dppu<0x140>(x)); return x; }
DI unsigned xrow_umax(unsigned x) {
  auto s = __builtin_amdgcn_permlane16_swap(x, x, false, false); x = umax(s[0], s[1]);
  auto t = __builtin_amdgcn_permlane32_swap(x, x, false, false); return umax(t[0], t[1]); }
DI float xrow_sum(float x) {
  auto s = __builtin_amdgcn_permlane16_swap(__float_as_uint(x), __float_as_uint(x), false, false);
  x = __uint_as_float(s[0]) + __uint_as_float(s[1]);
  auto t = __builtin_amdgcn_permlane32_swap(__float_as_uint(x), __float_as_uint(x), false, false);
  return __uint_as_float(t[0]) + __uint_as_float(t[1]); }
DI void xrow_sum2(float& a, float& b) {
  auto s = __builtin_amdgcn_permlane16_swap(__float_as_uint(a), __float_as_uint(b), false, false);
  const float x = __uint_as_float(s[0]) + __uint_as_float(s[1]);
  auto t = __builtin_amdgcn_permlane32_swap(__float_as_uint(x), __float_as_uint(x), false, false);
  const float y = __uint_as_float(t[0]) + __uint_as_float(t[1]);
  auto u = __builtin_amdgcn_permlane16_swap(__float_as_uint(y), __float_as_uint(y), false, false);
  a = __uint_as_float(u[0]); b = __uint_as_float(u[1]); }
DI float sq4(u32x2 o) { return bflo(o[0]) * bflo(o[0]) + bfhi(o[0]) * bfhi(o[0]) + bflo(o[1]) * bflo(o[1]) + bfhi(o[1]) * bfhi(o[1]); }
#define SB() __builtin_amdgcn_sched_barrier(0)
#define EX(x) __builtin_amdgcn_exp2f(x)
#define PIN1(a) asm volatile("" : "+v"(a))
#define PIN2(a, b) asm volatile("" : "+v"(a), "+v"(b))
#define PIN4(a, b, c, d) asm volatile("" : "+v"(a), "+v"(b), "+v"(c), "+v"(d))
constexpr int SC1 = 16;
DI void mfma16_ip(f32x4& acc, bf16x8 a, bf16x8 b) { asm volatile("v_mfma_f32_16x16x32_bf16 %0, %1, %2, %0" : "+v"(acc) : "v"(a), "v"(b)); }
DI void bld_asm(bf16x8& d, rsrc_t rs, unsigned voff, unsigned soff) { asm volatile("buffer_load_dwordx4 %0, %1, %2, %3 offen sc1" : "+v"(d) : "v"(voff), "s"(rs), "s"(soff)); }
template <int N> DI void mfma16_w(f32x4& acc, bf16x8& a, bf16x8 b) { asm volatile("s_waitcnt vmcnt(%3)\n\tv_mfma_f32_16x16x32_bf16 %0, %1, %2, %0" : "+v"(acc), "+v"(a) : "v"(b), "i"(N)); }
template <int AUX = 0> DI bf16x8 bld(rsrc_t rs, unsigned voff, unsigned soff) { return __builtin_bit_cast(bf16x8, __builtin_amdgcn_raw_buffer_load_b128(rs, voff, soff, AUX)); }
template <int AUX = 0> DI u32x4 bld4(rsrc_t rs, unsigned voff, unsigned soff) { return __builtin_amdgcn_raw_buffer_load_b128(rs, voff, soff, AUX); }
template <int AUX = 0> DI f32x4 bld4f(rsrc_t rs, unsigned voff, unsigned soff) { return __builtin_bit_cast(f32x4, __builtin_amdgcn_raw_buffer_load_b128(rs, voff, soff, AUX)); }
template <int AUX = 0> DI unsigned bld1(rsrc_t rs, unsigned voff, unsigned soff) { return __builtin_amdgcn_raw_buffer_load_b32(rs, voff, soff, AUX); }
template <int AUX = 0> DI void bst4(u32x4 v, rsrc_t rs, unsigned voff, unsigned soff) {
  __builtin_amdgcn_raw_buffer_store_b128(v, rs, voff, soff, AUX);
  asm volatile("s_nop 1" :: "v"(v) : "memory");
}
template <int AUX = 0> DI void bst2(u32x2 v, rsrc_t rs, unsigned voff, unsigned soff) { __builtin_amdgcn_raw_buffer_store_b64(v, rs, voff, soff, AUX); }
template <int AUX = 0> DI void bst1(unsigned v, rsrc_t rs, unsigned voff, unsigned soff) { __builtin_amdgcn_raw_buffer_store_b32(v, rs, voff, soff, AUX); }
constexpr unsigned OOB = 0x7ffffff0u;
DI void dma(rsrc_t rs, unsigned voff, unsigned soff, unsigned lds_dst) {
  unsigned sv;
  asm volatile("s_mov_b32 %0, m0\n\ts_mov_b32 m0, %1\n\ts_nop 0\n\tbuffer_load_dwordx4 %2, %3, %4 offen sc1 lds\n\ts_mov_b32 m0, %0"
               : "=&s"(sv) : "s"(lds_dst), "v"(voff), "s"(rs), "s"(soff) : "memory");
}
typedef unsigned long long u64;
typedef __attribute__((address_space(1))) u64 gu64;
DI void wg_signal(rsrc_t rs, unsigned foff, int tid, u64 tag) {
  asm volatile("s_waitcnt vmcnt(0)" ::: "memory");
  raw_barrier();
  asm volatile("" : "+s"(tag));
  bst2<SC1>((u32x2){(unsigned)tag, (unsigned)(tag >> 32)}, rs, tid == 0 ? 0u : OOB, foff);
}
DI u64 flag_ld(char* ws, unsigned foff, unsigned lane_off) { return __hip_atomic_load((gu64*)(ws + foff + lane_off), __ATOMIC_RELAXED, __HIP_MEMORY_SCOPE_AGENT); }
DI void wave_wait(char* ws, unsigned foff, unsigned lane_off, u64 tag) {
#pragma unroll 1
  for (unsigned spins = 0; spins < (1u << 22); ++spins) {
    if (__all(flag_ld(ws, foff, lane_off) == tag)) break;
    __builtin_amdgcn_s_sleep(1);
  }
  asm volatile("" ::: "memory");
}

DI unsigned smul(unsigned a, unsigned b) { unsigned r; asm("s_mul_i32 %0, %1, %2" : "=s"(r) : "s"(a), "s"(b)); return r; }
DI int mybid() { int b = blockIdx.x; asm volatile("" : "+s"(b)); return b; }

constexpr int LDS_BYTES = 163840;
constexpr int AL_RO = 0;
constexpr int AL_RL = AL_RO + 32768;
constexpr int AL_O  = AL_RL + 2048;
constexpr int AL_Q  = AL_O + 4096;
constexpr int AL_FL = AL_Q + 8192;
constexpr int AL_BND = AL_FL + 256;
constexpr int AL_CS = AL_BND + 512;
constexpr int AL_JUNK = AL_Q;
constexpr int AL_W1 = 49152;
constexpr int AL_MX = AL_W1;
static_assert(AL_CS + 512 <= AL_W1 && AL_W1 + 112 * 1024 <= LDS_BYTES, "lds");
constexpr float SHIFT = 32.f;

DI void dma_w1(rsrc_t rs, unsigned lds0, unsigned l16, int nt0, int w, int t, int kk) {
  dma(rs, l16, OFF_W1 + ((nt0 + t) * 16 + 2 * w + kk) * KB, lds0 + AL_W1 + smul(w, 14336) + (kk * 7 + t) * 1024);
}

DI void ffn2_prefetch(rsrc_t rs, unsigned l16, int nt0, int w, bf16x8 (&stg)[28], int i) {
  const int ti = i & 3;
  stg[i] = bld<SC1>(rs, l16, OFF_W2 + smul(nt0 + ((ti & 2) ^ (2 * (w & 1))), 56 * KB) + smul(w, 7 * KB) + ((ti & 1) * 56 + (i >> 2)) * KB);
}

#define PRE_ATT 0
DI void attn_phase(char* lds, char* ws, rsrc_t rs, const u16* xin, int layer, int tid, bf16x8 (&stg)[28], u64 tag) {
  const int lane = tid & 63, r = lane & 31, h = lane >> 5;
  const int w = __builtin_amdgcn_readfirstlane(tid >> 6);
  const int bid = mybid(), xcd = bid & 7, idx = bid >> 3;
  const int t0 = xcd * 256 + idx * 8;
  const unsigned lds0 = (unsigned)(uintptr_t)lds;
  const unsigned qkv = OFF_QKV + smul(layer, QKV_BYTES);
  const unsigned l16 = lane * 16;
  const int f1_nt0 = smul(idx & 15, 7);
  STAMP(layer ? 1 : 6, 0);
  wave_wait(ws, flag_off(layer, 3, 0), (h == 0 ? w * 8 + (lane >> 2) : (xcd * 4 + (idx >> 3)) * 2 + ((lane >> 2) & 1)) * 128 + (lane & 3) * 8, tag);
  STAMP(layer ? 1 : 6, 3);
  const unsigned qs = qkv + QO_Q + t0 * 1024;
  bf16x8 qf = bld<SC1>(rs, l16, qs);
  SB();
  bf16x8 kf[8], vf[16], qn, qw;
  unsigned kn_a, qn_a, c1p;
  u64 wfl;
  const unsigned kb = qkv + QO_K + w * 8192, vb = qkv + QO_VT + smul(w, 16 * 544);
  const unsigned ko = r * 32 + 16 * h, vo = r <= 16 ? h * 272 + r * 16 : OOB;
  auto load_kv = [&](int t) {
    if (t < 7) kf[t + 1] = bld<SC1>(rs, ko + ((t + 1) & 3) * 1024, kb + ((t + 1) >> 2) * 4096);
    else qn = bld<SC1>(rs, l16 + 1024, qs);
    vf[2 * t] = bld<SC1>(rs, vo, vb + (2 * t) * 544);
    vf[2 * t + 1] = bld<SC1>(rs, vo, vb + (2 * t + 1) * 544);
    asm volatile("" ::: "memory");
    SB();
  };
#define KV_PD 3
  kf[0] = bld<SC1>(rs, ko, kb);
  SB();
#pragma unroll
  for (int t = 0; t < KV_PD; ++t) load_kv(t);
  ((unsigned*)(lds + AL_FL))[lane] = h;
  const unsigned wos = OFF_WO + (4 * w * 16) * KB;
  f32x4 oacc[4] = {};
  f32x16 o;
  char* const ro_w = lds + AL_RO + w * 2048 + lane * 8;
  const int lf_w0 = h == 0 ? AL_RL + (r * 8 + w) * 4 : AL_FL + (w * 4 + (r & 3)) * 4, lf_step = h == 0 ? 1024 : 0;
  const int o_wr = AL_O + (r >> 4) * 1024 + ((2 * ((w & 3) >> 1) + h) * 16 + (w >> 2) * 8 + (r & 7)) * 16 + ((r >> 3) & 1) * 8 + (w & 1) * 4;
  auto publish = [&](int g) {
    char* ro = ro_w + (g & 1) * 16384;
    *(f32x2*)(ro) = (f32x2){o[0], o[1]}; *(f32x2*)(ro + 512) = (f32x2){o[2], o[3]};
    *(f32x2*)(ro + 1024) = (f32x2){o[4], o[5]}; *(f32x2*)(ro + 1536) = (f32x2){o[6], o[7]};
    asm volatile("" ::: "memory");
    *(volatile LAS unsigned*)((LAS char*)lds + lf_w0 + (g & 1) * lf_step) = h == 0 ? __float_as_uint(o[8]) : (unsigned)(g + 1);
  };
  f32x2 ra[4]; f32x4 rl0, rl1;
  const char* const red_r = lds + AL_RO + (w >> 2) * 8192 + (w & 3) * 512 + lane * 8;
  auto reduce_ld = [&](int g) {
#pragma unroll
    for (int i = 0; i < 4; ++i) ra[i] = *(const f32x2*)(red_r + (g & 1) * 16384 + i * 2048);
    rl0 = *(const f32x4*)(lds + AL_RL + (g & 1) * 1024 + r * 32); rl1 = *(const f32x4*)(lds + AL_RL + (g & 1) * 1024 + r * 32 + 16);
  };
  auto reduce_fin = [&](int g) {
    const f32x4 l4 = rl0 + rl1;
    const f32x2 l2 = (f32x2){l4[0], l4[1]} + (f32x2){l4[2], l4[3]};
    const float inv = __builtin_amdgcn_rcpf(l2[0] + l2[1]);
    const f32x2 sa = (ra[0] + ra[1]) + (ra[2] + ra[3]);
    *(unsigned*)(lds + o_wr + (g & 1) * 2048) = pk2(sa[0] * inv, sa[1] * inv);
    asm volatile("" ::: "memory");
    *(volatile LAS unsigned*)((LAS char*)lds + AL_FL + 128 + w * 16 + (lane & 3) * 4) = (unsigned)(g + 2);
  };
  auto reduce = [&](int g) { reduce_ld(g); reduce_fin(g); };
  const int g4 = lane >> 4, mm = lane & 15;
  const unsigned fs = ((t0 >> 4) * 16 + 2 * w) * KB, fv = (mm >> 3) * 1024 + (g4 * 16 + (t0 & 15) + (mm & 7)) * 16;
  u32x4 xr;
  auto load_res = [&]() {
    if (layer == 0) {
      const u16* xp = xin + (size_t)(t0 + (mm & 7)) * D_ + 64 * w + 32 * (mm >> 3) + 4 * g4;
      const u32x2 lo = *(const u32x2*)xp, hi = *(const u32x2*)(xp + 16);
      xr = (u32x4){lo[0], lo[1], hi[0], hi[1]};
    } else xr = bld4<SC1>(rs, fv, OFF_X2 + fs);
  };
  f32x16 cinit;
  { float ns = -SHIFT; PIN1(ns);
#pragma unroll
    for (int i = 0; i < 16; ++i) cinit[i] = ns; }
  PIN1(cinit);
  bf16x8 wof[4], of;
  { unsigned z0 = 0u; PIN1(z0); const u32x4 z = {z0, z0, z0, z0}; wof[0] = wof[1] = wof[2] = wof[3] = __builtin_bit_cast(bf16x8, z); }
  auto load_c1 = [&]() { c1p = bld1<SC1>(rs, (lane >> 4) * 7168 + (lane & 15) * 4, OFF_CS1 + (f1_nt0 + (w < 7 ? w : 6)) * 64); };
  auto store_c1 = [&]() { const float c1 = xrow_sum(__uint_as_float(c1p)); if (lane < 16) ((float*)(lds + AL_CS))[w * 16 + lane] = c1; };
  u32x4 p0, p1;
  unsigned flg;
  const unsigned fl_addr = lds0 + AL_FL + lane * 4;
  STAMP(layer ? 1 : 6, 1);
  f32x16 sc = mfma32(kf[0], qf, cinit);
  const unsigned w1s = OFF_W1 + (f1_nt0 * 16 + 2 * w) * KB, w1d = lds0 + AL_W1 + smul(w, 14336);
  bf16x8 wl[16];
  auto group = [&](auto mode_tag, const int g) {
    constexpr bool FIRST = decltype(mode_tag)::value == 0, SECOND = decltype(mode_tag)::value == 1, LAST = decltype(mode_tag)::value == 3;
    constexpr bool WGT = !FIRST && !SECOND;
#pragma unroll
    for (int t = 0; t < 8; ++t) {
      if (t == 3) __builtin_amdgcn_s_setprio(3); if (t == 5) __builtin_amdgcn_s_setprio(2); if (t == 7) __builtin_amdgcn_s_setprio(1); if (t == 1) __builtin_amdgcn_s_setprio(0);
      f32x16 sn;
      if (!(LAST && t == 7)) sn = mfma32(kf[(t + 1) & 7], t < 7 ? qf : qn, cinit);
      SB();
      if (FIRST) {
        if (t + KV_PD < 8) load_kv(t + KV_PD);
        if (t == 8 - KV_PD || (KV_PD == 8 && t == 0)) {
          qw = bld<SC1>(rs, l16, qs + w * 1024);
          kn_a = bld1<SC1>(rs, (lane & 15) * 4, qkv + QO_KN2 + w * 64);
          qn_a = bld1<SC1>(rs, (lane & 15) * 4, qkv + QO_QN2 + (t0 >> 4) * 64);
          asm volatile("" ::: "memory");
          SB();
        }
      }
      float e0 = EX(sc[0]), e1 = EX(sc[1]), e2 = EX(sc[2]), e3 = EX(sc[3]), e4 = EX(sc[4]), e5 = EX(sc[5]);
      PIN4(e0, e1, e2, e3); PIN2(e4, e5); SB();
      if (t > 0 || !FIRST) o = mfma32(vf[(2 * t + 15) & 15], __builtin_bit_cast(bf16x8, p1), o);
      SB();
      float e6 = EX(sc[6]), e7 = EX(sc[7]);
      PIN2(e6, e7); SB();
      p0[0] = pk2(e0, e1); p0[1] = pk2(e2, e3); p0[2] = pk2(e4, e5); p0[3] = pk2(e6, e7);
      PIN1(p0); SB();
      float e8 = EX(sc[8]), e9 = EX(sc[9]);
      PIN2(e8, e9); SB();
      float e10, e11, e12, e13;
      if (t == 0) {
        if (!FIRST) { e10 = EX(sc[10]); e11 = EX(sc[11]); e12 = EX(sc[12]); e13 = EX(sc[13]); PIN4(e10, e11, e12, e13); PIN1(o); SB(); publish(g - 1); SB(); }
        f32x16 z = {}; o = mfma32(vf[0], __builtin_bit_cast(bf16x8, p0), z);
      } else o = mfma32(vf[2 * t], __builtin_bit_cast(bf16x8, p0), o);
      SB();
      if (t > 0 || FIRST) { e10 = EX(sc[10]); e11 = EX(sc[11]); e12 = EX(sc[12]); e13 = EX(sc[13]); PIN4(e10, e11, e12, e13); SB(); }
      if (WGT) {
        constexpr int X = LAST ? 2 + PRE_ATT / 8 : 0, D = LAST ? 0 : 1;
        if (t == 0) mfma16_w<3>(oacc[0], wof[0], of); else if (t == 1) mfma16_w<3 + X>(oacc[1], wof[1], of);
        else if (t == 2) mfma16_w<3 + D + 2 * X>(oacc[2], wof[2], of); else if (t == 3) mfma16_w<3 + 2 * D + 3 * X>(oacc[3], wof[3], of);
        else if (t == 4) mfma16_w<3 + 3 * D + 4 * X>(oacc[0], wof[0], of); else if (t == 5) mfma16_w<3 + 3 * D + 4 * X>(oacc[1], wof[1], of);
        else if (t == 6) mfma16_w<3 + 2 * D + 4 * X>(oacc[2], wof[2], of); else mfma16_w<3 + D + 4 * X>(oacc[3], wof[3], of);
        SB();
      }
      float e14 = EX(sc[14]), e15 = EX(sc[15]);
      PIN2(e14, e15); SB();
      p1[0] = pk2(e8, e9); p1[1] = pk2(e10, e11); p1[2] = pk2(e12, e13); p1[3] = pk2(e14, e15);
      PIN1(p1); SB();
      if (WGT) {
        bld_asm(wof[t & 3], rs, l16, wos + ((t & 3) * 16 + 2 * (g - 2) + (t >> 2)) * 1024);
        SB();
      }
      if (LAST) {
#pragma unroll
        for (int i = 2 * t; i < 2 * t + 2; ++i) { wl[i] = bld<SC1>(rs, l16, wos + ((i & 3) * 16 + 12 + (i >> 2)) * 1024); asm volatile("" ::: "memory"); }
        if (PRE_ATT) { ffn2_prefetch(rs, l16, (idx & 7) * 4, w, stg, t); asm volatile("" ::: "memory"); }
        SB();
      }
      if (WGT && !LAST && t >= 1 && t <= 3) {
        const int pc = min(3 * (g - 2) + (t - 1), 13);
        dma(rs, l16, w1s + (pc >> 1) * 16 * KB + (pc & 1) * KB, w1d + (pc >> 1) * 1024 + ((pc & 1) ? 7168 : 0));
        SB();
      }
      if (SECOND && t == 5) { wfl = flag_ld(ws, flag_off(1, 2, w * 8), (r >> 2) * 128 + (lane & 3) * 8); SB(); }
      if (!FIRST && t == 1) { flg = *(volatile LAS unsigned*)((LAS char*)lds + AL_FL + lane * 4); SB(); }
      if (!FIRST && t == 2) {
        asm volatile("1:\n\tv_cmp_gt_u32 vcc, %2, %0\n\ts_cbranch_vccz 2f\n\ts_sleep 1\n\tds_read_b32 %0, %1\n\ts_waitcnt lgkmcnt(0)\n\ts_branch 1b\n2:"
                     : "+v"(flg) : "v"(fl_addr), "s"(g) : "vcc", "memory");
        reduce_ld(g - 1);
        SB();
      }
      if (!FIRST && t == 3) {
        reduce_fin(g - 1);
        SB();
      }
      if (WGT && (t & 3) == 3) { of = lds_frag(lds, AL_O + (g & 1) * 2048 + (t >> 2) * 1024 + l16); SB(); }
      if (!FIRST && !LAST && t == 5) { qn = lds_frag(lds, AL_Q + (g + 1) * 1024 + l16); SB(); }
      if (!(LAST && t == 7)) sc = sn;
    }
    qf = qn;
  };
  group(std::integral_constant<int, 0>{}, 0);
  *(LAS bf16x8*)((LAS char*)lds + AL_Q + w * 1024 + l16) = qw;
  ((float*)(lds + AL_BND))[w * 16 + (lane & 15)] = __uint_as_float(kn_a) * __uint_as_float(row16_umax(qn_a));
  wg_signal(rs, flag_off(1, 2, (xcd * 4 + (idx >> 3)) * 2 + ((idx >> 2) & 1)) + (idx & 3) * 8, tid, tag);
  auto weights_ready = [&]() { wave_wait(ws, flag_off(1, 2, w * 8), (r >> 2) * 128 + (lane & 3) * 8, tag); raw_barrier(); };
  const f32x2 kq2 = *(const f32x2*)(lds + AL_BND + lane * 8);
  const bool slow = !__all(kq2[0] <= SHIFT * SHIFT * 0.998f && kq2[1] <= SHIFT * SHIFT * 0.998f);
  of = wof[0];
  STAMP(layer ? 1 : 6, 2);
  if (!slow) {
    group(std::integral_constant<int, 1>{}, 1);
    if (!__all(wfl == tag)) wave_wait(ws, flag_off(1, 2, w * 8), (r >> 2) * 128 + (lane & 3) * 8, tag);
    raw_barrier();
#pragma unroll 1
    for (int g = 2; g < 7; ++g) group(std::integral_constant<int, 2>{}, g);
    group(std::integral_constant<int, 3>{}, 7);
    __builtin_amdgcn_s_setprio(0);
    o = mfma32(vf[15], __builtin_bit_cast(bf16x8, p1), o);
    SB();
    load_res(); load_c1();
    SB();
    publish(7);
    raw_barrier();
    STAMP(layer ? 1 : 6, 9);
    reduce_ld(7);
    bf16x8 of0 = lds_frag(lds, AL_O + l16), of1 = lds_frag(lds, AL_O + 1024 + l16);
    SB();
#pragma unroll
    for (int t = 0; t < 4; ++t) mfma16_w<4>(oacc[t], wof[t], of);
    SB();
    reduce_fin(7);
    SB();
#pragma unroll
    for (int t = 0; t < 4; ++t) mfma16_ip(oacc[t], wl[t], of0);
#pragma unroll
    for (int t = 0; t < 4; ++t) mfma16_ip(oacc[t], wl[4 + t], of1);
    store_c1();
    raw_barrier();
    STAMP(layer ? 1 : 6, 10);
    of0 = lds_frag(lds, AL_O + 2048 + l16); of1 = lds_frag(lds, AL_O + 3072 + l16);
#pragma unroll
    for (int t = 0; t < 4; ++t) mfma16_ip(oacc[t], wl[8 + t], of0);
#pragma unroll
    for (int t = 0; t < 4; ++t) mfma16_ip(oacc[t], wl[12 + t], of1);
    asm volatile("s_nop 7" : "+v"(oacc[0]), "+v"(oacc[1]), "+v"(oacc[2]), "+v"(oacc[3]));
  } else {
    __builtin_amdgcn_s_setprio(0);
    weights_ready();
    float* mx = (float*)(lds + AL_MX);
    const f32x16 z = {};
#pragma unroll 1
    for (int g = 0; g < 8; ++g) {
      const bf16x8 q = lds_frag(lds, AL_Q + g * 1024 + l16);
      float m = -INFINITY;
#pragma unroll
      for (int t = 0; t < 8; ++t) {
        const f32x16 s = mfma32(kf[t], q, z);
#pragma unroll
        for (int i = 0; i < 16; ++i) m = fmaxf(m, s[i]);
        SB();
      }
      { auto sw = __builtin_amdgcn_permlane32_swap(__float_as_uint(m), __float_as_uint(m), false, false); m = fmaxf(__uint_as_float(sw[0]), __uint_as_float(sw[1])); }
      if (h == 0) mx[w * 256 + g * 32 + r] = m;
    }
    __syncthreads();
#pragma unroll 1
    for (int g = 0; g < 8; ++g) {
      const bf16x8 q = lds_frag(lds, AL_Q + g * 1024 + l16);
      float m = mx[g * 32 + r];
#pragma unroll
      for (int i = 1; i < 8; ++i) m = fmaxf(m, mx[i * 256 + g * 32 + r]);
      o = z;
#pragma unroll
      for (int t = 0; t < 8; ++t) {
        const f32x16 s = mfma32(kf[t], q, z);
        u32x4 a0, a1;
#pragma unroll
        for (int i = 0; i < 4; ++i) {
          a0[i] = pk2(EX(s[2 * i] - m), EX(s[2 * i + 1] - m));
          a1[i] = pk2(EX(s[8 + 2 * i] - m), EX(s[9 + 2 * i] - m));
        }
        o = mfma32(vf[2 * t], __builtin_bit_cast(bf16x8, a0), o);
        o = mfma32(vf[2 * t + 1], __builtin_bit_cast(bf16x8, a1), o);
        SB();
      }
      publish(g);
      __syncthreads();
      reduce(g);
      __syncthreads();
      const bf16x8 b0 = lds_frag(lds, AL_O + (g & 1) * 2048 + l16), b1 = lds_frag(lds, AL_O + (g & 1) * 2048 + 1024 + l16);
#pragma unroll
      for (int t = 0; t < 4; ++t) {
        oacc[t] = mfma16(bld<SC1>(rs, l16, wos + (t * 16 + 2 * g) * 1024), b0, oacc[t]);
        oacc[t] = mfma16(bld<SC1>(rs, l16, wos + (t * 16 + 2 * g + 1) * 1024), b1, oacc[t]);
      }
    }
    load_res(); load_c1(); store_c1();
#pragma unroll
    for (int i = 0; i < PRE_ATT; ++i) ffn2_prefetch(rs, l16, (idx & 7) * 4, w, stg, i);
#pragma unroll 1
    for (int i = 0; i < 7; ++i) { dma_w1(rs, lds0, l16, f1_nt0, w, i, 0); dma_w1(rs, lds0, l16, f1_nt0, w, i, 1); }
    asm volatile("s_nop 7" : "+v"(oacc[0]), "+v"(oacc[1]), "+v"(oacc[2]), "+v"(oacc[3]));
  }
  float xv[8];
  asm volatile("s_nop 1\n\t"
               "v_add_f32_dpp %0, %8, %8 row_ror:8 row_mask:0xf bank_mask:0x3\n\t"
               "v_add_f32_dpp %1, %9, %9 row_ror:8 row_mask:0xf bank_mask:0x3\n\t"
               "v_add_f32_dpp %2, %10, %10 row_ror:8 row_mask:0xf bank_mask:0x3\n\t"
               "v_add_f32_dpp %3, %11, %11 row_ror:8 row_mask:0xf bank_mask:0x3\n\t"
               "v_add_f32_dpp %4, %12, %12 row_ror:8 row_mask:0xf bank_mask:0x3\n\t"
               "v_add_f32_dpp %5, %13, %13 row_ror:8 row_mask:0xf bank_mask:0x3\n\t"
               "v_add_f32_dpp %6, %14, %14 row_ror:8 row_mask:0xf bank_mask:0x3\n\t"
               "v_add_f32_dpp %7, %15, %15 row_ror:8 row_mask:0xf bank_mask:0x3\n\t"
               "v_add_f32_dpp %0, %16, %16 row_ror:8 row_mask:0xf bank_mask:0xc\n\t"
               "v_add_f32_dpp %1, %17, %17 row_ror:8 row_mask:0xf bank_mask:0xc\n\t"
               "v_add_f32_dpp %2, %18, %18 row_ror:8 row_mask:0xf bank_mask:0xc\n\t"
               "v_add_f32_dpp %3, %19, %19 row_ror:8 row_mask:0xf bank_mask:0xc\n\t"
               "v_add_f32_dpp %4, %20, %20 row_ror:8 row_mask:0xf bank_mask:0xc\n\t"
               "v_add_f32_dpp %5, %21, %21 row_ror:8 row_mask:0xf bank_mask:0xc\n\t"
               "v_add_f32_dpp %6, %22, %22 row_ror:8 row_mask:0xf bank_mask:0xc\n\t"
               "v_add_f32_dpp %7, %23, %23 row_ror:8 row_mask:0xf bank_mask:0xc\n\t"
               : "=&v"(xv[0]), "=&v"(xv[1]), "=&v"(xv[2]), "=&v"(xv[3]), "=&v"(xv[4]), "=&v"(xv[5]), "=&v"(xv[6]), "=&v"(xv[7])
               : "v"(oacc[0][0]), "v"(oacc[0][1]), "v"(oacc[0][2]), "v"(oacc[0][3]), "v"(oacc[1][0]), "v"(oacc[1][1]), "v"(oacc[1][2]), "v"(oacc[1][3]), "v"(oacc[2][0]), "v"(oacc[2][1]), "v"(oacc[2][2]), "v"(oacc[2][3]), "v"(oacc[3][0]), "v"(oacc[3][1]), "v"(oacc[3][2]), "v"(oacc[3][3]));
  u32x4 ov;
  float sm = 0.f, sq = 0.f;
#pragma unroll
  for (int q = 0; q < 4; ++q) {
    const float a = xv[2 * q] + bflo(xr[q]), b = xv[2 * q + 1] + bfhi(xr[q]);
    ov[q] = pk2(a, b);
    sm += a + b; sq = fmaf(a, a, fmaf(b, b, sq));
  }
  bst4<SC1>(ov, rs, fv, qkv + QO_X1 + fs);
  sm += dpp<0x128>(sm); sq += dpp<0x128>(sq);
  xrow_sum2(sm, sq);
  bst2<SC1>((u32x2){__float_as_uint(sm), __float_as_uint(sq)}, rs, lane < 8 ? lane * 64 : OOB, qkv + QO_ST + t0 * 64 + w * 8);
  STAMP(layer ? 1 : 6, 11);
  wg_signal(rs, flag_off(layer, 0, t0 >> 4) + (t0 & 8), tid, tag);
}

DI void ffn1_phase(char* lds, char* ws, rsrc_t rs, int layer, int tid, bf16x8 (&stg)[28], u64 tag) {
  const int lane = tid & 63, g = lane >> 4, mm = lane & 15;
  const int w = __builtin_amdgcn_readfirstlane(tid >> 6);
  const int bid = mybid(), xcd = bid & 7, idx = bid >> 3, rb = idx >> 4, cb = idx & 15;
  const int mt = xcd * 16 + rb * 8 + w, nt0 = smul(cb, 7);
  const unsigned l16 = lane * 16;
  STAMP(2, 0);
#define PRE_EARLY 8
#define PRE_LATE 4
#pragma unroll
  for (int i = PRE_ATT; i < PRE_EARLY; ++i) ffn2_prefetch(rs, l16, (idx & 7) * 4, w, stg, i);
  bf16x8 a[2][7];
#pragma unroll
  for (int t = 0; t < 7; ++t) a[0][t] = lds_frag(lds, AL_W1 + t * 1024 + l16);
  wave_wait(ws, flag_off(layer, 0, mt), (lane & 1) * 8, tag);
  STAMP(2, 1);
  constexpr int PD = 5;
  bf16x8 b[16];
  const unsigned lb = OFF_QKV + smul(layer, QKV_BYTES), bs = lb + QO_X1 + mt * 16 * KB;
#pragma unroll
  for (int ks = 0; ks < PD; ++ks) b[ks] = bld<SC1>(rs, l16, bs + ks * KB);
  const f32x4 stp = bld4f<SC1>(rs, mm * 64 + g * 16, lb + QO_ST + mt * 1024);
  SB();
  STAMP(2, 2);
  f32x4 acc[7] = {};
  float rstd, nmr;
#pragma unroll
  for (int ks = 0; ks < 16; ++ks) {
    if (ks < 15) {
#pragma unroll
      for (int t = 0; t < 7; ++t) a[(ks + 1) & 1][t] = lds_frag(lds, AL_W1 + ((ks + 1) * 7 + t) * 1024 + l16);
    }
    if (ks + PD < 16) b[ks + PD] = bld<SC1>(rs, l16, bs + (ks + PD) * KB);
    {
      constexpr int NMID = 28 - PRE_EARLY - PRE_LATE;
      const int lo = ks <= 10 ? (ks * 6) / 11 : 6 + (ks - 11) * 2, hi = ks + 1 <= 10 ? ((ks + 1) * 6) / 11 : 6 + (ks + 1 - 11) * 2;
#pragma unroll
      for (int i = lo; i < hi; ++i) if (i < NMID) ffn2_prefetch(rs, l16, (idx & 7) * 4, w, stg, PRE_EARLY + i);
    }
    if (ks == 8) {
      float sm = stp[0] + stp[2], sq = stp[1] + stp[3];
      xrow_sum2(sm, sq);
      const float mean = sm * (1.f / D_);
      rstd = __builtin_amdgcn_rsqf(fmaxf(sq * (1.f / D_) - mean * mean, 0.f) + LN_EPS); nmr = -mean * rstd;
      PIN2(rstd, nmr);
    }
    SB();
#pragma unroll
    for (int t = 0; t < 7; ++t) acc[t] = mfma16(a[ks & 1][t], b[ks], acc[t]);
    SB();
    if (ks == 0) STAMP(2, 3);
    if (ks == 7) STAMP(2, 4);
  }
  STAMP(2, 10);
  u32x2 hv[7];
#pragma unroll
  for (int t = 0; t < 7; ++t) {
    const f32x4 v = acc[t] * rstd + *(const f32x4*)(lds + AL_CS + t * 64 + g * 16) * nmr;
    hv[t] = (u32x2){relu_pk(pk2(v[0], v[1])), relu_pk(pk2(v[2], v[3]))};
  }
  const unsigned hs = lb + QO_H + smul(mt, 56 * KB) + (nt0 >> 1) * KB;
  if ((cb & 1) == 0) {
#pragma unroll
    for (int p = 0; p < 3; ++p) bst4<SC1>((u32x4){hv[2 * p][0], hv[2 * p][1], hv[2 * p + 1][0], hv[2 * p + 1][1]}, rs, l16, hs + p * KB);
    bst2<SC1>(hv[6], rs, l16, hs + 3 * KB);
  } else {
    bst2<SC1>(hv[0], rs, l16 + 8, hs);
#pragma unroll
    for (int p = 0; p < 3; ++p) bst4<SC1>((u32x4){hv[2 * p + 1][0], hv[2 * p + 1][1], hv[2 * p + 2][0], hv[2 * p + 2][1]}, rs, l16, hs + (p + 1) * KB);
  }
  STAMP(2, 11);
  wg_signal(rs, flag_off(layer, 1, (xcd * 2 + rb) * 8 + (cb >> 1)) + (cb & 1) * 8, tid, tag);
}

#define QW_EARLY
DI void ffn2_phase(char* lds, char* ws, rsrc_t rs, u16* out, int layer, int tid, bf16x8 (&stg)[28], u64 tag) {
  const int lane = tid & 63, g = lane >> 4, mm = lane & 15;
  const int w = __builtin_amdgcn_readfirstlane(tid >> 6);
  const int bid = mybid(), xcd = bid & 7, idx = bid >> 3, rb = idx >> 3, cb = idx & 7;
  const int fm = w >> 1, fp = w & 1, mt0 = xcd * 16 + rb * 4, nt0 = cb * 4;
  const unsigned lds0 = (unsigned)(uintptr_t)lds, l16 = lane * 16;
  STAMP(3, 0);
#pragma unroll
  for (int i = 28 - PRE_LATE; i < 28; ++i) ffn2_prefetch(rs, l16, nt0, w, stg, i);
  const unsigned fs = ((mt0 + fm) * 16 + (nt0 >> 1) + fp) * KB;
  const unsigned lb = OFF_QKV + smul(layer, QKV_BYTES);
  const u32x4 xr = bld4<SC1>(rs, l16, lb + QO_X1 + fs);
  if (layer == 0) {
#pragma unroll
    for (int i = 0; i < 6; ++i) { const int c = 6 * w + i; dma(rs, l16, OFF_WQKV + ((4 * cb + (c & 3)) * 16 + (c >> 2)) * KB, lds0 + c * 1024); }
  }
  wave_wait(ws, flag_off(layer, 1, (xcd * 2 + (rb >> 1)) * 8 + w), (lane & 1) * 8, tag);
  STAMP(3, 1);
#define F2_PD 6
  constexpr int PD = F2_PD;
  bf16x8 b[28];
  auto ldb = [&](int i) { b[i] = bld<SC1>(rs, l16, lb + QO_H + smul(mt0 + ((fm + 1 + i / 7) & 3), 56 * KB) + smul(w, 7 * KB) + (i % 7) * KB); };
#pragma unroll
  for (int i = 0; i < PD; ++i) ldb(i);
  SB();
  STAMP(3, 2);
  f32x4 acc[2][4];
  auto send = [&](int mi, int pr) {
    const int d = 2 * ((fm + 1 + mi) & 3) + (pr ^ fp);
    char* xb = lds + AL_W1 + (d * 7 + ((w - d - 1) & 7)) * 2048 + l16;
    *(f32x4*)(xb) = acc[mi & 1][2 * pr]; *(f32x4*)(xb + 1024) = acc[mi & 1][2 * pr + 1];
  };
#pragma unroll
  for (int i = 0; i < 28; ++i) {
    const int mi = i / 7, j = i % 7;
    if (i + PD < 28) ldb(i + PD);
    if (false) {
      const int c = 6 * w + (i - (28 - PD));
      dma(rs, l16, OFF_WQKV + ((4 * cb + (c & 3)) * 16 + (c >> 2)) * KB, lds0 + c * 1024);
    }
    if (mi > 0 && j == 3) send(mi - 1, 0);
    if (mi > 0 && j == 5) send(mi - 1, 1);
    SB();
#pragma unroll
    for (int t = 0; t < 4; ++t) {
      if (j == 0) { const f32x4 z = {}; acc[mi & 1][t] = mfma16(stg[t], b[i], z); }
      else acc[mi & 1][t] = mfma16(stg[4 * j + t], b[i], acc[mi & 1][t]);
    }
    SB();
  }
  STAMP(3, 9);
  send(3, 1);
  f32x4 s0 = acc[1][0] + (f32x4){bflo(xr[0]), bfhi(xr[0]), bflo(xr[1]), bfhi(xr[1])};
  f32x4 s1 = acc[1][1] + (f32x4){bflo(xr[2]), bfhi(xr[2]), bflo(xr[3]), bfhi(xr[3])};
  raw_barrier();
#pragma unroll
  for (int d = 0; d < 7; ++d) {
    const char* xb = lds + AL_W1 + (w * 7 + d) * 2048 + l16;
    s0 += *(const f32x4*)(xb); s1 += *(const f32x4*)(xb + 1024);
  }
  STAMP(3, 10);
  const u32x4 o = {pk2(s0[0], s0[1]), pk2(s0[2], s0[3]), pk2(s1[0], s1[1]), pk2(s1[2], s1[3])};
  if (layer == 1) {
    u16* op = out + (size_t)((mt0 + fm) * 16 + mm) * D_ + (nt0 + 2 * fp) * 16 + 4 * g;
    *(u32x2*)op = (u32x2){o[0], o[1]};
    *(u32x2*)(op + 16) = (u32x2){o[2], o[3]};
    asm volatile("" : "+s"(tag));
    bst2<SC1>((u32x2){(unsigned)tag, (unsigned)(tag >> 32)}, rs, (bid | tid) == 0 ? 0u : OOB, OFF_EPOCH);
    STAMP(3, 11);
  } else {
    bst4<SC1>(o, rs, l16, OFF_X2 + fs);
    STAMP(3, 11);
    wg_signal(rs, flag_off(0, 2, (xcd * 4 + rb) * 2 + (cb >> 2)) + (cb & 3) * 8, tid, tag);
  }
}

typedef short v4i16_t __attribute__((ext_vector_type(4)));
DI bf16x8 tr_frag(const char* lds, int off0, int off1) {
  const v4i16_t lo = __builtin_amdgcn_ds_read_tr16_b64_v4i16((LAS v4i16_t*)((LAS char*)lds + off0));
  const v4i16_t hi = __builtin_amdgcn_ds_read_tr16_b64_v4i16((LAS v4i16_t*)((LAS char*)lds + off1));
  return __builtin_shufflevector(lo, hi, 0, 1, 2, 3, 4, 5, 6, 7);
}
constexpr int QL_KV = 65536, QL_P = 81920, QL_TR = QL_P + 65536;
constexpr int QP_T = 0, QP_KV = 2048, QP_CS = 3072, QP_CK = 3136, QP_ST = 3200;
template <bool FIRST, class H1, class H2, class H3, class TG>
DI void qkv_phase(char* lds, char* ws, rsrc_t rs, int tid, const u16* x, const u16* wq, const u16* wkv, H1 hook1, H2 hook2, H3 hook3, TG gettag) {
  const int lane = tid & 63, g = lane >> 4, mm = lane & 15;
  const int w = __builtin_amdgcn_readfirstlane(tid >> 6);
  const int bid = mybid(), xcd = bid & 7, idx = bid >> 3, rb = idx >> 3, cb = idx & 7;
  const int ml = w & 3, hf = w >> 2, mt = xcd * 16 + rb * 4 + ml;
  const int kvml = cb >> 1;
  const unsigned lds0 = (unsigned)(uintptr_t)lds, l16 = lane * 16;
  const unsigned qkv = OFF_QKV + (FIRST ? 0 : QKV_BYTES);
  char* const mine = lds + QL_P + w * 8192;
  const char* const other = lds + QL_P + (w ^ 4) * 8192;
  STAMP(FIRST ? 5 : 0, 0);
  bf16x8 b[8];
  f32x4 cs0, cs1, csk; float csv;
  if (FIRST) {
    const rsrc_t rw = __builtin_amdgcn_make_buffer_rsrc((void*)wq, 0, D_ * D_ * 2, 0x00020000);
    const rsrc_t rk = __builtin_amdgcn_make_buffer_rsrc((void*)wkv, 0, D_ * 16 * 2, 0x00020000);
    const rsrc_t rx = __builtin_amdgcn_make_buffer_rsrc((void*)x, 0, S_ * D_ * 2, 0x00020000);
#pragma unroll
    for (int i = 0; i < 2; ++i) dma(rk, l16, (2 * w + i) * 1024, lds0 + QL_KV + (2 * w + i) * 1024);
    const unsigned vx = (lane >> 5) * 1024 + (((lane & 31) ^ (lane >> 5)) << 4);
#pragma unroll
    for (int i = 0; i < 8; ++i) dma(rx, vx ^ (i << 5), mt * 16384 + i * 2048 + hf * 512, lds0 + QL_P + w * 8192 + i * 1024);
    const unsigned v0 = (lane >> 3) * 1024 + (((lane & 7) ^ ((lane >> 3) & 2)) << 4), v1 = v0 ^ 64;
#pragma unroll
    for (int i = 0; i < 8; ++i) dma(rw, (i & 1) ? v1 : v0, (8 * w + i) * 8192 + cb * 128, lds0 + (8 * w + i) * 1024);
    wait_vm<0>();
#pragma unroll
    for (int j = 0; j < 8; ++j) b[j] = lds_frag(mine, mm * 512 + (((4 * j + g) ^ mm) << 4));
  } else {
#pragma unroll
    for (int i = 0; i < 2; ++i) {
      const int c = 48 + 2 * w + i;
      dma(rs, l16, OFF_WQKV + ((4 * cb + (c & 3)) * 16 + (c >> 2)) * KB, lds0 + c * 1024);
      dma(rs, l16, OFF_WQKV + ((32 + (cb & 1)) * 16 + 2 * w + i) * KB, lds0 + QL_KV + (2 * w + i) * 1024);
    }
    cs0 = bld4f<SC1>(rs, g * 16, OFF_CSUM + (4 * cb + 2 * hf) * 64); cs1 = bld4f<SC1>(rs, g * 16 + 64, OFF_CSUM + (4 * cb + 2 * hf) * 64);
    csk = bld4f<SC1>(rs, g * 16, OFF_CSUM + 32 * 64);
    csv = __uint_as_float(bld1<SC1>(rs, mm * 4, OFF_CSUM + 33 * 64));
    wave_wait(ws, flag_off(0, 2, (xcd * 4 + rb) * 2 + hf), (lane & 3) * 8, gettag());
#pragma unroll
    for (int j = 0; j < 8; ++j) b[j] = bld<SC1>(rs, l16, OFF_X2 + (mt * 16 + 8 * hf + j) * KB);
  }
  raw_barrier();
  STAMP(FIRST ? 5 : 0, 1);
  hook1();
  const int q4 = (lane >> 2) & 3, p4 = lane & 3;
  const int fsw = ((q4 >> 1) & 1) | ((g & 1) << 1);
  const int abase = FIRST ? hf * 32768 + (8 * g + q4) * 128 + p4 * 8 : hf * 32768 + l16;
  auto aq = [&](int t  ) { return FIRST ? abase + ((t ^ fsw) << 5) : abase + t * 1024; };
  auto ldq = [&](int base, int j) { return FIRST ? tr_frag(lds, base + j * 4096, base + j * 4096 + 512) : lds_frag(lds, base + j * 4096); };
  const int kvb = QL_KV + hf * 8192 + (FIRST ? (8 * g + q4) * 32 + p4 * 8 : l16);
  auto ldkv = [&](int j) { return FIRST ? tr_frag(lds, kvb + j * 1024, kvb + j * 1024 + 128) : lds_frag(lds, kvb + j * 1024); };
  bf16x8 ones; for (int i = 0; i < 8; ++i) ones[i] = 0x3F80;
  if (FIRST) {
    const int cbs = aq(ml);
    f32x4 c0 = {}, c1 = {}, ck = {};
#pragma unroll
    for (int j = 0; j < 8; j += 2) { c0 = mfma16(ldq(cbs, j), ones, c0); c1 = mfma16(ldq(cbs, j + 1), ones, c1); }
    const int kv2 = kvb + ml * 2048;
#pragma unroll
    for (int j = 0; j < 2; ++j) ck = mfma16(FIRST ? tr_frag(lds, kv2 + j * 1024, kv2 + j * 1024 + 128) : lds_frag(lds, kv2 + j * 1024), ones, ck);
    if (mm == 0) { *(f32x4*)(mine + QP_CS + g * 16) = c0 + c1; *(f32x4*)(mine + QP_CK + g * 16) = ck; }
  }
  STAMP(FIRST ? 5 : 0, 2);
  f32x4 acc[4] = {}, accs = {}, accg = {};
  bf16x8 a[2][4];
  const int ab0 = aq(2 * hf), ab1 = aq(2 * hf + 1), ab2 = aq(2 - 2 * hf), ab3 = aq(3 - 2 * hf);
  auto lda = [&](int j) { a[j & 1][0] = ldq(ab0, j); a[j & 1][1] = ldq(ab1, j); a[j & 1][2] = ldq(ab2, j); a[j & 1][3] = ldq(ab3, j); };
  lda(0);
#pragma unroll
  for (int j = 0; j < 8; ++j) {
    if (j < 7) lda(j + 1);
    SB();
    accs = mfma16(ones, b[j], accs); accg = mfma16(b[j], b[j], accg);
#pragma unroll
    for (int t = 0; t < 4; ++t) acc[t] = mfma16(a[j & 1][t], b[j], acc[t]);
    SB();
  }
  f32x4 akv = {};
  if (ml == kvml) {
    if (cb & 1) {
#pragma unroll
      for (int j = 0; j < 8; ++j) akv = mfma16(b[j], ldkv(j), akv);
    } else {
#pragma unroll
      for (int j = 0; j < 8; ++j) akv = mfma16(ldkv(j), b[j], akv);
    }
    *(f32x4*)(mine + QP_KV + l16) = akv;
  }
  STAMP(FIRST ? 5 : 0, 9);
  *(f32x4*)(mine + QP_T + l16) = acc[2];
  *(f32x4*)(mine + QP_T + 1024 + l16) = acc[3];
  {
    const float s01 = (mm & 1) ? accg[1] : accg[0], s23 = (mm & 1) ? accg[3] : accg[2];
    if (g == (mm >> 2)) *(f32x2*)(mine + QP_ST + mm * 8) = (f32x2){accs[0], (mm & 2) ? s23 : s01};
  }
  hook2();
  raw_barrier();
  hook3();
  STAMP(FIRST ? 5 : 0, 10);
  float invd = 1.f / D_; PIN1(invd);
  auto stats = [&](int tokl, float& mean, float& rstd) {
    const f32x2 s = *(const f32x2*)(mine + QP_ST + tokl * 8) + *(const f32x2*)(other + QP_ST + tokl * 8);
    mean = s[0] * invd;
    rstd = __builtin_amdgcn_rsqf(fmaxf(s[1] * invd - mean * mean, 0.f) + LN_EPS);
  };
  float mean, rstd; stats(mm, mean, rstd);
  const char* csp = lds + QL_P + 2 * hf * 8192 + QP_CS + g * 16;
  if (FIRST) {
    cs0 = *(const f32x4*)(csp) + *(const f32x4*)(csp + 32768); cs1 = *(const f32x4*)(csp + 8192) + *(const f32x4*)(csp + 8192 + 32768);
    if (mt == 0) {
      bst4<SC1>(__builtin_bit_cast(u32x4, cs0), rs, mm == 0 ? g * 16 : OOB, OFF_CSUM + (4 * cb + 2 * hf) * 64);
      bst4<SC1>(__builtin_bit_cast(u32x4, cs1), rs, mm == 0 ? g * 16 + 64 : OOB, OFF_CSUM + (4 * cb + 2 * hf) * 64);
    }
  }
  const f32x4 s0 = acc[0] + *(const f32x4*)(other + QP_T + l16), s1 = acc[1] + *(const f32x4*)(other + QP_T + 1024 + l16);
  const float rq = rstd * QSCALE, nmq = -mean * rq;
  const f32x4 q0 = s0 * rq + cs0 * nmq, q1 = s1 * rq + cs1 * nmq;
  const u32x2 o0 = {pk2(q0[0], q0[1]), pk2(q0[2], q0[3])}, o1 = {pk2(q1[0], q1[1]), pk2(q1[2], q1[3])};
  {
    auto s0_ = __builtin_amdgcn_permlane16_swap(o0[0], o1[0], false, false);
    auto s1_ = __builtin_amdgcn_permlane16_swap(o0[1], o1[1], false, false);
    bst4<SC1>((u32x4){s0_[0], s1_[0], s0_[1], s1_[1]}, rs, (mm >> 3) * 8192 + ((g >> 1) * 32 + (2 * hf + (g & 1)) * 8 + (mm & 7)) * 16, qkv + QO_Q + mt * 16384 + cb * 1024);
  }
  float n0 = sq4(o0), n1 = sq4(o1);
  xrow_sum2(n0, n1);
  const unsigned qn = row16_umax(umax(__float_as_uint(n0), __float_as_uint(n1)));
  bst1<SC1>(qn, rs, lane == 0 ? 0u : OOB, qkv + QO_QN2 + mt * 64 + (cb * 2 + hf) * 4);
  if (hf == 0 && ml == kvml) {
    const f32x4 skv = akv + *(const f32x4*)(other + QP_KV + l16);
    if (cb & 1) {
      if (FIRST) {
        csv = 0.f;
#pragma unroll
        for (int i = 0; i < 8; ++i) csv += *(const float*)(lds + QL_P + i * 8192 + QP_CK + mm * 4);
        if (mt == 0) bst1<SC1>(__float_as_uint(csv), rs, g == 0 ? mm * 4 : OOB, OFF_CSUM + 33 * 64);
      }
      float v[4];
#pragma unroll
      for (int i = 0; i < 4; ++i) { float mn, rsd; stats(4 * g + i, mn, rsd); v[i] = (skv[i] - mn * csv) * rsd; }
      const u32x2 o = {pk2(v[0], v[1]), pk2(v[2], v[3])};
      bst2<SC1>(o, rs, (g & 1) * 272 + mm * 16 + (g >> 1) * 8, qkv + QO_VT + smul(mt, 544));
      bst2<SC1>((u32x2){0x3F803F80u, 0x3F803F80u}, rs, mm == 0 ? (g & 1) * 272 + 256 + (g >> 1) * 8 : OOB, qkv + QO_VT + smul(mt, 544));
    } else {
      if (FIRST) {
        csk = (f32x4){};
#pragma unroll
        for (int i = 0; i < 8; ++i) csk += *(const f32x4*)(lds + QL_P + i * 8192 + QP_CK + g * 16);
        if (mt == 0) bst4<SC1>(__builtin_bit_cast(u32x4, csk), rs, mm == 0 ? g * 16 : OOB, OFF_CSUM + 32 * 64);
      }
      const f32x4 k = (skv - csk * mean) * rstd;
      const u32x2 o = {pk2(k[0], k[1]), pk2(k[2], k[3])};
      bst2<SC1>(o, rs, mm * 32 + g * 8, qkv + QO_K + mt * 512);
      const unsigned n2 = row16_umax(__float_as_uint(xrow_sum(sq4(o))));
      bst1<SC1>(n2, rs, lane == 0 ? 0u : OOB, qkv + QO_KN2 + mt * 4);
    }
  }
  STAMP(FIRST ? 5 : 0, 11);
  wg_signal(rs, flag_off(FIRST ? 0 : 1, 3, (xcd * 4 + rb) * 2 + (cb >> 2)) + (cb & 3) * 8, tid, gettag());
}

DI void first_phase(char* lds, char* ws, rsrc_t rs, int tid, const u16* x, const u16* wq, const u16* wk, const u16* wv, const u16* wo,
                    const u16* w1, const u16* w2, u64& tag) {
  const int lane = tid & 63, g = lane >> 4;
  const int w = __builtin_amdgcn_readfirstlane(tid >> 6);
  const unsigned lds0 = (unsigned)(uintptr_t)lds, l16 = lane * 16;
  const int xcd = blockIdx.x & 7, idx = blockIdx.x >> 3, cb = idx & 7;
  u64 ep;
  const u16* tw; int tpitch, tks, tnb; unsigned tdst; int tkst;
  {
    const int bi = blockIdx.x * 4 + (w & 3);
    if (bi < 128) { tw = wo; tpitch = 1024; tks = bi >> 3; tnb = bi & 7; tdst = OFF_WO; tkst = 16; }
    else if (bi < 576) { const int cw = (bi - 128) >> 2, kq = cw / 28; tw = w1; tpitch = 3584; tks = 4 * kq + (w & 3); tnb = cw - 28 * kq; tdst = OFF_W1; tkst = 16; }
    else { const int c = bi - 576; tw = w2; tpitch = 1024; tks = c >> 3; tnb = c & 7; tdst = OFF_W2; tkst = 56; }
  }
  auto fetch = [&]() {
    ep = flag_ld(ws, OFF_EPOCH, 0);
    if (w < 4) {
      const rsrc_t rt = __builtin_amdgcn_make_buffer_rsrc((void*)tw, 0, D_ * DFF * 2, 0x00020000);
      const unsigned v = (lane >> 3) * tpitch + (lane & 7) * 16;
#pragma unroll
      for (int i = 0; i < 4; ++i) dma(rt, v, (32 * tks + 8 * i) * tpitch + tnb * 128, lds0 + QL_TR + w * 4096 + i * 1024);
    }
  };
  auto gettag = [&]() {
    const u64 e = ep + 1ull;
    tag = ((u64)(unsigned)__builtin_amdgcn_readfirstlane((int)(e >> 32)) << 32) | (unsigned)__builtin_amdgcn_readfirstlane((int)e);
    return tag;
  };
  auto convert = [&]() {
    const int q4 = (lane >> 2) & 3, p4 = lane & 3;
    if (w < 4) {
      wait_vm<0>();
      const int base = QL_TR + w * 4096 + (4 * g + q4) * 128 + p4 * 8;
      bf16x8 ones; for (int i = 0; i < 8; ++i) ones[i] = 0x3F80;
#pragma unroll
      for (int t = 0; t < 4; ++t) {
        const bf16x8 f = tr_frag(lds, base + t * 32, base + t * 32 + 2048);
        bst4<SC1>(__builtin_bit_cast(u32x4, f), rs, l16, tdst + ((4 * tnb + t) * tkst + tks) * KB);
        const f32x4 z = {};
        const f32x4 c = mfma16(f, ones, z);
        if ((lane & 15) == 0) *(f32x4*)(lds + QL_P + w * 8192 + 4096 + t * 64 + g * 16) = c;
      }
    } else if (w < 6) {
      const int c = (xcd * 4 + (idx >> 3)) * 2 + (w & 1), ks = c >> 2, t = c & 3;
      const int r0 = 4 * g + q4, f0 = ((r0 >> 1) & 1) | (((r0 >> 3) & 1) << 1);
      const int base = (32 * ks + r0) * 128 + ((t ^ f0) << 5) + p4 * 8;
      bst4<SC1>(__builtin_bit_cast(u32x4, tr_frag(lds, base, base + 2048)), rs, l16, OFF_WQKV + ((4 * cb + t) * 16 + ks) * KB);
    } else if (w == 6) {
      const int ks = xcd * 4 + (idx >> 3);
      if (cb < 2 && ks < 16) {
        const int base = QL_KV + (32 * ks + 4 * g + q4) * 32 + p4 * 8;
        bst4<SC1>(__builtin_bit_cast(u32x4, tr_frag(lds, base, base + 512)), rs, l16, OFF_WQKV + ((32 + cb) * 16 + ks) * KB);
      }
    }
  };
  auto colsum = [&]() {
    if (w == 7 && tdst == OFF_W1) {
      const float* c = (const float*)(lds + QL_P + 4096) + lane;
      bst1<SC1>(__float_as_uint((c[0] + c[2048]) + (c[4096] + c[6144])), rs, lane * 4, OFF_CS1 + (tks >> 2) * 7168 + tnb * 256);
    }
  };
  qkv_phase<true>(lds, ws, rs, tid, x, wq, (cb & 1) ? wv : wk, fetch, []() {}, []() {}, gettag);
  convert();
  raw_barrier();
  colsum();
}

DI int mytid(int w64) { int t; asm volatile("v_mbcnt_lo_u32_b32 %0, -1, %1\n\tv_mbcnt_hi_u32_b32 %0, -1, %0" : "=v"(t) : "s"(w64)); return t; }
__global__ __launch_bounds__(512, 2) void k_main(const u16* __restrict__ x, const u16* __restrict__ wq, const u16* __restrict__ wk, const u16* __restrict__ wv,
                                                 const u16* __restrict__ wo, const u16* __restrict__ w1, const u16* __restrict__ w2,
                                                 char* __restrict__ ws, u16* __restrict__ out) {
  extern __shared__ __attribute__((aligned(16))) char lds[];
  const int w64 = __builtin_amdgcn_readfirstlane(threadIdx.x & ~63);
  const rsrc_t rs = __builtin_amdgcn_make_buffer_rsrc((void*)ws, 0, WS_NEED, 0x00020000);
  u64 tag;
  first_phase(lds, ws, rs, mytid(w64), x, wq, wk, wv, wo, w1, w2, tag);
#pragma unroll 1
  for (int layer = 0; layer < 2; ++layer) {
    bf16x8 stg[28];
    attn_phase(lds, ws, rs, x, layer, mytid(w64), stg, tag);
    ffn1_phase(lds, ws, rs, layer, mytid(w64), stg, tag);
    ffn2_phase(lds, ws, rs, out, layer, mytid(w64), stg, tag);
    if (layer == 0) qkv_phase<false>(lds, ws, rs, mytid(w64), nullptr, nullptr, nullptr, []() {}, []() {}, []() {}, [&]() { return tag; });
  }
}

extern "C" void kernel_launch(void* const* d_in, const int* in_sizes, int n_in, void* d_out, int out_size, void* d_ws, size_t ws_size,
                              hipStream_t stream) {
  const u16* x = (const u16*)d_in[0];
  const u16* wq = (const u16*)d_in[1];
  const u16* wk = (const u16*)d_in[2];
  const u16* wv = (const u16*)d_in[3];
  const u16* wo = (const u16*)d_in[4];
  const u16* w1 = (const u16*)d_in[6];
  const u16* w2 = (const u16*)d_in[7];
  char* ws = (char*)d_ws;
  u16* out = (u16*)d_out;
  (void)hipFuncSetAttribute((const void*)k_main, hipFuncAttributeMaxDynamicSharedMemorySize, LDS_BYTES);
  k_main<<<256, 512, LDS_BYTES, stream>>>(x, wq, wk, wv, wo, w1, w2, ws, out);
}
```

```cpp
#include <hip/hip_runtime.h>
#include <hip/hip_bf16.h>
#include <stdint.h>
#include <type_traits>

typedef __attribute__((ext_vector_type(8))) short bf16x8;
typedef __attribute__((ext_vector_type(2))) float f32x2;
typedef __attribute__((ext_vector_type(4))) float f32x4;
typedef __attribute__((ext_vector_type(16))) float f32x16;
typedef __attribute__((ext_vector_type(4))) unsigned u32x4;
typedef __attribute__((ext_vector_type(2))) unsigned u32x2;
typedef unsigned short u16;
typedef __amdgpu_buffer_rsrc_t rsrc_t;
typedef __attribute__((address_space(1))) unsigned gu32;
#define DI __device__ __forceinline__
#define LAS __attribute__((address_space(3)))

constexpr int S_ = 2048, D_ = 512, DFF = 1792;
constexpr float QSCALE = 0.25f * 1.4426950408889634f;
constexpr float LN_EPS = 1e-5f;

constexpr unsigned KB = 1024;
constexpr unsigned OFF_FLAG = 0;
constexpr unsigned OFF_EPOCH = 128 * KB;
constexpr unsigned FLAG_BYTES = 129 * KB;
constexpr unsigned OFF_WQKV = OFF_FLAG + FLAG_BYTES;
constexpr unsigned OFF_WO   = OFF_WQKV + 544 * KB;
constexpr unsigned OFF_W1   = OFF_WO + 512 * KB;
constexpr unsigned OFF_W2   = OFF_W1 + 1792 * KB;
constexpr unsigned OFF_CSUM = OFF_W2 + 1792 * KB;
constexpr unsigned OFF_CS1  = OFF_CSUM + 4 * KB;
constexpr unsigned OFF_X2   = OFF_CS1 + 28 * KB;
constexpr unsigned OFF_QKV  = OFF_X2 + 2048 * KB;
constexpr unsigned QO_X1 = 0;
constexpr unsigned QO_ST = QO_X1 + 2048 * KB;
constexpr unsigned QO_H  = QO_ST + 128 * KB;
constexpr unsigned QO_Q  = QO_H + 7168 * KB;
constexpr unsigned QO_K = QO_Q + 2048 * KB;
constexpr unsigned QO_VT = QO_K + 64 * KB;
constexpr unsigned QO_KN2 = QO_VT + 68 * KB;
constexpr unsigned QO_QN2 = QO_KN2 + 1 * KB;
constexpr unsigned QKV_BYTES = QO_QN2 + 8 * KB;
constexpr unsigned WS_NEED  = OFF_QKV + 2 * QKV_BYTES;
DI unsigned flag_off(int layer, int edge, int line) { return OFF_FLAG + ((layer * 4 + edge) * 128 + line) * 128; }

DI float bf2f(u16 v) { return __uint_as_float(((unsigned)v) << 16); }
DI float bflo(unsigned v) { return __uint_as_float(v << 16); }
DI float bfhi(unsigned v) { return __uint_as_float(v & 0xffff0000u); }
DI unsigned pk2(float lo, float hi) {
  typedef __attribute__((ext_vector_type(2))) __bf16 bf16x2_;
  f32x2 v = {lo, hi};
  bf16x2_ r = __builtin_convertvector(v, bf16x2_);
  return __builtin_bit_cast(unsigned, r);
}
DI unsigned relu_pk(unsigned v) {
  typedef __attribute__((ext_vector_type(2))) short s16x2;
  const s16x2 z = {0, 0};
  return __builtin_bit_cast(unsigned, __builtin_elementwise_max(__builtin_bit_cast(s16x2, v), z));
}
DI f32x4 mfma16(bf16x8 a, bf16x8 b, f32x4 c) {
  typedef __attribute__((ext_vector_type(8))) __bf16 b8;
  return __builtin_amdgcn_mfma_f32_16x16x32_bf16(__builtin_bit_cast(b8, a), __builtin_bit_cast(b8, b), c, 0, 0, 0);
}
DI f32x16 mfma32(bf16x8 a, bf16x8 b, f32x16 c) {
  typedef __attribute__((ext_vector_type(8))) __bf16 b8;
  return __builtin_amdgcn_mfma_f32_32x32x16_bf16(__builtin_bit_cast(b8, a), __builtin_bit_cast(b8, b), c, 0, 0, 0);
}
template <int N> DI void wait_vm() { asm volatile("s_waitcnt vmcnt(%0)" :: "i"(N) : "memory"); }
DI void raw_barrier() { asm volatile("s_waitcnt lgkmcnt(0)\n\ts_barrier" ::: "memory"); }
DI bf16x8 lds_frag(const char* lds, int off) { return *(const LAS bf16x8*)((const LAS char*)lds + off); }
#define STAMP(kid, i)
DI int kperm(int g, int j) { return (j < 4) ? 4 * g + j : 16 + 4 * g + (j - 4); }

template <int CTRL> DI float dpp(float x) { return __builtin_bit_cast(float, __builtin_amdgcn_mov_dpp(__builtin_bit_cast(int, x), CTRL, 0xf, 0xf, true)); }
template <int CTRL> DI unsigned dppu(unsigned x) { return (unsigned)__builtin_amdgcn_mov_dpp((int)x, CTRL, 0xf, 0xf, true); }
DI unsigned umax(unsigned a, unsigned b) { return a > b ? a : b; }
DI unsigned row16_umax(unsigned x) { x = umax(x, dppu<0xB1>(x)); x = umax(x, dppu<0x4E>(x)); x = umax(x, dppu<0x141>(x)); x = umax(x, dppu<0x140>(x)); return x; }
DI unsigned xrow_umax(unsigned x) {
  auto s = __builtin_amdgcn_permlane16_swap(x, x, false, false); x = umax(s[0], s[1]);
  auto t = __builtin_amdgcn_permlane32_swap(x, x, false, false); return umax(t[0], t[1]); }
DI float xrow_sum(float x) {
  auto s = __builtin_amdgcn_permlane16_swap(__float_as_uint(x), __float_as_uint(x), false, false);
  x = __uint_as_float(s[0]) + __uint_as_float(s[1]);
  auto t = __builtin_amdgcn_permlane32_swap(__float_as_uint(x), __float_as_uint(x), false, false);
  return __uint_as_float(t[0]) + __uint_as_float(t[1]); }
DI void xrow_sum2(float& a, float& b) {
  auto s = __builtin_amdgcn_permlane16_swap(__float_as_uint(a), __float_as_uint(b), false, false);
  const float x = __uint_as_float(s[0]) + __uint_as_float(s[1]);
  auto t = __builtin_amdgcn_permlane32_swap(__float_as_uint(x), __float_as_uint(x), false, false);
  const float y = __uint_as_float(t[0]) + __uint_as_float(t[1]);
  auto u = __builtin_amdgcn_permlane16_swap(__float_as_uint(y), __float_as_uint(y), false, false);
  a = __uint_as_float(u[0]); b = __uint_as_float(u[1]); }
DI float sq4(u32x2 o) { return bflo(o[0]) * bflo(o[0]) + bfhi(o[0]) * bfhi(o[0]) + bflo(o[1]) * bflo(o[1]) + bfhi(o[1]) * bfhi(o[1]); }
#define SB() __builtin_amdgcn_sched_barrier(0)
#define EX(x) __builtin_amdgcn_exp2f(x)
#define PIN1(a) asm volatile("" : "+v"(a))
#define PIN2(a, b) asm volatile("" : "+v"(a), "+v"(b))
#define PIN4(a, b, c, d) asm volatile("" : "+v"(a), "+v"(b), "+v"(c), "+v"(d))
constexpr int SC1 = 16;
DI void mfma16_ip(f32x4& acc, bf16x8 a, bf16x8 b) { asm volatile("v_mfma_f32_16x16x32_bf16 %0, %1, %2, %0" : "+v"(acc) : "v"(a), "v"(b)); }
DI void bld_asm(bf16x8& d, rsrc_t rs, unsigned voff, unsigned soff) { asm volatile("buffer_load_dwordx4 %0, %1, %2, %3 offen sc1" : "+v"(d) : "v"(voff), "s"(rs), "s"(soff)); }
template <int N> DI void mfma16_w(f32x4& acc, bf16x8& a, bf16x8 b) { asm volatile("s_waitcnt vmcnt(%3)\n\tv_mfma_f32_16x16x32_bf16 %0, %1, %2, %0" : "+v"(acc), "+v"(a) : "v"(b), "i"(N)); }
template <int AUX = 0> DI bf16x8 bld(rsrc_t rs, unsigned voff, unsigned soff) { return __builtin_bit_cast(bf16x8, __builtin_amdgcn_raw_buffer_load_b128(rs, voff, soff, AUX)); }
template <int AUX = 0> DI u32x4 bld4(rsrc_t rs, unsigned voff, unsigned soff) { return __builtin_amdgcn_raw_buffer_load_b128(rs, voff, soff, AUX); }
template <int AUX = 0> DI f32x4 bld4f(rsrc_t rs, unsigned voff, unsigned soff) { return __builtin_bit_cast(f32x4, __builtin_amdgcn_raw_buffer_load_b128(rs, voff, soff, AUX)); }
template <int AUX = 0> DI unsigned bld1(rsrc_t rs, unsigned voff, unsigned soff) { return __builtin_amdgcn_raw_buffer_load_b32(rs, voff, soff, AUX); }
template <int AUX = 0> DI void bst4(u32x4 v, rsrc_t rs, unsigned voff, unsigned soff) {
  __builtin_amdgcn_raw_buffer_store_b128(v, rs, voff, soff, AUX);
  asm volatile("s_nop 1" :: "v"(v) : "memory");
}
template <int AUX = 0> DI void bst2(u32x2 v, rsrc_t rs, unsigned voff, unsigned soff) { __builtin_amdgcn_raw_buffer_store_b64(v, rs, voff, soff, AUX); }
template <int AUX = 0> DI void bst1(unsigned v, rsrc_t rs, unsigned voff, unsigned soff) { __builtin_amdgcn_raw_buffer_store_b32(v, rs, voff, soff, AUX); }
constexpr unsigned OOB = 0x7ffffff0u;
DI void dma(rsrc_t rs, unsigned voff, unsigned soff, unsigned lds_dst) {
  unsigned sv;
  asm volatile("s_mov_b32 %0, m0\n\ts_mov_b32 m0, %1\n\ts_nop 0\n\tbuffer_load_dwordx4 %2, %3, %4 offen sc1 lds\n\ts_mov_b32 m0, %0"
               : "=&s"(sv) : "s"(lds_dst), "v"(voff), "s"(rs), "s"(soff) : "memory");
}
typedef unsigned long long u64;
typedef __attribute__((address_space(1))) u64 gu64;
DI void wg_signal(rsrc_t rs, unsigned foff, int tid, u64 tag) {
  asm volatile("s_waitcnt vmcnt(0)" ::: "memory");
  raw_barrier();
  asm volatile("" : "+s"(tag));
  bst2<SC1>((u32x2){(unsigned)tag, (unsigned)(tag >> 32)}, rs, tid == 0 ? 0u : OOB, foff);
}
DI u64 flag_ld(char* ws, unsigned foff, unsigned lane_off) { return __hip_atomic_load((gu64*)(ws + foff + lane_off), __ATOMIC_RELAXED, __HIP_MEMORY_SCOPE_AGENT); }
DI void wave_wait(char* ws, unsigned foff, unsigned lane_off, u64 tag) {
#pragma unroll 1
  for (unsigned spins = 0; spins < (1u << 22); ++spins) {
    if (__all(flag_ld(ws, foff, lane_off) == tag)) break;
    __builtin_amdgcn_s_sleep(1);
  }
  asm volatile("" ::: "memory");
}

DI unsigned smul(unsigned a, unsigned b) { unsigned r; asm("s_mul_i32 %0, %1, %2" : "=s"(r) : "s"(a), "s"(b)); return r; }
DI int mybid() { int b = blockIdx.x; asm volatile("" : "+s"(b)); return b; }

constexpr int LDS_BYTES = 163840;
constexpr int AL_RO = 0;
constexpr int AL_RL = AL_RO + 32768;
constexpr int AL_O  = AL_RL + 2048;
constexpr int AL_Q  = AL_O + 4096;
constexpr int AL_FL = AL_Q + 8192;
constexpr int AL_BND = AL_FL + 256;
constexpr int AL_CS = AL_BND + 512;
constexpr int AL_JUNK = AL_Q;
constexpr int AL_W1 = 49152;
constexpr int AL_MX = AL_W1;
static_assert(AL_CS + 512 <= AL_W1 && AL_W1 + 112 * 1024 <= LDS_BYTES, "lds");
constexpr float SHIFT = 32.f;

DI void dma_w1(rsrc_t rs, unsigned lds0, unsigned l16, int nt0, int w, int t, int kk) {
  dma(rs, l16, OFF_W1 + ((nt0 + t) * 16 + 2 * w + kk) * KB, lds0 + AL_W1 + smul(w, 14336) + (kk * 7 + t) * 1024);
}

DI void ffn2_prefetch(rsrc_t rs, unsigned l16, int nt0, int w, bf16x8 (&stg)[28], int i) {
  const int ti = i & 3;
  stg[i] = bld<SC1>(rs, l16, OFF_W2 + smul(nt0 + ((ti & 2) ^ (2 * (w & 1))), 56 * KB) + smul(w, 7 * KB) + ((ti & 1) * 56 + (i >> 2)) * KB);
}

#define PRE_ATT 0
DI void attn_phase(char* lds, char* ws, rsrc_t rs, const u16* xin, int layer, int tid, bf16x8 (&stg)[28], u64 tag) {
  const int lane = tid & 63, r = lane & 31, h = lane >> 5;
  const int w = __builtin_amdgcn_readfirstlane(tid >> 6);
  const int bid = mybid(), xcd = bid & 7, idx = bid >> 3;
  const int t0 = xcd * 256 + idx * 8;
  const unsigned lds0 = (unsigned)(uintptr_t)lds;
  const unsigned qkv = OFF_QKV + smul(layer, QKV_BYTES);
  const unsigned l16 = lane * 16;
  const int f1_nt0 = smul(idx & 15, 7);
  STAMP(layer ? 1 : 6, 0);
  wave_wait(ws, flag_off(layer, 3, 0), (h == 0 ? w * 8 + (lane >> 2) : (xcd * 4 + (idx >> 3)) * 2 + ((lane >> 2) & 1)) * 128 + (lane & 3) * 8, tag);
  STAMP(layer ? 1 : 6, 3);
  const unsigned qs = qkv + QO_Q + t0 * 1024;
  bf16x8 qf = bld<SC1>(rs, l16, qs);
  SB();
  bf16x8 kf[8], vf[16], qn, qw;
  unsigned kn_a, qn_a, c1p;
  u64 wfl;
  const unsigned kb = qkv + QO_K + w * 8192, vb = qkv + QO_VT + smul(w, 16 * 544);
  const unsigned ko = r * 32 + 16 * h, vo = r <= 16 ? h * 272 + r * 16 : OOB;
  auto load_kv = [&](int t) {
    if (t < 7) kf[t + 1] = bld<SC1>(rs, ko + ((t + 1) & 3) * 1024, kb + ((t + 1) >> 2) * 4096);
    else qn = bld<SC1>(rs, l16 + 1024, qs);
    vf[2 * t] = bld<SC1>(rs, vo, vb + (2 * t) * 544);
    vf[2 * t + 1] = bld<SC1>(rs, vo, vb + (2 * t + 1) * 544);
    asm volatile("" ::: "memory");
    SB();
  };
#define KV_PD 3
  kf[0] = bld<SC1>(rs, ko, kb);
  SB();
#pragma unroll
  for (int t = 0; t < KV_PD; ++t) load_kv(t);
  ((unsigned*)(lds + AL_FL))[lane] = h;
  const unsigned wos = OFF_WO + (4 * w * 16) * KB;
  f32x4 oacc[4] = {};
  f32x16 o;
  char* const ro_w = lds + AL_RO + w * 2048 + lane * 8;
  const int lf_w0 = h == 0 ? AL_RL + (r * 8 + w) * 4 : AL_FL + (w * 4 + (r & 3)) * 4, lf_step = h == 0 ? 1024 : 0;
  const int o_wr = AL_O + (r >> 4) * 1024 + ((2 * ((w & 3) >> 1) + h) * 16 + (w >> 2) * 8 + (r & 7)) * 16 + ((r >> 3) & 1) * 8 + (w & 1) * 4;
  auto publish = [&](int g) {
    char* ro = ro_w + (g & 1) * 16384;
    *(f32x2*)(ro) = (f32x2){o[0], o[1]}; *(f32x2*)(ro + 512) = (f32x2){o[2], o[3]};
    *(f32x2*)(ro + 1024) = (f32x2){o[4], o[5]}; *(f32x2*)(ro + 1536) = (f32x2){o[6], o[7]};
    asm volatile("" ::: "memory");
    *(volatile LAS unsigned*)((LAS char*)lds + lf_w0 + (g & 1) * lf_step) = h == 0 ? __float_as_uint(o[8]) : (unsigned)(g + 1);
  };
  f32x2 ra[4]; f32x4 rl0, rl1;
  const char* const red_r = lds + AL_RO + (w >> 2) * 8192 + (w & 3) * 512 + lane * 8;
  auto reduce_ld = [&](int g) {
#pragma unroll
    for (int i = 0; i < 4; ++i) ra[i] = *(const f32x2*)(red_r + (g & 1) * 16384 + i * 2048);
    rl0 = *(const f32x4*)(lds + AL_RL + (g & 1) * 1024 + r * 32); rl1 = *(const f32x4*)(lds + AL_RL + (g & 1) * 1024 + r * 32 + 16);
  };
  auto reduce_fin = [&](int g) {
    const f32x4 l4 = rl0 + rl1;
    const f32x2 l2 = (f32x2){l4[0], l4[1]} + (f32x2){l4[2], l4[3]};
    const float inv = __builtin_amdgcn_rcpf(l2[0] + l2[1]);
    const f32x2 sa = (ra[0] + ra[1]) + (ra[2] + ra[3]);
    *(unsigned*)(lds + o_wr + (g & 1) * 2048) = pk2(sa[0] * inv, sa[1] * inv);
    asm volatile("" ::: "memory");
    *(volatile LAS unsigned*)((LAS char*)lds + AL_FL + 128 + w * 16 + (lane & 3) * 4) = (unsigned)(g + 2);
  };
  auto reduce = [&](int g) { reduce_ld(g); reduce_fin(g); };
  const int g4 = lane >> 4, mm = lane & 15;
  const unsigned fs = ((t0 >> 4) * 16 + 2 * w) * KB, fv = (mm >> 3) * 1024 + (g4 * 16 + (t0 & 15) + (mm & 7)) * 16;
  u32x4 xr;
  auto load_res = [&]() {
    if (layer == 0) {
      const u16* xp = xin + (size_t)(t0 + (mm & 7)) * D_ + 64 * w + 32 * (mm >> 3) + 4 * g4;
      const u32x2 lo = *(const u32x2*)xp, hi = *(const u32x2*)(xp + 16);
      xr = (u32x4){lo[0], lo[1], hi[0], hi[1]};
    } else xr = bld4<SC1>(rs, fv, OFF_X2 + fs);
  };
  f32x16 cinit;
  { float ns = -SHIFT; PIN1(ns);
#pragma unroll
    for (int i = 0; i < 16; ++i) cinit[i] = ns; }
  PIN1(cinit);
  bf16x8 wof[4], of;
  { unsigned z0 = 0u; PIN1(z0); const u32x4 z = {z0, z0, z0, z0}; wof[0] = wof[1] = wof[2] = wof[3] = __builtin_bit_cast(bf16x8, z); }
  auto load_c1 = [&]() { c1p = bld1<SC1>(rs, (lane >> 4) * 7168 + (lane & 15) * 4, OFF_CS1 + (f1_nt0 + (w < 7 ? w : 6)) * 64); };
  auto store_c1 = [&]() { const float c1 = xrow_sum(__uint_as_float(c1p)); if (lane < 16) ((float*)(lds + AL_CS))[w * 16 + lane] = c1; };
  u32x4 p0, p1;
  unsigned flg;
  const unsigned fl_addr = lds0 + AL_FL + lane * 4;
  STAMP(layer ? 1 : 6, 1);
  f32x16 sc = mfma32(kf[0], qf, cinit);
  const unsigned w1s = OFF_W1 + (f1_nt0 * 16 + 2 * w) * KB, w1d = lds0 + AL_W1 + smul(w, 14336);
  bf16x8 wl[16];
  auto group = [&](auto mode_tag, const int g) {
    constexpr bool FIRST = decltype(mode_tag)::value == 0, SECOND = decltype(mode_tag)::value == 1, LAST = decltype(mode_tag)::value == 3;
    constexpr bool WGT = !FIRST && !SECOND;
#pragma unroll
    for (int t = 0; t < 8; ++t) {
      if (t == 3) __builtin_amdgcn_s_setprio(3); if (t == 5) __builtin_amdgcn_s_setprio(2); if (t == 7) __builtin_amdgcn_s_setprio(1); if (t == 1) __builtin_amdgcn_s_setprio(0);
      f32x16 sn;
      if (!(LAST && t == 7)) sn = mfma32(kf[(t + 1) & 7], t < 7 ? qf : qn, cinit);
      SB();
      if (FIRST) {
        if (t + KV_PD < 8) load_kv(t + KV_PD);
        if (t == 8 - KV_PD || (KV_PD == 8 && t == 0)) {
          qw = bld<SC1>(rs, l16, qs + w * 1024);
          kn_a = bld1<SC1>(rs, (lane & 15) * 4, qkv + QO_KN2 + w * 64);
          qn_a = bld1<SC1>(rs, (lane & 15) * 4, qkv + QO_QN2 + (t0 >> 4) * 64);
          asm volatile("" ::: "memory");
          SB();
        }
      }
      float e0 = EX(sc[0]), e1 = EX(sc[1]), e2 = EX(sc[2]), e3 = EX(sc[3]), e4 = EX(sc[4]), e5 = EX(sc[5]);
      PIN4(e0, e1, e2, e3); PIN2(e4, e5); SB();
      if (t > 0 || !FIRST) o = mfma32(vf[(2 * t + 15) & 15], __builtin_bit_cast(bf16x8, p1), o);
      SB();
      float e6 = EX(sc[6]), e7 = EX(sc[7]);
      PIN2(e6, e7); SB();
      p0[0] = pk2(e0, e1); p0[1] = pk2(e2, e3); p0[2] = pk2(e4, e5); p0[3] = pk2(e6, e7);
      PIN1(p0); SB();
      float e8 = EX(sc[8]), e9 = EX(sc[9]);
      PIN2(e8, e9); SB();
      float e10, e11, e12, e13;
      if (t == 0) {
        if (!FIRST) { e10 = EX(sc[10]); e11 = EX(sc[11]); e12 = EX(sc[12]); e13 = EX(sc[13]); PIN4(e10, e11, e12, e13); PIN1(o); SB(); publish(g - 1); SB(); }
        f32x16 z = {}; o = mfma32(vf[0], __builtin_bit_cast(bf16x8, p0), z);
      } else o = mfma32(vf[2 * t], __builtin_bit_cast(bf16x8, p0), o);
      SB();
      if (t > 0 || FIRST) { e10 = EX(sc[10]); e11 = EX(sc[11]); e12 = EX(sc[12]); e13 = EX(sc[13]); PIN4(e10, e11, e12, e13); SB(); }
      if (WGT) {
        constexpr int X = LAST ? 2 + PRE_ATT / 8 : 0, D = LAST ? 0 : 1;
        if (t == 0) mfma16_w<3>(oacc[0], wof[0], of); else if (t == 1) mfma16_w<3 + X>(oacc[1], wof[1], of);
        else if (t == 2) mfma16_w<3 + D + 2 * X>(oacc[2], wof[2], of); else if (t == 3) mfma16_w<3 + 2 * D + 3 * X>(oacc[3], wof[3], of);
        else if (t == 4) mfma16_w<3 + 3 * D + 4 * X>(oacc[0], wof[0], of); else if (t == 5) mfma16_w<3 + 3 * D + 4 * X>(oacc[1], wof[1], of);
        else if (t == 6) mfma16_w<3 + 2 * D + 4 * X>(oacc[2], wof[2], of); else mfma16_w<3 + D + 4 * X>(oacc[3], wof[3], of);
        SB();
      }
      float e14 = EX(sc[14]), e15 = EX(sc[15]);
      PIN2(e14, e15); SB();
      p1[0] = pk2(e8, e9); p1[1] = pk2(e10, e11); p1[2] = pk2(e12, e13); p1[3] = pk2(e14, e15);
      PIN1(p1); SB();
      if (WGT) {
        bld_asm(wof[t & 3], rs, l16, wos + ((t & 3) * 16 + 2 * (g - 2) + (t >> 2)) * 1024);
        SB();
      }
      if (LAST) {
#pragma unroll
        for (int i = 2 * t; i < 2 * t + 2; ++i) { wl[i] = bld<SC1>(rs, l16, wos + ((i & 3) * 16 + 12 + (i >> 2)) * 1024); asm volatile("" ::: "memory"); }
        if (PRE_ATT) { ffn2_prefetch(rs, l16, (idx & 7) * 4, w, stg, t); asm volatile("" ::: "memory"); }
        SB();
      }
      if (WGT && !LAST && t >= 1 && t <= 3) {
        const int pc = min(3 * (g - 2) + (t - 1), 13);
        dma(rs, l16, w1s + (pc >> 1) * 16 * KB + (pc & 1) * KB, w1d + (pc >> 1) * 1024 + ((pc & 1) ? 7168 : 0));
        SB();
      }
      if (SECOND && t == 5) { wfl = flag_ld(ws, flag_off(1, 2, w * 8), (r >> 2) * 128 + (lane & 3) * 8); SB(); }
      if (!FIRST && t == 1) { flg = *(volatile LAS unsigned*)((LAS char*)lds + AL_FL + lane * 4); SB(); }
      if (!FIRST && t == 2) {
        asm volatile("1:\n\tv_cmp_gt_u32 vcc, %2, %0\n\ts_cbranch_vccz 2f\n\ts_sleep 1\n\tds_read_b32 %0, %1\n\ts_waitcnt lgkmcnt(0)\n\ts_branch 1b\n2:"
                     : "+v"(flg) : "v"(fl_addr), "s"(g) : "vcc", "memory");
        reduce_ld(g - 1);
        SB();
      }
      if (!FIRST && t == 3) {
        reduce_fin(g - 1);
        SB();
      }
      if (WGT && (t & 3) == 3) { of = lds_frag(lds, AL_O + (g & 1) * 2048 + (t >> 2) * 1024 + l16); SB(); }
      if (!FIRST && !LAST && t == 5) { qn = lds_frag(lds, AL_Q + (g + 1) * 1024 + l16); SB(); }
      if (!(LAST && t == 7)) sc = sn;
    }
    qf = qn;
  };
  group(std::integral_constant<int, 0>{}, 0);
  *(LAS bf16x8*)((LAS char*)lds + AL_Q + w * 1024 + l16) = qw;
  ((float*)(lds + AL_BND))[w * 16 + (lane & 15)] = __uint_as_float(kn_a) * __uint_as_float(row16_umax(qn_a));
  wg_signal(rs, flag_off(1, 2, (xcd * 4 + (idx >> 3)) * 2 + ((idx >> 2) & 1)) + (idx & 3) * 8, tid, tag);
  auto weights_ready = [&]() { wave_wait(ws, flag_off(1, 2, w * 8), (r >> 2) * 128 + (lane & 3) * 8, tag); raw_barrier(); };
  const f32x2 kq2 = *(const f32x2*)(lds + AL_BND + lane * 8);
  const bool slow = !__all(kq2[0] <= SHIFT * SHIFT * 0.998f && kq2[1] <= SHIFT * SHIFT * 0.998f);
  of = wof[0];
  STAMP(layer ? 1 : 6, 2);
  if (!slow) {
    group(std::integral_constant<int, 1>{}, 1);
    if (!__all(wfl == tag)) wave_wait(ws, flag_off(1, 2, w * 8), (r >> 2) * 128 + (lane & 3) * 8, tag);
    raw_barrier();
#pragma unroll 1
    for (int g = 2; g < 7; ++g) group(std::integral_constant<int, 2>{}, g);
    group(std::integral_constant<int, 3>{}, 7);
    __builtin_amdgcn_s_setprio(0);
    o = mfma32(vf[15], __builtin_bit_cast(bf16x8, p1), o);
    SB();
    load_res(); load_c1();
    SB();
    publish(7);
    raw_barrier();
    STAMP(layer ? 1 : 6, 9);
    reduce_ld(7);
    bf16x8 of0 = lds_frag(lds, AL_O + l16), of1 = lds_frag(lds, AL_O + 1024 + l16);
    SB();
#pragma unroll
    for (int t = 0; t < 4; ++t) mfma16_w<4>(oacc[t], wof[t], of);
    SB();
    reduce_fin(7);
    SB();
#pragma unroll
    for (int t = 0; t < 4; ++t) mfma16_ip(oacc[t], wl[t], of0);
#pragma unroll
    for (int t = 0; t < 4; ++t) mfma16_ip(oacc[t], wl[4 + t], of1);
    store_c1();
    raw_barrier();
    STAMP(layer ? 1 : 6, 10);
    of0 = lds_frag(lds, AL_O + 2048 + l16); of1 = lds_frag(lds, AL_O + 3072 + l16);
#pragma unroll
    for (int t = 0; t < 4; ++t) mfma16_ip(oacc[t], wl[8 + t], of0);
#pragma unroll
    for (int t = 0; t < 4; ++t) mfma16_ip(oacc[t], wl[12 + t], of1);
    asm volatile("s_nop 7" : "+v"(oacc[0]), "+v"(oacc[1]), "+v"(oacc[2]), "+v"(oacc[3]));
  } else {
    __builtin_amdgcn_s_setprio(0);
    weights_ready();
    float* mx = (float*)(lds + AL_MX);
    const f32x16 z = {};
#pragma unroll 1
    for (int g = 0; g < 8; ++g) {
      const bf16x8 q = lds_frag(lds, AL_Q + g * 1024 + l16);
      float m = -INFINITY;
#pragma unroll
      for (int t = 0; t < 8; ++t) {
        const f32x16 s = mfma32(kf[t], q, z);
#pragma unroll
        for (int i = 0; i < 16; ++i) m = fmaxf(m, s[i]);
        SB();
      }
      { auto sw = __builtin_amdgcn_permlane32_swap(__float_as_uint(m), __float_as_uint(m), false, false); m = fmaxf(__uint_as_float(sw[0]), __uint_as_float(sw[1])); }
      if (h == 0) mx[w * 256 + g * 32 + r] = m;
    }
    __syncthreads();
#pragma unroll 1
    for (int g = 0; g < 8; ++g) {
      const bf16x8 q = lds_frag(lds, AL_Q + g * 1024 + l16);
      float m = mx[g * 32 + r];
#pragma unroll
      for (int i = 1; i < 8; ++i) m = fmaxf(m, mx[i * 256 + g * 32 + r]);
      o = z;
#pragma unroll
      for (int t = 0; t < 8; ++t) {
        const f32x16 s = mfma32(kf[t], q, z);
        u32x4 a0, a1;
#pragma unroll
        for (int i = 0; i < 4; ++i) {
          a0[i] = pk2(EX(s[2 * i] - m), EX(s[2 * i + 1] - m));
          a1[i] = pk2(EX(s[8 + 2 * i] - m), EX(s[9 + 2 * i] - m));
        }
        o = mfma32(vf[2 * t], __builtin_bit_cast(bf16x8, a0), o);
        o = mfma32(vf[2 * t + 1], __builtin_bit_cast(bf16x8, a1), o);
        SB();
      }
      publish(g);
      __syncthreads();
      reduce(g);
      __syncthreads();
      const bf16x8 b0 = lds_frag(lds, AL_O + (g & 1) * 2048 + l16), b1 = lds_frag(lds, AL_O + (g & 1) * 2048 + 1024 + l16);
#pragma unroll
      for (int t = 0; t < 4; ++t) {
        oacc[t] = mfma16(bld<SC1>(rs, l16, wos + (t * 16 + 2 * g) * 1024), b0, oacc[t]);
        oacc[t] = mfma16(bld<SC1>(rs, l16, wos + (t * 16 + 2 * g + 1) * 1024), b1, oacc[t]);
      }
    }
    load_res(); load_c1(); store_c1();
#pragma unroll
    for (int i = 0; i < PRE_ATT; ++i) ffn2_prefetch(rs, l16, (idx & 7) * 4, w, stg, i);
#pragma unroll 1
    for (int i = 0; i < 7; ++i) { dma_w1(rs, lds0, l16, f1_nt0, w, i, 0); dma_w1(rs, lds0, l16, f1_nt0, w, i, 1); }
    asm volatile("s_nop 7" : "+v"(oacc[0]), "+v"(oacc[1]), "+v"(oacc[2]), "+v"(oacc[3]));
  }
  float xv[8];
  asm volatile("s_nop 1\n\t"
               "v_add_f32_dpp %0, %8, %8 row_ror:8 row_mask:0xf bank_mask:0x3\n\t"
               "v_add_f32_dpp %1, %9, %9 row_ror:8 row_mask:0xf bank_mask:0x3\n\t"
               "v_add_f32_dpp %2, %10, %10 row_ror:8 row_mask:0xf bank_mask:0x3\n\t"
               "v_add_f32_dpp %3, %11, %11 row_ror:8 row_mask:0xf bank_mask:0x3\n\t"
               "v_add_f32_dpp %4, %12, %12 row_ror:8 row_mask:0xf bank_mask:0x3\n\t"
               "v_add_f32_dpp %5, %13, %13 row_ror:8 row_mask:0xf bank_mask:0x3\n\t"
               "v_add_f32_dpp %6, %14, %14 row_ror:8 row_mask:0xf bank_mask:0x3\n\t"
               "v_add_f32_dpp %7, %15, %15 row_ror:8 row_mask:0xf bank_mask:0x3\n\t"
               "v_add_f32_dpp %0, %16, %16 row_ror:8 row_mask:0xf bank_mask:0xc\n\t"
               "v_add_f32_dpp %1, %17, %17 row_ror:8 row_mask:0xf bank_mask:0xc\n\t"
               "v_add_f32_dpp %2, %18, %18 row_ror:8 row_mask:0xf bank_mask:0xc\n\t"
               "v_add_f32_dpp %3, %19, %19 row_ror:8 row_mask:0xf bank_mask:0xc\n\t"
               "v_add_f32_dpp %4, %20, %20 row_ror:8 row_mask:0xf bank_mask:0xc\n\t"
               "v_add_f32_dpp %5, %21, %21 row_ror:8 row_mask:0xf bank_mask:0xc\n\t"
               "v_add_f32_dpp %6, %22, %22 row_ror:8 row_mask:0xf bank_mask:0xc\n\t"
               "v_add_f32_dpp %7, %23, %23 row_ror:8 row_mask:0xf bank_mask:0xc\n\t"
               : "=&v"(xv[0]), "=&v"(xv[1]), "=&v"(xv[2]), "=&v"(xv[3]), "=&v"(xv[4]), "=&v"(xv[5]), "=&v"(xv[6]), "=&v"(xv[7])
               : "v"(oacc[0][0]), "v"(oacc[0][1]), "v"(oacc[0][2]), "v"(oacc[0][3]), "v"(oacc[1][0]), "v"(oacc[1][1]), "v"(oacc[1][2]), "v"(oacc[1][3]), "v"(oacc[2][0]), "v"(oacc[2][1]), "v"(oacc[2][2]), "v"(oacc[2][3]), "v"(oacc[3][0]), "v"(oacc[3][1]), "v"(oacc[3][2]), "v"(oacc[3][3]));
  u32x4 ov;
  float sm = 0.f, sq = 0.f;
#pragma unroll
  for (int q = 0; q < 4; ++q) {
    const float a = xv[2 * q] + bflo(xr[q]), b = xv[2 * q + 1] + bfhi(xr[q]);
    ov[q] = pk2(a, b);
    sm += a + b; sq = fmaf(a, a, fmaf(b, b, sq));
  }
  bst4<SC1>(ov, rs, fv, qkv + QO_X1 + fs);
  sm += dpp<0x128>(sm); sq += dpp<0x128>(sq);
  xrow_sum2(sm, sq);
  bst2<SC1>((u32x2){__float_as_uint(sm), __float_as_uint(sq)}, rs, lane < 8 ? lane * 64 : OOB, qkv + QO_ST + t0 * 64 + w * 8);
  STAMP(layer ? 1 : 6, 11);
  wg_signal(rs, flag_off(layer, 0, t0 >> 4) + (t0 & 8), tid, tag);
}

DI void ffn1_phase(char* lds, char* ws, rsrc_t rs, int layer, int tid, bf16x8 (&stg)[28], u64 tag) {
  const int lane = tid & 63, g = lane >> 4, mm = lane & 15;
  const int w = __builtin_amdgcn_readfirstlane(tid >> 6);
  const int bid = mybid(), xcd = bid & 7, idx = bid >> 3, rb = idx >> 4, cb = idx & 15;
  const int mt = xcd * 16 + rb * 8 + w, nt0 = smul(cb, 7);
  const unsigned l16 = lane * 16;
  STAMP(2, 0);
#define PRE_EARLY 8
#define PRE_LATE 4
#pragma unroll
  for (int i = PRE_ATT; i < PRE_EARLY; ++i) ffn2_prefetch(rs, l16, (idx & 7) * 4, w, stg, i);
  bf16x8 a[2][7];
#pragma unroll
  for (int t = 0; t < 7; ++t) a[0][t] = lds_frag(lds, AL_W1 + t * 1024 + l16);
  wave_wait(ws, flag_off(layer, 0, mt), (lane & 1) * 8, tag);
  STAMP(2, 1);
  constexpr int PD = 5;
  bf16x8 b[16];
  const unsigned lb = OFF_QKV + smul(layer, QKV_BYTES), bs = lb + QO_X1 + mt * 16 * KB;
#pragma unroll
  for (int ks = 0; ks < PD; ++ks) b[ks] = bld<SC1>(rs, l16, bs + ks * KB);
  const f32x4 stp = bld4f<SC1>(rs, mm * 64 + g * 16, lb + QO_ST + mt * 1024);
  SB();
  STAMP(2, 2);
  f32x4 acc[7] = {};
  float rstd, nmr;
#pragma unroll
  for (int ks = 0; ks < 16; ++ks) {
    if (ks < 15) {
#pragma unroll
      for (int t = 0; t < 7; ++t) a[(ks + 1) & 1][t] = lds_frag(lds, AL_W1 + ((ks + 1) * 7 + t) * 1024 + l16);
    }
    if (ks + PD < 16) b[ks + PD] = bld<SC1>(rs, l16, bs + (ks + PD) * KB);
    {
      constexpr int NMID = 28 - PRE_EARLY - PRE_LATE;
      const int lo = ks <= 10 ? (ks * 6) / 11 : 6 + (ks - 11) * 2, hi = ks + 1 <= 10 ? ((ks + 1) * 6) / 11 : 6 + (ks + 1 - 11) * 2;
#pragma unroll
      for (int i = lo; i < hi; ++i) if (i < NMID) ffn2_prefetch(rs, l16, (idx & 7) * 4, w, stg, PRE_EARLY + i);
    }
    if (ks == 8) {
      float sm = stp[0] + stp[2], sq = stp[1] + stp[3];
      xrow_sum2(sm, sq);
      const float mean = sm * (1.f / D_);
      rstd = __builtin_amdgcn_rsqf(fmaxf(sq * (1.f / D_) - mean * mean, 0.f) + LN_EPS); nmr = -mean * rstd;
      PIN2(rstd, nmr);
    }
    SB();
#pragma unroll
    for (int t = 0; t < 7; ++t) acc[t] = mfma16(a[ks & 1][t], b[ks], acc[t]);
    SB();
    if (ks == 0) STAMP(2, 3);
    if (ks == 7) STAMP(2, 4);
  }
  STAMP(2, 10);
  u32x2 hv[7];
#pragma unroll
  for (int t = 0; t < 7; ++t) {
    const f32x4 v = acc[t] * rstd + *(const f32x4*)(lds + AL_CS + t * 64 + g * 16) * nmr;
    hv[t] = (u32x2){relu_pk(pk2(v[0], v[1])), relu_pk(pk2(v[2], v[3]))};
  }
  const unsigned hs = lb + QO_H + smul(mt, 56 * KB) + (nt0 >> 1) * KB;
  if ((cb & 1) == 0) {
#pragma unroll
    for (int p = 0; p < 3; ++p) bst4<SC1>((u32x4){hv[2 * p][0], hv[2 * p][1], hv[2 * p + 1][0], hv[2 * p + 1][1]}, rs, l16, hs + p * KB);
    bst2<SC1>(hv[6], rs, l16, hs + 3 * KB);
  } else {
    bst2<SC1>(hv[0], rs, l16 + 8, hs);
#pragma unroll
    for (int p = 0; p < 3; ++p) bst4<SC1>((u32x4){hv[2 * p + 1][0], hv[2 * p + 1][1], hv[2 * p + 2][0], hv[2 * p + 2][1]}, rs, l16, hs + (p + 1) * KB);
  }
  STAMP(2, 11);
  wg_signal(rs, flag_off(layer, 1, (xcd * 2 + rb) * 8 + (cb >> 1)) + (cb & 1) * 8, tid, tag);
}

#define QW_EARLY
#define CODE_PF 30720
DI void ffn2_phase(char* lds, char* ws, rsrc_t rs, u16* out, int layer, int tid, bf16x8 (&stg)[28], u64 tag) {
  const int lane = tid & 63, g = lane >> 4, mm = lane & 15;
  const int w = __builtin_amdgcn_readfirstlane(tid >> 6);
  const int bid = mybid(), xcd = bid & 7, idx = bid >> 3, rb = idx >> 3, cb = idx & 7;
  const int fm = w >> 1, fp = w & 1, mt0 = xcd * 16 + rb * 4, nt0 = cb * 4;
  const unsigned lds0 = (unsigned)(uintptr_t)lds, l16 = lane * 16;
  STAMP(3, 0);
#pragma unroll
  for (int i = 28 - PRE_LATE; i < 28; ++i) ffn2_prefetch(rs, l16, nt0, w, stg, i);
  const unsigned fs = ((mt0 + fm) * 16 + (nt0 >> 1) + fp) * KB;
  const unsigned lb = OFF_QKV + smul(layer, QKV_BYTES);
  const u32x4 xr = bld4<SC1>(rs, l16, lb + QO_X1 + fs);
  if (layer == 0) {
#pragma unroll
    for (int i = 0; i < 6; ++i) { const int c = 6 * w + i; dma(rs, l16, OFF_WQKV + ((4 * cb + (c & 3)) * 16 + (c >> 2)) * KB, lds0 + c * 1024); }
  }
  wave_wait(ws, flag_off(layer, 1, (xcd * 2 + (rb >> 1)) * 8 + w), (lane & 1) * 8, tag);
  STAMP(3, 1);
#define F2_PD 6
  constexpr int PD = F2_PD;
  bf16x8 b[28];
  auto ldb = [&](int i) { b[i] = bld<SC1>(rs, l16, lb + QO_H + smul(mt0 + ((fm + 1 + i / 7) & 3), 56 * KB) + smul(w, 7 * KB) + (i % 7) * KB); };
#pragma unroll
  for (int i = 0; i < PD; ++i) ldb(i);
  SB();
  STAMP(3, 2);
  f32x4 acc[2][4];
  auto send = [&](int mi, int pr) {
    const int d = 2 * ((fm + 1 + mi) & 3) + (pr ^ fp);
    char* xb = lds + AL_W1 + (d * 7 + ((w - d - 1) & 7)) * 2048 + l16;
    *(f32x4*)(xb) = acc[mi & 1][2 * pr]; *(f32x4*)(xb + 1024) = acc[mi & 1][2 * pr + 1];
  };
#pragma unroll
  for (int i = 0; i < 28; ++i) {
    const int mi = i / 7, j = i % 7;
    if (i + PD < 28) ldb(i + PD);
    if (false) {
      const int c = 6 * w + (i - (28 - PD));
      dma(rs, l16, OFF_WQKV + ((4 * cb + (c & 3)) * 16 + (c >> 2)) * KB, lds0 + c * 1024);
    }
    if (mi > 0 && j == 3) send(mi - 1, 0);
    if (mi > 0 && j == 5) send(mi - 1, 1);
    SB();
#pragma unroll
    for (int t = 0; t < 4; ++t) {
      if (j == 0) { const f32x4 z = {}; acc[mi & 1][t] = mfma16(stg[t], b[i], z); }
      else acc[mi & 1][t] = mfma16(stg[4 * j + t], b[i], acc[mi & 1][t]);
    }
    SB();
  }
  STAMP(3, 9);
  send(3, 1);
  f32x4 s0 = acc[1][0] + (f32x4){bflo(xr[0]), bfhi(xr[0]), bflo(xr[1]), bfhi(xr[1])};
  f32x4 s1 = acc[1][1] + (f32x4){bflo(xr[2]), bfhi(xr[2]), bflo(xr[3]), bfhi(xr[3])};
  raw_barrier();
#pragma unroll
  for (int d = 0; d < 7; ++d) {
    const char* xb = lds + AL_W1 + (w * 7 + d) * 2048 + l16;
    s0 += *(const f32x4*)(xb); s1 += *(const f32x4*)(xb + 1024);
  }
  STAMP(3, 10);
  const u32x4 o = {pk2(s0[0], s0[1]), pk2(s0[2], s0[3]), pk2(s1[0], s1[1]), pk2(s1[2], s1[3])};
  if (layer == 1) {
    u16* op = out + (size_t)((mt0 + fm) * 16 + mm) * D_ + (nt0 + 2 * fp) * 16 + 4 * g;
    *(u32x2*)op = (u32x2){o[0], o[1]};
    *(u32x2*)(op + 16) = (u32x2){o[2], o[3]};
    asm volatile("" : "+s"(tag));
    bst2<SC1>((u32x2){(unsigned)tag, (unsigned)(tag >> 32)}, rs, (bid | tid) == 0 ? 0u : OOB, OFF_EPOCH);
    STAMP(3, 11);
  } else {
    bst4<SC1>(o, rs, l16, OFF_X2 + fs);
    STAMP(3, 11);
    wg_signal(rs, flag_off(0, 2, (xcd * 4 + rb) * 2 + (cb >> 2)) + (cb & 3) * 8, tid, tag);
  }
}

typedef short v4i16_t __attribute__((ext_vector_type(4)));
DI bf16x8 tr_frag(const char* lds, int off0, int off1) {
  const v4i16_t lo = __builtin_amdgcn_ds_read_tr16_b64_v4i16((LAS v4i16_t*)((LAS char*)lds + off0));
  const v4i16_t hi = __builtin_amdgcn_ds_read_tr16_b64_v4i16((LAS v4i16_t*)((LAS char*)lds + off1));
  return __builtin_shufflevector(lo, hi, 0, 1, 2, 3, 4, 5, 6, 7);
}
constexpr int QL_KV = 65536, QL_P = 81920, QL_TR = QL_P + 65536;
constexpr int QP_T = 0, QP_KV = 2048, QP_CS = 3072, QP_CK = 3136, QP_ST = 3200;
template <bool FIRST, class H1, class H2, class H3, class TG>
DI void qkv_phase(char* lds, char* ws, rsrc_t rs, int tid, const u16* x, const u16* wq, const u16* wkv, H1 hook1, H2 hook2, H3 hook3, TG gettag) {
  const int lane = tid & 63, g = lane >> 4, mm = lane & 15;
  const int w = __builtin_amdgcn_readfirstlane(tid >> 6);
  const int bid = mybid(), xcd = bid & 7, idx = bid >> 3, rb = idx >> 3, cb = idx & 7;
  const int ml = w & 3, hf = w >> 2, mt = xcd * 16 + rb * 4 + ml;
  const int kvml = cb >> 1;
  const unsigned lds0 = (unsigned)(uintptr_t)lds, l16 = lane * 16;
  const unsigned qkv = OFF_QKV + (FIRST ? 0 : QKV_BYTES);
  char* const mine = lds + QL_P + w * 8192;
  const char* const other = lds + QL_P + (w ^ 4) * 8192;
  STAMP(FIRST ? 5 : 0, 0);
  bf16x8 b[8];
  f32x4 cs0, cs1, csk; float csv;
  if (FIRST) {
    const rsrc_t rw = __builtin_amdgcn_make_buffer_rsrc((void*)wq, 0, D_ * D_ * 2, 0x00020000);
    const rsrc_t rk = __builtin_amdgcn_make_buffer_rsrc((void*)wkv, 0, D_ * 16 * 2, 0x00020000);
    const rsrc_t rx = __builtin_amdgcn_make_buffer_rsrc((void*)x, 0, S_ * D_ * 2, 0x00020000);
#pragma unroll
    for (int i = 0; i < 2; ++i) dma(rk, l16, (2 * w + i) * 1024, lds0 + QL_KV + (2 * w + i) * 1024);
    const unsigned vx = (lane >> 5) * 1024 + (((lane & 31) ^ (lane >> 5)) << 4);
#pragma unroll
    for (int i = 0; i < 8; ++i) dma(rx, vx ^ (i << 5), mt * 16384 + i * 2048 + hf * 512, lds0 + QL_P + w * 8192 + i * 1024);
    const unsigned v0 = (lane >> 3) * 1024 + (((lane & 7) ^ ((lane >> 3) & 2)) << 4), v1 = v0 ^ 64;
#pragma unroll
    for (int i = 0; i < 8; ++i) dma(rw, (i & 1) ? v1 : v0, (8 * w + i) * 8192 + cb * 128, lds0 + (8 * w + i) * 1024);
    wait_vm<0>();
#pragma unroll
    for (int j = 0; j < 8; ++j) b[j] = lds_frag(mine, mm * 512 + (((4 * j + g) ^ mm) << 4));
  } else {
#pragma unroll
    for (int i = 0; i < 2; ++i) {
      const int c = 48 + 2 * w + i;
      dma(rs, l16, OFF_WQKV + ((4 * cb + (c & 3)) * 16 + (c >> 2)) * KB, lds0 + c * 1024);
      dma(rs, l16, OFF_WQKV + ((32 + (cb & 1)) * 16 + 2 * w + i) * KB, lds0 + QL_KV + (2 * w + i) * 1024);
    }
    cs0 = bld4f<SC1>(rs, g * 16, OFF_CSUM + (4 * cb + 2 * hf) * 64); cs1 = bld4f<SC1>(rs, g * 16 + 64, OFF_CSUM + (4 * cb + 2 * hf) * 64);
    csk = bld4f<SC1>(rs, g * 16, OFF_CSUM + 32 * 64);
    csv = __uint_as_float(bld1<SC1>(rs, mm * 4, OFF_CSUM + 33 * 64));
    wave_wait(ws, flag_off(0, 2, (xcd * 4 + rb) * 2 + hf), (lane & 3) * 8, gettag());
#pragma unroll
    for (int j = 0; j < 8; ++j) b[j] = bld<SC1>(rs, l16, OFF_X2 + (mt * 16 + 8 * hf + j) * KB);
  }
  raw_barrier();
  STAMP(FIRST ? 5 : 0, 1);
  hook1();
  const int q4 = (lane >> 2) & 3, p4 = lane & 3;
  const int fsw = ((q4 >> 1) & 1) | ((g & 1) << 1);
  const int abase = FIRST ? hf * 32768 + (8 * g + q4) * 128 + p4 * 8 : hf * 32768 + l16;
  auto aq = [&](int t  ) { return FIRST ? abase + ((t ^ fsw) << 5) : abase + t * 1024; };
  auto ldq = [&](int base, int j) { return FIRST ? tr_frag(lds, base + j * 4096, base + j * 4096 + 512) : lds_frag(lds, base + j * 4096); };
  const int kvb = QL_KV + hf * 8192 + (FIRST ? (8 * g + q4) * 32 + p4 * 8 : l16);
  auto ldkv = [&](int j) { return FIRST ? tr_frag(lds, kvb + j * 1024, kvb + j * 1024 + 128) : lds_frag(lds, kvb + j * 1024); };
  bf16x8 ones; for (int i = 0; i < 8; ++i) ones[i] = 0x3F80;
  if (FIRST) {
    const int cbs = aq(ml);
    f32x4 c0 = {}, c1 = {}, ck = {};
#pragma unroll
    for (int j = 0; j < 8; j += 2) { c0 = mfma16(ldq(cbs, j), ones, c0); c1 = mfma16(ldq(cbs, j + 1), ones, c1); }
    const int kv2 = kvb + ml * 2048;
#pragma unroll
    for (int j = 0; j < 2; ++j) ck = mfma16(FIRST ? tr_frag(lds, kv2 + j * 1024, kv2 + j * 1024 + 128) : lds_frag(lds, kv2 + j * 1024), ones, ck);
    if (mm == 0) { *(f32x4*)(mine + QP_CS + g * 16) = c0 + c1; *(f32x4*)(mine + QP_CK + g * 16) = ck; }
  }
  STAMP(FIRST ? 5 : 0, 2);
  f32x4 acc[4] = {}, accs = {}, accg = {};
  bf16x8 a[2][4];
  const int ab0 = aq(2 * hf), ab1 = aq(2 * hf + 1), ab2 = aq(2 - 2 * hf), ab3 = aq(3 - 2 * hf);
  auto lda = [&](int j) { a[j & 1][0] = ldq(ab0, j); a[j & 1][1] = ldq(ab1, j); a[j & 1][2] = ldq(ab2, j); a[j & 1][3] = ldq(ab3, j); };
  lda(0);
#pragma unroll
  for (int j = 0; j < 8; ++j) {
    if (j < 7) lda(j + 1);
    SB();
    accs = mfma16(ones, b[j], accs); accg = mfma16(b[j], b[j], accg);
#pragma unroll
    for (int t = 0; t < 4; ++t) acc[t] = mfma16(a[j & 1][t], b[j], acc[t]);
    SB();
  }
  f32x4 akv = {};
  if (ml == kvml) {
    if (cb & 1) {
#pragma unroll
      for (int j = 0; j < 8; ++j) akv = mfma16(b[j], ldkv(j), akv);
    } else {
#pragma unroll
      for (int j = 0; j < 8; ++j) akv = mfma16(ldkv(j), b[j], akv);
    }
    *(f32x4*)(mine + QP_KV + l16) = akv;
  }
  STAMP(FIRST ? 5 : 0, 9);
  *(f32x4*)(mine + QP_T + l16) = acc[2];
  *(f32x4*)(mine + QP_T + 1024 + l16) = acc[3];
  {
    const float s01 = (mm & 1) ? accg[1] : accg[0], s23 = (mm & 1) ? accg[3] : accg[2];
    if (g == (mm >> 2)) *(f32x2*)(mine + QP_ST + mm * 8) = (f32x2){accs[0], (mm & 2) ? s23 : s01};
  }
  hook2();
  raw_barrier();
  hook3();
  STAMP(FIRST ? 5 : 0, 10);
  float invd = 1.f / D_; PIN1(invd);
  auto stats = [&](int tokl, float& mean, float& rstd) {
    const f32x2 s = *(const f32x2*)(mine + QP_ST + tokl * 8) + *(const f32x2*)(other + QP_ST + tokl * 8);
    mean = s[0] * invd;
    rstd = __builtin_amdgcn_rsqf(fmaxf(s[1] * invd - mean * mean, 0.f) + LN_EPS);
  };
  float mean, rstd; stats(mm, mean, rstd);
  const char* csp = lds + QL_P + 2 * hf * 8192 + QP_CS + g * 16;
  if (FIRST) {
    cs0 = *(const f32x4*)(csp) + *(const f32x4*)(csp + 32768); cs1 = *(const f32x4*)(csp + 8192) + *(const f32x4*)(csp + 8192 + 32768);
    if (mt == 0) {
      bst4<SC1>(__builtin_bit_cast(u32x4, cs0), rs, mm == 0 ? g * 16 : OOB, OFF_CSUM + (4 * cb + 2 * hf) * 64);
      bst4<SC1>(__builtin_bit_cast(u32x4, cs1), rs, mm == 0 ? g * 16 + 64 : OOB, OFF_CSUM + (4 * cb + 2 * hf) * 64);
    }
  }
  const f32x4 s0 = acc[0] + *(const f32x4*)(other + QP_T + l16), s1 = acc[1] + *(const f32x4*)(other + QP_T + 1024 + l16);
  const float rq = rstd * QSCALE, nmq = -mean * rq;
  const f32x4 q0 = s0 * rq + cs0 * nmq, q1 = s1 * rq + cs1 * nmq;
  const u32x2 o0 = {pk2(q0[0], q0[1]), pk2(q0[2], q0[3])}, o1 = {pk2(q1[0], q1[1]), pk2(q1[2], q1[3])};
  {
    auto s0_ = __builtin_amdgcn_permlane16_swap(o0[0], o1[0], false, false);
    auto s1_ = __builtin_amdgcn_permlane16_swap(o0[1], o1[1], false, false);
    bst4<SC1>((u32x4){s0_[0], s1_[0], s0_[1], s1_[1]}, rs, (mm >> 3) * 8192 + ((g >> 1) * 32 + (2 * hf + (g & 1)) * 8 + (mm & 7)) * 16, qkv + QO_Q + mt * 16384 + cb * 1024);
  }
  float n0 = sq4(o0), n1 = sq4(o1);
  xrow_sum2(n0, n1);
  const unsigned qn = row16_umax(umax(__float_as_uint(n0), __float_as_uint(n1)));
  bst1<SC1>(qn, rs, lane == 0 ? 0u : OOB, qkv + QO_QN2 + mt * 64 + (cb * 2 + hf) * 4);
  if (hf == 0 && ml == kvml) {
    const f32x4 skv = akv + *(const f32x4*)(other + QP_KV + l16);
    if (cb & 1) {
      if (FIRST) {
        csv = 0.f;
#pragma unroll
        for (int i = 0; i < 8; ++i) csv += *(const float*)(lds + QL_P + i * 8192 + QP_CK + mm * 4);
        if (mt == 0) bst1<SC1>(__float_as_uint(csv), rs, g == 0 ? mm * 4 : OOB, OFF_CSUM + 33 * 64);
      }
      float v[4];
#pragma unroll
      for (int i = 0; i < 4; ++i) { float mn, rsd; stats(4 * g + i, mn, rsd); v[i] = (skv[i] - mn * csv) * rsd; }
      const u32x2 o = {pk2(v[0], v[1]), pk2(v[2], v[3])};
      bst2<SC1>(o, rs, (g & 1) * 272 + mm * 16 + (g >> 1) * 8, qkv + QO_VT + smul(mt, 544));
      bst2<SC1>((u32x2){0x3F803F80u, 0x3F803F80u}, rs, mm == 0 ? (g & 1) * 272 + 256 + (g >> 1) * 8 : OOB, qkv + QO_VT + smul(mt, 544));
    } else {
      if (FIRST) {
        csk = (f32x4){};
#pragma unroll
        for (int i = 0; i < 8; ++i) csk += *(const f32x4*)(lds + QL_P + i * 8192 + QP_CK + g * 16);
        if (mt == 0) bst4<SC1>(__builtin_bit_cast(u32x4, csk), rs, mm == 0 ? g * 16 : OOB, OFF_CSUM + 32 * 64);
      }
      const f32x4 k = (skv - csk * mean) * rstd;
      const u32x2 o = {pk2(k[0], k[1]), pk2(k[2], k[3])};
      bst2<SC1>(o, rs, mm * 32 + g * 8, qkv + QO_K + mt * 512);
      const unsigned n2 = row16_umax(__float_as_uint(xrow_sum(sq4(o))));
      bst1<SC1>(n2, rs, lane == 0 ? 0u : OOB, qkv + QO_KN2 + mt * 4);
    }
  }
  STAMP(FIRST ? 5 : 0, 11);
  wg_signal(rs, flag_off(FIRST ? 0 : 1, 3, (xcd * 4 + rb) * 2 + (cb >> 2)) + (cb & 3) * 8, tid, gettag());
}

DI void first_phase(char* lds, char* ws, rsrc_t rs, int tid, const u16* x, const u16* wq, const u16* wk, const u16* wv, const u16* wo,
                    const u16* w1, const u16* w2, u64& tag) {
  const int lane = tid & 63, g = lane >> 4;
  const int w = __builtin_amdgcn_readfirstlane(tid >> 6);
  const unsigned lds0 = (unsigned)(uintptr_t)lds, l16 = lane * 16;
  const int xcd = blockIdx.x & 7, idx = blockIdx.x >> 3, cb = idx & 7;
  u64 ep;
  const u16* tw; int tpitch, tks, tnb; unsigned tdst; int tkst;
  {
    const int bi = blockIdx.x * 4 + (w & 3);
    if (bi < 128) { tw = wo; tpitch = 1024; tks = bi >> 3; tnb = bi & 7; tdst = OFF_WO; tkst = 16; }
    else if (bi < 576) { const int cw = (bi - 128) >> 2, kq = cw / 28; tw = w1; tpitch = 3584; tks = 4 * kq + (w & 3); tnb = cw - 28 * kq; tdst = OFF_W1; tkst = 16; }
    else { const int c = bi - 576; tw = w2; tpitch = 1024; tks = c >> 3; tnb = c & 7; tdst = OFF_W2; tkst = 56; }
  }
  auto fetch = [&]() {
    ep = flag_ld(ws, OFF_EPOCH, 0);
    if (w == 7) {
      u64 pc; asm volatile("s_getpc_b64 %0" : "=s"(pc));
      const rsrc_t rc = __builtin_amdgcn_make_buffer_rsrc((void*)pc, 0, CODE_PF, 0x00020000);
      unsigned sv;
#pragma unroll
      for (int i = 0; i < (CODE_PF + 8191) / 8192; ++i)
        asm volatile("s_mov_b32 %0, m0\n\ts_mov_b32 m0, %1\n\ts_nop 0\n\tbuffer_load_dword %2, %3, %4 offen lds\n\ts_mov_b32 m0, %0"
                     : "=&s"(sv) : "s"(lds0 + QL_P + 7 * 8192 + 6144), "v"(lane * 128), "s"(rc), "s"(i * 8192) : "memory");
    }
    if (w < 4) {
      const rsrc_t rt = __builtin_amdgcn_make_buffer_rsrc((void*)tw, 0, D_ * DFF * 2, 0x00020000);
      const unsigned v = (lane >> 3) * tpitch + (lane & 7) * 16;
#pragma unroll
      for (int i = 0; i < 4; ++i) dma(rt, v, (32 * tks + 8 * i) * tpitch + tnb * 128, lds0 + QL_TR + w * 4096 + i * 1024);
    }
  };
  auto gettag = [&]() {
    const u64 e = ep + 1ull;
    tag = ((u64)(unsigned)__builtin_amdgcn_readfirstlane((int)(e >> 32)) << 32) | (unsigned)__builtin_amdgcn_readfirstlane((int)e);
    return tag;
  };
  auto convert = [&]() {
    const int q4 = (lane >> 2) & 3, p4 = lane & 3;
    if (w < 4) {
      wait_vm<0>();
      const int base = QL_TR + w * 4096 + (4 * g + q4) * 128 + p4 * 8;
      bf16x8 ones; for (int i = 0; i < 8; ++i) ones[i] = 0x3F80;
#pragma unroll
      for (int t = 0; t < 4; ++t) {
        const bf16x8 f = tr_frag(lds, base + t * 32, base + t * 32 + 2048);
        bst4<SC1>(__builtin_bit_cast(u32x4, f), rs, l16, tdst + ((4 * tnb + t) * tkst + tks) * KB);
        const f32x4 z = {};
        const f32x4 c = mfma16(f, ones, z);
        if ((lane & 15) == 0) *(f32x4*)(lds + QL_P + w * 8192 + 4096 + t * 64 + g * 16) = c;
      }
    } else if (w < 6) {
      const int c = (xcd * 4 + (idx >> 3)) * 2 + (w & 1), ks = c >> 2, t = c & 3;
      const int r0 = 4 * g + q4, f0 = ((r0 >> 1) & 1) | (((r0 >> 3) & 1) << 1);
      const int base = (32 * ks + r0) * 128 + ((t ^ f0) << 5) + p4 * 8;
      bst4<SC1>(__builtin_bit_cast(u32x4, tr_frag(lds, base, base + 2048)), rs, l16, OFF_WQKV + ((4 * cb + t) * 16 + ks) * KB);
    } else if (w == 6) {
      const int ks = xcd * 4 + (idx >> 3);
      if (cb < 2 && ks < 16) {
        const int base = QL_KV + (32 * ks + 4 * g + q4) * 32 + p4 * 8;
        bst4<SC1>(__builtin_bit_cast(u32x4, tr_frag(lds, base, base + 512)), rs, l16, OFF_WQKV + ((32 + cb) * 16 + ks) * KB);
      }
    }
  };
  auto colsum = [&]() {
    if (w == 7 && tdst == OFF_W1) {
      const float* c = (const float*)(lds + QL_P + 4096) + lane;
      bst1<SC1>(__float_as_uint((c[0] + c[2048]) + (c[4096] + c[6144])), rs, lane * 4, OFF_CS1 + (tks >> 2) * 7168 + tnb * 256);
    }
  };
  qkv_phase<true>(lds, ws, rs, tid, x, wq, (cb & 1) ? wv : wk, fetch, []() {}, []() {}, gettag);
  convert();
  raw_barrier();
  colsum();
}

DI int mytid(int w64) { int t; asm volatile("v_mbcnt_lo_u32_b32 %0, -1, %1\n\tv_mbcnt_hi_u32_b32 %0, -1, %0" : "=v"(t) : "s"(w64)); return t; }
__global__ __launch_bounds__(512, 2) void k_main(const u16* __restrict__ x, const u16* __restrict__ wq, const u16* __restrict__ wk, const u16* __restrict__ wv,
                                                 const u16* __restrict__ wo, const u16* __restrict__ w1, const u16* __restrict__ w2,
                                                 char* __restrict__ ws, u16* __restrict__ out) {
  extern __shared__ __attribute__((aligned(16))) char lds[];
  const int w64 = __builtin_amdgcn_readfirstlane(threadIdx.x & ~63);
  const rsrc_t rs = __builtin_amdgcn_make_buffer_rsrc((void*)ws, 0, WS_NEED, 0x00020000);
  u64 tag;
  first_phase(lds, ws, rs, mytid(w64), x, wq, wk, wv, wo, w1, w2, tag);
#pragma unroll 1
  for (int layer = 0; layer < 2; ++layer) {
    bf16x8 stg[28];
    attn_phase(lds, ws, rs, x, layer, mytid(w64), stg, tag);
    ffn1_phase(lds, ws, rs, layer, mytid(w64), stg, tag);
    ffn2_phase(lds, ws, rs, out, layer, mytid(w64), stg, tag);
    if (layer == 0) qkv_phase<false>(lds, ws, rs, mytid(w64), nullptr, nullptr, nullptr, []() {}, []() {}, []() {}, [&]() { return tag; });
  }
}

extern "C" void kernel_launch(void* const* d_in, const int* in_sizes, int n_in, void* d_out, int out_size, void* d_ws, size_t ws_size,
                              hipStream_t stream) {
  const u16* x = (const u16*)d_in[0];
  const u16* wq = (const u16*)d_in[1];
  const u16* wk = (const u16*)d_in[2];
  const u16* wv = (const u16*)d_in[3];
  const u16* wo = (const u16*)d_in[4];
  const u16* w1 = (const u16*)d_in[6];
  const u16* w2 = (const u16*)d_in[7];
  char* ws = (char*)d_ws;
  u16* out = (u16*)d_out;
  (void)hipFuncSetAttribute((const void*)k_main, hipFuncAttributeMaxDynamicSharedMemorySize, LDS_BYTES);
  k_main<<<256, 512, LDS_BYTES, stream>>>(x, wq, wk, wv, wo, w1, w2, ws, out);
}
```

```cpp
#include <hip/hip_runtime.h>
#include <hip/hip_bf16.h>
#include <stdint.h>
#include <type_traits>

typedef __attribute__((ext_vector_type(8))) short bf16x8;
typedef __attribute__((ext_vector_type(2))) float f32x2;
typedef __attribute__((ext_vector_type(4))) float f32x4;
typedef __attribute__((ext_vector_type(16))) float f32x16;
typedef __attribute__((ext_vector_type(4))) unsigned u32x4;
typedef __attribute__((ext_vector_type(2))) unsigned u32x2;
typedef unsigned short u16;
typedef __amdgpu_buffer_rsrc_t rsrc_t;
#define DI __device__ __forceinline__
#define LAS __attribute__((address_space(3)))

constexpr int S_ = 2048, D_ = 512, DFF = 1792;
constexpr float QSCALE = 0.25f * 1.4426950408889634f;
constexpr float LN_EPS = 1e-5f;

constexpr unsigned KB = 1024;
constexpr unsigned OFF_FLAG = 0;
constexpr unsigned OFF_EPOCH = 128 * KB;
constexpr unsigned FLAG_BYTES = 129 * KB;
constexpr unsigned OFF_WQKV = OFF_FLAG + FLAG_BYTES;
constexpr unsigned OFF_WO   = OFF_WQKV + 544 * KB;
constexpr unsigned OFF_W1   = OFF_WO + 512 * KB;
constexpr unsigned OFF_W2   = OFF_W1 + 1792 * KB;
constexpr unsigned OFF_CSUM = OFF_W2 + 1792 * KB;
constexpr unsigned OFF_CS1  = OFF_CSUM + 4 * KB;
constexpr unsigned OFF_X2   = OFF_CS1 + 28 * KB;
constexpr unsigned OFF_QKV  = OFF_X2 + 2048 * KB;
constexpr unsigned QO_X1 = 0;
constexpr unsigned QO_ST = QO_X1 + 2048 * KB;
constexpr unsigned QO_H  = QO_ST + 256 * KB;
constexpr unsigned QO_Q  = QO_H + 7168 * KB;
constexpr unsigned QO_K = QO_Q + 2048 * KB;
constexpr unsigned QO_VT = QO_K + 64 * KB;
constexpr unsigned QO_KN2 = QO_VT + 80 * KB;
constexpr unsigned QO_QN2 = QO_KN2 + 16 * KB;
constexpr unsigned QKV_BYTES = QO_QN2 + 256 * KB;
constexpr unsigned WS_NEED  = OFF_QKV + 2 * QKV_BYTES;
DI unsigned flag_off(int layer, int edge, int line) { return OFF_FLAG + ((layer * 4 + edge) * 128 + line) * 128; }

DI float bflo(unsigned v) { return __uint_as_float(v << 16); }
DI float bfhi(unsigned v) { return __uint_as_float(v & 0xffff0000u); }
DI unsigned pk2(float lo, float hi) {
  typedef __attribute__((ext_vector_type(2))) __bf16 bf16x2_;
  f32x2 v = {lo, hi};
  bf16x2_ r = __builtin_convertvector(v, bf16x2_);
  return __builtin_bit_cast(unsigned, r);
}
DI unsigned relu_pk(unsigned v) {
  typedef __attribute__((ext_vector_type(2))) short s16x2;
  const s16x2 z = {0, 0};
  return __builtin_bit_cast(unsigned, __builtin_elementwise_max(__builtin_bit_cast(s16x2, v), z));
}
DI f32x4 mfma16(bf16x8 a, bf16x8 b, f32x4 c) {
  typedef __attribute__((ext_vector_type(8))) __bf16 b8;
  return __builtin_amdgcn_mfma_f32_16x16x32_bf16(__builtin_bit_cast(b8, a), __builtin_bit_cast(b8, b), c, 0, 0, 0);
}
DI f32x16 mfma32(bf16x8 a, bf16x8 b, f32x16 c) {
  typedef __attribute__((ext_vector_type(8))) __bf16 b8;
  return __builtin_amdgcn_mfma_f32_32x32x16_bf16(__builtin_bit_cast(b8, a), __builtin_bit_cast(b8, b), c, 0, 0, 0);
}
template <int N> DI void wait_vm() { asm volatile("s_waitcnt vmcnt(%0)" :: "i"(N) : "memory"); }
DI void raw_barrier() { asm volatile("s_waitcnt lgkmcnt(0)\n\ts_barrier" ::: "memory"); }
DI bf16x8 lds_frag(const char* lds, int off) { return *(const LAS bf16x8*)((const LAS char*)lds + off); }

template <int CTRL> DI float dpp(float x) { return __builtin_bit_cast(float, __builtin_amdgcn_mov_dpp(__builtin_bit_cast(int, x), CTRL, 0xf, 0xf, true)); }
template <int CTRL> DI unsigned dppu(unsigned x) { return (unsigned)__builtin_amdgcn_mov_dpp((int)x, CTRL, 0xf, 0xf, true); }
DI unsigned umax(unsigned a, unsigned b) { return a > b ? a : b; }
DI unsigned row16_umax(unsigned x) { x = umax(x, dppu<0xB1>(x)); x = umax(x, dppu<0x4E>(x)); x = umax(x, dppu<0x141>(x)); x = umax(x, dppu<0x140>(x)); return x; }
DI float xrow_sum(float x) {
  auto s = __builtin_amdgcn_permlane16_swap(__float_as_uint(x), __float_as_uint(x), false, false);
  x = __uint_as_float(s[0]) + __uint_as_float(s[1]);
  auto t = __builtin_amdgcn_permlane32_swap(__float_as_uint(x), __float_as_uint(x), false, false);
  return __uint_as_float(t[0]) + __uint_as_float(t[1]); }
DI void xrow_sum2(float& a, float& b) {
  auto s = __builtin_amdgcn_permlane16_swap(__float_as_uint(a), __float_as_uint(b), false, false);
  const float x = __uint_as_float(s[0]) + __uint_as_float(s[1]);
  auto t = __builtin_amdgcn_permlane32_swap(__float_as_uint(x), __float_as_uint(x), false, false);
  const float y = __uint_as_float(t[0]) + __uint_as_float(t[1]);
  auto u = __builtin_amdgcn_permlane16_swap(__float_as_uint(y), __float_as_uint(y), false, false);
  a = __uint_as_float(u[0]); b = __uint_as_float(u[1]); }
DI float sq4(u32x2 o) { return bflo(o[0]) * bflo(o[0]) + bfhi(o[0]) * bfhi(o[0]) + bflo(o[1]) * bflo(o[1]) + bfhi(o[1]) * bfhi(o[1]); }
#define SB() __builtin_amdgcn_sched_barrier(0)
#define EX(x) __builtin_amdgcn_exp2f(x)
#define PIN1(a) asm volatile("" : "+v"(a))
#define PIN2(a, b) asm volatile("" : "+v"(a), "+v"(b))
#define PIN4(a, b, c, d) asm volatile("" : "+v"(a), "+v"(b), "+v"(c), "+v"(d))
constexpr int SC1 = 16;
DI void mfma16_ip(f32x4& acc, bf16x8 a, bf16x8 b) { asm volatile("v_mfma_f32_16x16x32_bf16 %0, %1, %2, %0" : "+v"(acc) : "v"(a), "v"(b)); }
template <int IMM> DI void bld_asm(bf16x8& d, rsrc_t rs, unsigned voff, unsigned soff) { asm volatile("buffer_load_dwordx4 %0, %1, %2, %3 offen offset:%4 sc1" : "+v"(d) : "v"(voff), "s"(rs), "s"(soff), "i"(IMM)); }
template <int N> DI void mfma16_w(f32x4& acc, bf16x8& a, bf16x8 b) { asm volatile("s_waitcnt vmcnt(%3)\n\tv_mfma_f32_16x16x32_bf16 %0, %1, %2, %0" : "+v"(acc), "+v"(a) : "v"(b), "i"(N)); }
template <int AUX = 0> DI bf16x8 bld(rsrc_t rs, unsigned voff, unsigned soff) { return __builtin_bit_cast(bf16x8, __builtin_amdgcn_raw_buffer_load_b128(rs, voff, soff, AUX)); }
template <int AUX = 0> DI u32x4 bld4(rsrc_t rs, unsigned voff, unsigned soff) { return __builtin_amdgcn_raw_buffer_load_b128(rs, voff, soff, AUX); }
template <int AUX = 0> DI f32x4 bld4f(rsrc_t rs, unsigned voff, unsigned soff) { return __builtin_bit_cast(f32x4, __builtin_amdgcn_raw_buffer_load_b128(rs, voff, soff, AUX)); }
template <int AUX = 0> DI unsigned bld1(rsrc_t rs, unsigned voff, unsigned soff) { return __builtin_amdgcn_raw_buffer_load_b32(rs, voff, soff, AUX); }
template <int AUX = 0> DI void bst4(u32x4 v, rsrc_t rs, unsigned voff, unsigned soff) {
  __builtin_amdgcn_raw_buffer_store_b128(v, rs, voff, soff, AUX);
  asm volatile("s_nop 1" :: "v"(v) : "memory");
}
template <int AUX = 0> DI void bst2(u32x2 v, rsrc_t rs, unsigned voff, unsigned soff) { __builtin_amdgcn_raw_buffer_store_b64(v, rs, voff, soff, AUX); }
template <int AUX = 0> DI void bst1(unsigned v, rsrc_t rs, unsigned voff, unsigned soff) { __builtin_amdgcn_raw_buffer_store_b32(v, rs, voff, soff, AUX); }
constexpr unsigned OOB = 0x7ffffff0u;
DI void dma(rsrc_t rs, unsigned voff, unsigned soff, unsigned lds_dst) {
  asm volatile("s_mov_b32 m0, %0\n\ts_nop 0\n\tbuffer_load_dwordx4 %1, %2, %3 offen sc1 lds"
               :: "s"(lds_dst), "v"(voff), "s"(rs), "s"(soff) : "m0", "memory");
}
typedef unsigned long long u64;
typedef __attribute__((address_space(1))) u64 gu64;
DI void wg_signal(rsrc_t rs, unsigned foff, int tid, u64 tag) {
  asm volatile("s_waitcnt vmcnt(0)" ::: "memory");
  raw_barrier();
  asm volatile("" : "+s"(tag));
  bst2<SC1>((u32x2){(unsigned)tag, (unsigned)(tag >> 32)}, rs, tid == 0 ? 0u : OOB, foff);
}
DI u64 flag_ld(char* ws, unsigned foff, unsigned lane_off) { return __hip_atomic_load((gu64*)(ws + foff + lane_off), __ATOMIC_RELAXED, __HIP_MEMORY_SCOPE_AGENT); }
DI void wave_wait(char* ws, unsigned foff, unsigned lane_off, u64 tag) {
#pragma unroll 1
  for (unsigned spins = 0; spins < (1u << 17); ++spins) {
    if (__all(flag_ld(ws, foff, lane_off) == tag)) break;
    __builtin_amdgcn_s_sleep(1);
  }
  asm volatile("" ::: "memory");
}

DI unsigned smul(unsigned a, unsigned b) { unsigned r; asm("s_mul_i32 %0, %1, %2" : "=s"(r) : "s"(a), "s"(b)); return r; }
DI int mybid() { int b = blockIdx.x; asm volatile("" : "+s"(b)); return b; }

constexpr int CODE_PF = 30720, PF_EARLY = 2;

constexpr int LDS_BYTES = 163840;
constexpr int AL_RO = 0;
constexpr int AL_RL = AL_RO + 32768;
constexpr int AL_O  = AL_RL + 2048;
constexpr int AL_Q  = AL_O + 4096;
constexpr int AL_FL = AL_Q + 8192;
constexpr int AL_BND = AL_FL + 256;
constexpr int AL_CS = AL_BND + 512;
constexpr int AL_W1 = 49152;
constexpr int AL_MX = AL_W1;
static_assert(AL_CS + 512 <= AL_W1 && AL_W1 + 112 * 1024 <= LDS_BYTES, "lds");
constexpr float SHIFT = 32.f;

constexpr int PRE_EARLY = 8, PRE_LATE = 4;
DI void ffn2_prefetch(rsrc_t rs, unsigned l16, int nt0, int w, bf16x8 (&stg)[28], int i) {
  const int ti = i & 3;
  stg[i] = bld<SC1>(rs, l16, OFF_W2 + smul(nt0 + ((ti & 2) ^ (2 * (w & 1))), 56 * KB) + smul(w, 7 * KB) + ((ti & 1) * 56 + (i >> 2)) * KB);
}

DI void attn_phase(char* lds, char* ws, rsrc_t rs, const u16* xin, int layer, int tid, u64 tag) {
  const int lane = tid & 63, r = lane & 31, h = lane >> 5;
  const int w = __builtin_amdgcn_readfirstlane(tid >> 6);
  const int bid = mybid(), xcd = bid & 7, idx = bid >> 3;
  const int t0 = xcd * 256 + idx * 8;
  const unsigned lds0 = (unsigned)(uintptr_t)lds;
  const unsigned qkv = OFF_QKV + smul(layer, QKV_BYTES);
  const unsigned l16 = lane * 16;
  const int f1_nt0 = smul(idx & 15, 7);
  wave_wait(ws, flag_off(layer, 3, 0), (h == 0 ? w * 8 + (lane >> 2) : 64 + (xcd * 4 + (idx >> 3)) * 2 + ((lane >> 2) & 1)) * 128 + (lane & 3) * 8, tag);
  const unsigned qs = qkv + QO_Q + t0 * 1024;
  bf16x8 qf = bld<SC1>(rs, l16, qs);
  SB();
  bf16x8 kf[8], vf[16], qn, qw;
  unsigned kn_a, qn_a, c1p;
  u64 wfl;
  const unsigned kb = qkv + QO_K + w * 8192, vb = qkv + QO_VT + smul(w, 16 * 640);
  const unsigned ko = r * 32 + 16 * h, vo = r <= 16 ? h * 272 + r * 16 : OOB;
  auto load_kv = [&](int t) {
    if (t < 7) kf[t + 1] = bld<SC1>(rs, ko + ((t + 1) & 3) * 1024, kb + ((t + 1) >> 2) * 4096);
    else qn = bld<SC1>(rs, l16 + 1024, qs);
    vf[2 * t] = bld<SC1>(rs, vo, vb + (2 * t) * 640);
    vf[2 * t + 1] = bld<SC1>(rs, vo, vb + (2 * t + 1) * 640);
    asm volatile("" ::: "memory");
    SB();
  };
  constexpr int KV_PD = 3;
  kf[0] = bld<SC1>(rs, ko, kb);
  SB();
#pragma unroll
  for (int t = 0; t < KV_PD; ++t) load_kv(t);
  ((unsigned*)(lds + AL_FL))[lane] = h;
  const unsigned wos = OFF_WO + w * 64 * KB;
  f32x4 oacc[4] = {};
  f32x16 o;
  char* const ro_w = lds + AL_RO + w * 2048 + lane * 8;
  const int lf_w0 = h == 0 ? AL_RL + (r * 8 + w) * 4 : AL_FL + (w * 4 + (r & 3)) * 4, lf_step = h == 0 ? 1024 : 0;
  const int o_wr = AL_O + (r >> 4) * 1024 + ((2 * ((w & 3) >> 1) + h) * 16 + (w >> 2) * 8 + (r & 7)) * 16 + ((r >> 3) & 1) * 8 + (w & 1) * 4;
  auto publish = [&](int g) {
    char* ro = ro_w + (g & 1) * 16384;
    *(f32x2*)(ro) = (f32x2){o[0], o[1]}; *(f32x2*)(ro + 512) = (f32x2){o[2], o[3]};
    *(f32x2*)(ro + 1024) = (f32x2){o[4], o[5]}; *(f32x2*)(ro + 1536) = (f32x2){o[6], o[7]};
    asm volatile("" ::: "memory");
    *(volatile LAS unsigned*)((LAS char*)lds + lf_w0 + (g & 1) * lf_step) = h == 0 ? __float_as_uint(o[8]) : (unsigned)(g + 1);
  };
  f32x2 ra[4]; f32x4 rl0, rl1;
  const char* const red_r = lds + AL_RO + (w >> 2) * 8192 + (w & 3) * 512 + lane * 8;
  auto reduce_ld = [&](int g) {
#pragma unroll
    for (int i = 0; i < 4; ++i) ra[i] = *(const f32x2*)(red_r + (g & 1) * 16384 + i * 2048);
    rl0 = *(const f32x4*)(lds + AL_RL + (g & 1) * 1024 + r * 32); rl1 = *(const f32x4*)(lds + AL_RL + (g & 1) * 1024 + r * 32 + 16);
  };
  auto reduce_fin = [&](int g) {
    const f32x4 l4 = rl0 + rl1;
    const f32x2 l2 = (f32x2){l4[0], l4[1]} + (f32x2){l4[2], l4[3]};
    const float inv = __builtin_amdgcn_rcpf(l2[0] + l2[1]);
    const f32x2 sa = (ra[0] + ra[1]) + (ra[2] + ra[3]);
    *(unsigned*)(lds + o_wr + (g & 1) * 2048) = pk2(sa[0] * inv, sa[1] * inv);
    asm volatile("" ::: "memory");
    *(volatile LAS unsigned*)((LAS char*)lds + AL_FL + 128 + w * 16 + (lane & 3) * 4) = (unsigned)(g + 2);
  };
  auto reduce = [&](int g) { reduce_ld(g); reduce_fin(g); };
  const int g4 = lane >> 4, mm = lane & 15;
  const unsigned fs = ((t0 >> 4) * 16 + 2 * w) * KB, fv = (mm >> 3) * 1024 + (g4 * 16 + (t0 & 15) + (mm & 7)) * 16;
  u32x4 xr;
  auto load_res = [&]() {
    if (layer == 0) {
      const u16* xp = xin + (size_t)(t0 + (mm & 7)) * D_ + 64 * w + 32 * (mm >> 3) + 4 * g4;
      const u32x2 lo = *(const u32x2*)xp, hi = *(const u32x2*)(xp + 16);
      xr = (u32x4){lo[0], lo[1], hi[0], hi[1]};
    } else xr = bld4<SC1>(rs, fv, OFF_X2 + fs);
  };
  f32x16 cinit;
  { float ns = -SHIFT; PIN1(ns);
#pragma unroll
    for (int i = 0; i < 16; ++i) cinit[i] = ns; }
  PIN1(cinit);
  bf16x8 wof[4], of;
  { unsigned z0 = 0u; PIN1(z0); const u32x4 z = {z0, z0, z0, z0}; wof[0] = wof[1] = wof[2] = wof[3] = __builtin_bit_cast(bf16x8, z); }
  auto load_c1 = [&]() { c1p = bld1<SC1>(rs, (lane >> 4) * 7168 + (lane & 15) * 4, OFF_CS1 + (f1_nt0 + (w < 7 ? w : 6)) * 64); };
  auto store_c1 = [&]() { const float c1 = xrow_sum(__uint_as_float(c1p)); if (lane < 16) ((float*)(lds + AL_CS))[w * 16 + lane] = c1; };
  u32x4 p0, p1;
  unsigned flg;
  const unsigned fl_addr = lds0 + AL_FL + lane * 4;
  f32x16 sc = mfma32(kf[0], qf, cinit);
  const unsigned w1s = OFF_W1 + f1_nt0 * 16 * KB + smul(w, 14336), w1d = lds0 + AL_W1 + smul(w, 14336);
  bf16x8 wl[16];
  auto group = [&](auto mode_tag, const int g) {
    constexpr bool FIRST = decltype(mode_tag)::value == 0, SECOND = decltype(mode_tag)::value == 1, LAST = decltype(mode_tag)::value == 3;
    constexpr bool WGT = !FIRST && !SECOND;
#pragma unroll
    for (int t = 0; t < 8; ++t) {
      if ((t & 3) == 3) __builtin_amdgcn_s_setprio(3); if ((t & 3) == 0) __builtin_amdgcn_s_setprio(2); if ((t & 3) == 1) __builtin_amdgcn_s_setprio(1); if ((t & 3) == 2) __builtin_amdgcn_s_setprio(0);
      f32x16 sn;
      if (!(LAST && t == 7)) sn = mfma32(kf[(t + 1) & 7], t < 7 ? qf : qn, cinit);
      SB();
      if (FIRST) {
        if (t + KV_PD < 8) load_kv(t + KV_PD);
        if (t == 8 - KV_PD) {
          qw = bld<SC1>(rs, l16, qs + w * 1024);
          kn_a = bld1<SC1>(rs, (lane & 15) * 128, qkv + QO_KN2 + w * 2048);
          qn_a = bld1<SC1>(rs, (lane & 15) * 128, qkv + QO_QN2 + (t0 >> 4) * 2048);
          asm volatile("" ::: "memory");
          SB();
        }
      }
      float e0 = EX(sc[0]), e1 = EX(sc[1]), e2 = EX(sc[2]), e3 = EX(sc[3]), e4 = EX(sc[4]), e5 = EX(sc[5]);
      PIN4(e0, e1, e2, e3); PIN2(e4, e5); SB();
      if (t > 0 || !FIRST) o = mfma32(vf[(2 * t + 15) & 15], __builtin_bit_cast(bf16x8, p1), o);
      SB();
      float e6 = EX(sc[6]), e7 = EX(sc[7]);
      PIN2(e6, e7); SB();
      p0[0] = pk2(e0, e1); p0[1] = pk2(e2, e3); p0[2] = pk2(e4, e5); p0[3] = pk2(e6, e7);
      PIN1(p0); SB();
      float e8 = EX(sc[8]), e9 = EX(sc[9]);
      PIN2(e8, e9); SB();
      float e10, e11, e12, e13;
      if (t == 0) {
        if (!FIRST) { e10 = EX(sc[10]); e11 = EX(sc[11]); e12 = EX(sc[12]); e13 = EX(sc[13]); PIN4(e10, e11, e12, e13); PIN1(o); SB(); publish(g - 1); SB(); }
        f32x16 z = {}; o = mfma32(vf[0], __builtin_bit_cast(bf16x8, p0), z);
      } else o = mfma32(vf[2 * t], __builtin_bit_cast(bf16x8, p0), o);
      SB();
      if (t > 0 || FIRST) { e10 = EX(sc[10]); e11 = EX(sc[11]); e12 = EX(sc[12]); e13 = EX(sc[13]); PIN4(e10, e11, e12, e13); SB(); }
      if (WGT) {
        constexpr int X = LAST ? 2 : 0, D = LAST ? 0 : 1;
        if (t == 0) mfma16_w<3>(oacc[0], wof[0], of); else if (t == 1) mfma16_w<3 + X>(oacc[1], wof[1], of);
        else if (t == 2) mfma16_w<3 + D + 2 * X>(oacc[2], wof[2], of); else if (t == 3) mfma16_w<3 + 2 * D + 3 * X>(oacc[3], wof[3], of);
        else if (t == 4) mfma16_w<3 + 3 * D + 4 * X>(oacc[0], wof[0], of); else if (t == 5) mfma16_w<3 + 3 * D + 4 * X>(oacc[1], wof[1], of);
        else if (t == 6) mfma16_w<3 + 2 * D + 4 * X>(oacc[2], wof[2], of); else mfma16_w<3 + D + 4 * X>(oacc[3], wof[3], of);
        SB();
      }
      float e14 = EX(sc[14]), e15 = EX(sc[15]);
      PIN2(e14, e15); SB();
      p1[0] = pk2(e8, e9); p1[1] = pk2(e10, e11); p1[2] = pk2(e12, e13); p1[3] = pk2(e14, e15);
      PIN1(p1); SB();
      if (WGT) {
        const unsigned so = wos + (2 * (g - 2) + (t >> 2)) * 4096;
        if ((t & 3) == 0) bld_asm<0>(wof[0], rs, l16, so); else if ((t & 3) == 1) bld_asm<1024>(wof[1], rs, l16, so);
        else if ((t & 3) == 2) bld_asm<2048>(wof[2], rs, l16, so); else bld_asm<3072>(wof[3], rs, l16, so);
        SB();
      }
      if (LAST) {
#pragma unroll
        for (int i = 2 * t; i < 2 * t + 2; ++i) { wl[i] = bld<SC1>(rs, l16 + (i & 3) * 1024, wos + (12 + (i >> 2)) * 4096); asm volatile("" ::: "memory"); }
        SB();
      }
      if (WGT && !LAST && t >= 1 && t <= 3) {
        const int pc = min(3 * (g - 2) + (t - 1), 13);
        dma(rs, l16, w1s + pc * KB, w1d + pc * KB);
        SB();
      }
      if (SECOND && t == 5) { wfl = flag_ld(ws, flag_off(1, 2, w * 8), (r >> 2) * 128 + (lane & 3) * 8); SB(); }
      if (!FIRST && t == 1) { flg = *(volatile LAS unsigned*)((LAS char*)lds + AL_FL + lane * 4); SB(); }
      if (!FIRST && t == 2) {
        asm volatile("1:\n\tv_cmp_gt_u32 vcc, %2, %0\n\ts_cbranch_vccz 2f\n\ts_sleep 1\n\tds_read_b32 %0, %1\n\ts_waitcnt lgkmcnt(0)\n\ts_branch 1b\n2:"
                     : "+v"(flg) : "v"(fl_addr), "s"(g) : "vcc", "memory");
        reduce_ld(g - 1);
        SB();
      }
      if (!FIRST && t == 3) {
        reduce_fin(g - 1);
        SB();
      }
      if (WGT && (t & 3) == 3) { of = lds_frag(lds, AL_O + (g & 1) * 2048 + (t >> 2) * 1024 + l16); SB(); }
      if (!FIRST && !LAST && t == 5) { qn = lds_frag(lds, AL_Q + (g + 1) * 1024 + l16); SB(); }
      if (!(LAST && t == 7)) sc = sn;
    }
    qf = qn;
  };
  group(std::integral_constant<int, 0>{}, 0);
  *(LAS bf16x8*)((LAS char*)lds + AL_Q + w * 1024 + l16) = qw;
  ((float*)(lds + AL_BND))[w * 16 + (lane & 15)] = __uint_as_float(kn_a) * __uint_as_float(row16_umax(qn_a));
  wg_signal(rs, flag_off(1, 2, (xcd * 4 + (idx >> 3)) * 2 + ((idx >> 2) & 1)) + (idx & 3) * 8, tid, tag);
  auto weights_ready = [&]() { wave_wait(ws, flag_off(1, 2, w * 8), (r >> 2) * 128 + (lane & 3) * 8, tag); raw_barrier(); };
  const f32x2 kq2 = *(const f32x2*)(lds + AL_BND + lane * 8);
  const bool slow = !__all(kq2[0] <= SHIFT * SHIFT * 0.998f && kq2[1] <= SHIFT * SHIFT * 0.998f);
  of = wof[0];
  if (!slow) {
    group(std::integral_constant<int, 1>{}, 1);
    if (layer == 0) {
      if (!__all(wfl == tag)) wave_wait(ws, flag_off(1, 2, w * 8), (r >> 2) * 128 + (lane & 3) * 8, tag);
      raw_barrier();
    }
#pragma unroll 1
    for (int g = 2; g < 7; ++g) group(std::integral_constant<int, 2>{}, g);
    group(std::integral_constant<int, 3>{}, 7);
    __builtin_amdgcn_s_setprio(0);
    o = mfma32(vf[15], __builtin_bit_cast(bf16x8, p1), o);
    SB();
    load_res(); load_c1();
    SB();
    publish(7);
    raw_barrier();
    reduce_ld(7);
    bf16x8 of0 = lds_frag(lds, AL_O + l16), of1 = lds_frag(lds, AL_O + 1024 + l16);
    SB();
#pragma unroll
    for (int t = 0; t < 4; ++t) mfma16_w<4>(oacc[t], wof[t], of);
    SB();
    reduce_fin(7);
    SB();
#pragma unroll
    for (int t = 0; t < 4; ++t) mfma16_ip(oacc[t], wl[t], of0);
#pragma unroll
    for (int t = 0; t < 4; ++t) mfma16_ip(oacc[t], wl[4 + t], of1);
    store_c1();
    raw_barrier();
    of0 = lds_frag(lds, AL_O + 2048 + l16); of1 = lds_frag(lds, AL_O + 3072 + l16);
#pragma unroll
    for (int t = 0; t < 4; ++t) mfma16_ip(oacc[t], wl[8 + t], of0);
#pragma unroll
    for (int t = 0; t < 4; ++t) mfma16_ip(oacc[t], wl[12 + t], of1);
    asm volatile("s_nop 7" : "+v"(oacc[0]), "+v"(oacc[1]), "+v"(oacc[2]), "+v"(oacc[3]));
  } else {
    __builtin_amdgcn_s_setprio(0);
    weights_ready();
    float* mx = (float*)(lds + AL_MX);
    const f32x16 z = {};
#pragma unroll 1
    for (int g = 0; g < 8; ++g) {
      const bf16x8 q = lds_frag(lds, AL_Q + g * 1024 + l16);
      float m = -INFINITY;
#pragma unroll
      for (int t = 0; t < 8; ++t) {
        const f32x16 s = mfma32(kf[t], q, z);
#pragma unroll
        for (int i = 0; i < 16; ++i) m = fmaxf(m, s[i]);
        SB();
      }
      { auto sw = __builtin_amdgcn_permlane32_swap(__float_as_uint(m), __float_as_uint(m), false, false); m = fmaxf(__uint_as_float(sw[0]), __uint_as_float(sw[1])); }
      if (h == 0) mx[w * 256 + g * 32 + r] = m;
    }
    __syncthreads();
#pragma unroll 1
    for (int g = 0; g < 8; ++g) {
      const bf16x8 q = lds_frag(lds, AL_Q + g * 1024 + l16);
      float m = mx[g * 32 + r];
#pragma unroll
      for (int i = 1; i < 8; ++i) m = fmaxf(m, mx[i * 256 + g * 32 + r]);
      o = z;
#pragma unroll
      for (int t = 0; t < 8; ++t) {
        const f32x16 s = mfma32(kf[t], q, z);
        u32x4 a0, a1;
#pragma unroll
        for (int i = 0; i < 4; ++i) {
          a0[i] = pk2(EX(s[2 * i] - m), EX(s[2 * i + 1] - m));
          a1[i] = pk2(EX(s[8 + 2 * i] - m), EX(s[9 + 2 * i] - m));
        }
        o = mfma32(vf[2 * t], __builtin_bit_cast(bf16x8, a0), o);
        o = mfma32(vf[2 * t + 1], __builtin_bit_cast(bf16x8, a1), o);
        SB();
      }
      publish(g);
      __syncthreads();
      reduce(g);
      __syncthreads();
      const bf16x8 b0 = lds_frag(lds, AL_O + (g & 1) * 2048 + l16), b1 = lds_frag(lds, AL_O + (g & 1) * 2048 + 1024 + l16);
#pragma unroll
      for (int t = 0; t < 4; ++t) {
        oacc[t] = mfma16(bld<SC1>(rs, l16, wos + (2 * g * 4 + t) * 1024), b0, oacc[t]);
        oacc[t] = mfma16(bld<SC1>(rs, l16, wos + ((2 * g + 1) * 4 + t) * 1024), b1, oacc[t]);
      }
    }
    load_res(); load_c1(); store_c1();
#pragma unroll 1
    for (int i = 0; i < 14; ++i) dma(rs, l16, w1s + i * KB, w1d + i * KB);
    asm volatile("s_nop 7" : "+v"(oacc[0]), "+v"(oacc[1]), "+v"(oacc[2]), "+v"(oacc[3]));
  }
  float xv[8];
  asm volatile("s_nop 1\n\t"
               "v_add_f32_dpp %0, %8, %8 row_ror:8 row_mask:0xf bank_mask:0x3\n\t"
               "v_add_f32_dpp %1, %9, %9 row_ror:8 row_mask:0xf bank_mask:0x3\n\t"
               "v_add_f32_dpp %2, %10, %10 row_ror:8 row_mask:0xf bank_mask:0x3\n\t"
               "v_add_f32_dpp %3, %11, %11 row_ror:8 row_mask:0xf bank_mask:0x3\n\t"
               "v_add_f32_dpp %4, %12, %12 row_ror:8 row_mask:0xf bank_mask:0x3\n\t"
               "v_add_f32_dpp %5, %13, %13 row_ror:8 row_mask:0xf bank_mask:0x3\n\t"
               "v_add_f32_dpp %6, %14, %14 row_ror:8 row_mask:0xf bank_mask:0x3\n\t"
               "v_add_f32_dpp %7, %15, %15 row_ror:8 row_mask:0xf bank_mask:0x3\n\t"
               "v_add_f32_dpp %0, %16, %16 row_ror:8 row_mask:0xf bank_mask:0xc\n\t"
               "v_add_f32_dpp %1, %17, %17 row_ror:8 row_mask:0xf bank_mask:0xc\n\t"
               "v_add_f32_dpp %2, %18, %18 row_ror:8 row_mask:0xf bank_mask:0xc\n\t"
               "v_add_f32_dpp %3, %19, %19 row_ror:8 row_mask:0xf bank_mask:0xc\n\t"
               "v_add_f32_dpp %4, %20, %20 row_ror:8 row_mask:0xf bank_mask:0xc\n\t"
               "v_add_f32_dpp %5, %21, %21 row_ror:8 row_mask:0xf bank_mask:0xc\n\t"
               "v_add_f32_dpp %6, %22, %22 row_ror:8 row_mask:0xf bank_mask:0xc\n\t"
               "v_add_f32_dpp %7, %23, %23 row_ror:8 row_mask:0xf bank_mask:0xc\n\t"
               : "=&v"(xv[0]), "=&v"(xv[1]), "=&v"(xv[2]), "=&v"(xv[3]), "=&v"(xv[4]), "=&v"(xv[5]), "=&v"(xv[6]), "=&v"(xv[7])
               : "v"(oacc[0][0]), "v"(oacc[0][1]), "v"(oacc[0][2]), "v"(oacc[0][3]), "v"(oacc[1][0]), "v"(oacc[1][1]), "v"(oacc[1][2]), "v"(oacc[1][3]), "v"(oacc[2][0]), "v"(oacc[2][1]), "v"(oacc[2][2]), "v"(oacc[2][3]), "v"(oacc[3][0]), "v"(oacc[3][1]), "v"(oacc[3][2]), "v"(oacc[3][3]));
  u32x4 ov;
  float sm = 0.f, sq = 0.f;
#pragma unroll
  for (int q = 0; q < 4; ++q) {
    const float a = xv[2 * q] + bflo(xr[q]), b = xv[2 * q + 1] + bfhi(xr[q]);
    ov[q] = pk2(a, b);
    sm += a + b; sq = fmaf(a, a, fmaf(b, b, sq));
  }
  bst4<SC1>(ov, rs, fv, qkv + QO_X1 + fs);
  sm += dpp<0x128>(sm); sq += dpp<0x128>(sq);
  xrow_sum2(sm, sq);
  bst2<SC1>((u32x2){__float_as_uint(sm), __float_as_uint(sq)}, rs, lane < 8 ? lane * 8 : OOB, qkv + QO_ST + t0 * 128 + w * 128);
  wg_signal(rs, flag_off(layer, 0, t0 >> 4) + (t0 & 8), tid, tag);
}

DI void ffn1_phase(char* lds, char* ws, rsrc_t rs, int layer, int tid, bf16x8 (&stg)[28], u64 tag) {
  const int lane = tid & 63, g = lane >> 4, mm = lane & 15;
  const int w = __builtin_amdgcn_readfirstlane(tid >> 6);
  const int bid = mybid(), xcd = bid & 7, idx = bid >> 3, rb = idx >> 4, cb = idx & 15;
  const int mt = xcd * 16 + rb * 8 + w, nt0 = smul(cb, 7);
  const unsigned l16 = lane * 16;
#pragma unroll
  for (int i = 0; i < PRE_EARLY; ++i) ffn2_prefetch(rs, l16, (idx & 7) * 4, w, stg, i);
  bf16x8 a[2][7];
#pragma unroll
  for (int t = 0; t < 7; ++t) a[0][t] = lds_frag(lds, AL_W1 + t * 1024 + l16);
  wave_wait(ws, flag_off(layer, 0, mt), (lane & 1) * 8, tag);
  constexpr int PD = 5;
  bf16x8 b[16];
  const unsigned lb = OFF_QKV + smul(layer, QKV_BYTES), bs = lb + QO_X1 + mt * 16 * KB;
#pragma unroll
  for (int ks = 0; ks < PD; ++ks) b[ks] = bld<SC1>(rs, l16, bs + ks * KB);
  const u32x2 st0 = __builtin_amdgcn_raw_buffer_load_b64(rs, (mm >> 3) * 1024 + g * 128 + (mm & 7) * 8, lb + QO_ST + mt * 2048, SC1);
  const u32x2 st1 = __builtin_amdgcn_raw_buffer_load_b64(rs, (mm >> 3) * 1024 + g * 128 + (mm & 7) * 8 + 512, lb + QO_ST + mt * 2048, SC1);
  const f32x4 stp = __builtin_bit_cast(f32x4, (u32x4){st0[0], st0[1], st1[0], st1[1]});
  SB();
  f32x4 acc[7] = {};
  float rstd, nmr;
#pragma unroll
  for (int ks = 0; ks < 16; ++ks) {
    if (ks < 15) {
#pragma unroll
      for (int t = 0; t < 7; ++t) a[(ks + 1) & 1][t] = lds_frag(lds, AL_W1 + ((ks + 1) * 7 + t) * 1024 + l16);
    }
    if (ks + PD < 16) b[ks + PD] = bld<SC1>(rs, l16, bs + (ks + PD) * KB);
    {
      constexpr int NMID = 28 - PRE_EARLY - PRE_LATE;
      const int lo = ks <= 10 ? (ks * 6) / 11 : 6 + (ks - 11) * 2, hi = ks + 1 <= 10 ? ((ks + 1) * 6) / 11 : 6 + (ks + 1 - 11) * 2;
#pragma unroll
      for (int i = lo; i < hi; ++i) if (i < NMID) ffn2_prefetch(rs, l16, (idx & 7) * 4, w, stg, PRE_EARLY + i);
    }
    if (ks == 8) {
      float sm = stp[0] + stp[2], sq = stp[1] + stp[3];
      xrow_sum2(sm, sq);
      const float mean = sm * (1.f / D_);
      rstd = __builtin_amdgcn_rsqf(fmaxf(sq * (1.f / D_) - mean * mean, 0.f) + LN_EPS); nmr = -mean * rstd;
      PIN2(rstd, nmr);
    }
    SB();
#pragma unroll
    for (int t = 0; t < 7; ++t) acc[t] = mfma16(a[ks & 1][t], b[ks], acc[t]);
    SB();
  }
  u32x2 hv[7];
#pragma unroll
  for (int t = 0; t < 7; ++t) {
    const f32x4 v = acc[t] * rstd + *(const f32x4*)(lds + AL_CS + t * 64 + g * 16) * nmr;
    hv[t] = (u32x2){relu_pk(pk2(v[0], v[1])), relu_pk(pk2(v[2], v[3]))};
  }
  const unsigned hs = lb + QO_H + smul(mt, 56 * KB) + (nt0 >> 1) * KB;
  if ((cb & 1) == 0) {
#pragma unroll
    for (int p = 0; p < 3; ++p) bst4<SC1>((u32x4){hv[2 * p][0], hv[2 * p][1], hv[2 * p + 1][0], hv[2 * p + 1][1]}, rs, l16, hs + p * KB);
    bst2<SC1>(hv[6], rs, lane * 8, hs + 3 * KB);
  } else {
    bst2<SC1>(hv[0], rs, lane * 8 + 512, hs);
#pragma unroll
    for (int p = 0; p < 3; ++p) bst4<SC1>((u32x4){hv[2 * p + 1][0], hv[2 * p + 1][1], hv[2 * p + 2][0], hv[2 * p + 2][1]}, rs, l16, hs + (p + 1) * KB);
  }
  wg_signal(rs, flag_off(layer, 1, (xcd * 2 + rb) * 8 + (cb >> 1)) + (cb & 1) * 8, tid, tag);
}

DI void ffn2_phase(char* lds, char* ws, rsrc_t rs, u16* out, int layer, int tid, bf16x8 (&stg)[28], u64 tag) {
  const int lane = tid & 63, g = lane >> 4, mm = lane & 15;
  const int w = __builtin_amdgcn_readfirstlane(tid >> 6);
  const int bid = mybid(), xcd = bid & 7, idx = bid >> 3, rb = idx >> 3, cb = idx & 7;
  const int fm = w >> 1, fp = w & 1, mt0 = xcd * 16 + rb * 4, nt0 = cb * 4;
  const unsigned lds0 = (unsigned)(uintptr_t)lds, l16 = lane * 16;
#pragma unroll
  for (int i = 28 - PRE_LATE; i < 28; ++i) ffn2_prefetch(rs, l16, nt0, w, stg, i);
  const unsigned fs = ((mt0 + fm) * 16 + (nt0 >> 1) + fp) * KB;
  const unsigned lb = OFF_QKV + smul(layer, QKV_BYTES);
  const u32x4 xr = bld4<SC1>(rs, l16, lb + QO_X1 + fs);
  if (layer == 0) {
#pragma unroll
    for (int i = 0; i < 6; ++i) { const int c = 6 * w + i; dma(rs, l16, OFF_WQKV + ((4 * cb + (c & 3)) * 16 + (c >> 2)) * KB, lds0 + c * 1024); }
  }
  wave_wait(ws, flag_off(layer, 1, (xcd * 2 + (rb >> 1)) * 8 + w), (lane & 1) * 8, tag);
  constexpr int PD = 6;
  bf16x8 b[28];
  auto ldb = [&](int i) {
    const unsigned so = lb + QO_H + smul(mt0 + ((fm + 1 + i / 7) & 3), 56 * KB) + smul(w, 7 * KB) + (i % 7) * KB;
    if (i % 7 == 3) {
      const u32x2 lo = __builtin_amdgcn_raw_buffer_load_b64(rs, lane * 8, so, SC1), hi = __builtin_amdgcn_raw_buffer_load_b64(rs, lane * 8 + 512, so, SC1);
      b[i] = __builtin_bit_cast(bf16x8, (u32x4){lo[0], lo[1], hi[0], hi[1]});
    } else b[i] = bld<SC1>(rs, l16, so);
  };
#pragma unroll
  for (int i = 0; i < PD; ++i) ldb(i);
  SB();
  f32x4 acc[2][4];
  auto send = [&](int mi, int pr) {
    const int d = 2 * ((fm + 1 + mi) & 3) + (pr ^ fp);
    char* xb = lds + AL_W1 + (d * 7 + ((w - d - 1) & 7)) * 2048 + l16;
    *(f32x4*)(xb) = acc[mi & 1][2 * pr]; *(f32x4*)(xb + 1024) = acc[mi & 1][2 * pr + 1];
  };
#pragma unroll
  for (int i = 0; i < 28; ++i) {
    const int mi = i / 7, j = i % 7;
    if (i + PD < 28) ldb(i + PD);
    if (mi > 0 && j == 3) send(mi - 1, 0);
    if (mi > 0 && j == 5) send(mi - 1, 1);
    SB();
#pragma unroll
    for (int t = 0; t < 4; ++t) {
      if (j == 0) { const f32x4 z = {}; acc[mi & 1][t] = mfma16(stg[t], b[i], z); }
      else acc[mi & 1][t] = mfma16(stg[4 * j + t], b[i], acc[mi & 1][t]);
    }
    SB();
  }
  send(3, 1);
  f32x4 s0 = acc[1][0] + (f32x4){bflo(xr[0]), bfhi(xr[0]), bflo(xr[1]), bfhi(xr[1])};
  f32x4 s1 = acc[1][1] + (f32x4){bflo(xr[2]), bfhi(xr[2]), bflo(xr[3]), bfhi(xr[3])};
  raw_barrier();
#pragma unroll
  for (int d = 0; d < 7; ++d) {
    const char* xb = lds + AL_W1 + (w * 7 + d) * 2048 + l16;
    s0 += *(const f32x4*)(xb); s1 += *(const f32x4*)(xb + 1024);
  }
  const u32x4 o = {pk2(s0[0], s0[1]), pk2(s0[2], s0[3]), pk2(s1[0], s1[1]), pk2(s1[2], s1[3])};
  if (layer == 1) {
    u16* op = out + (size_t)((mt0 + fm) * 16 + mm) * D_ + (nt0 + 2 * fp) * 16 + 4 * g;
    *(u32x2*)op = (u32x2){o[0], o[1]};
    *(u32x2*)(op + 16) = (u32x2){o[2], o[3]};
    asm volatile("" : "+s"(tag));
    bst2<SC1>((u32x2){(unsigned)tag, (unsigned)(tag >> 32)}, rs, (bid | tid) == 0 ? 0u : OOB, OFF_EPOCH);
  } else {
    bst4<SC1>(o, rs, l16, OFF_X2 + fs);
    wg_signal(rs, flag_off(0, 2, (xcd * 4 + rb) * 2 + (cb >> 2)) + (cb & 3) * 8, tid, tag);
  }
}

typedef short v4i16_t __attribute__((ext_vector_type(4)));
DI bf16x8 tr_frag(const char* lds, int off0, int off1) {
  const v4i16_t lo = __builtin_amdgcn_ds_read_tr16_b64_v4i16((LAS v4i16_t*)((LAS char*)lds + off0));
  const v4i16_t hi = __builtin_amdgcn_ds_read_tr16_b64_v4i16((LAS v4i16_t*)((LAS char*)lds + off1));
  return __builtin_shufflevector(lo, hi, 0, 1, 2, 3, 4, 5, 6, 7);
}
constexpr int QL_KV = 65536, QL_P = 81920, QL_TR = QL_P + 65536;
constexpr int QP_T = 0, QP_KV = 2048, QP_CS = 3072, QP_CK = 3136, QP_ST = 3200;
template <bool FIRST, class H1, class TG>
DI void qkv_phase(char* lds, char* ws, rsrc_t rs, int tid, const u16* x, const u16* wq, const u16* wkv, H1 hook1, TG gettag) {
  const int lane = tid & 63, g = lane >> 4, mm = lane & 15;
  const int w = __builtin_amdgcn_readfirstlane(tid >> 6);
  const int bid = mybid(), xcd = bid & 7, idx = bid >> 3, rb = idx >> 3, cb = idx & 7;
  const int ml = w & 3, hf = w >> 2, mt = xcd * 16 + rb * 4 + ml;
  const int kvml = cb >> 1;
  const unsigned lds0 = (unsigned)(uintptr_t)lds, l16 = lane * 16;
  const unsigned qkv = OFF_QKV + (FIRST ? 0 : QKV_BYTES);
  char* const mine = lds + QL_P + w * 8192;
  const char* const other = lds + QL_P + (w ^ 4) * 8192;
  bf16x8 b[8];
  f32x4 cs0, cs1, csk; float csv;
  if (FIRST) {
    const rsrc_t rw = __builtin_amdgcn_make_buffer_rsrc((void*)wq, 0, D_ * D_ * 2, 0x00020000);
    const rsrc_t rk = __builtin_amdgcn_make_buffer_rsrc((void*)wkv, 0, D_ * 16 * 2, 0x00020000);
    const rsrc_t rx = __builtin_amdgcn_make_buffer_rsrc((void*)x, 0, S_ * D_ * 2, 0x00020000);
#pragma unroll
    for (int i = 0; i < 2; ++i) dma(rk, l16, (2 * w + i) * 1024, lds0 + QL_KV + (2 * w + i) * 1024);
    const unsigned vx = (lane >> 5) * 1024 + (((lane & 31) ^ (lane >> 5)) << 4);
#pragma unroll
    for (int i = 0; i < 8; ++i) dma(rx, vx ^ (i << 5), mt * 16384 + i * 2048 + hf * 512, lds0 + QL_P + w * 8192 + i * 1024);
    const unsigned v0 = (lane >> 3) * 1024 + (((lane & 7) ^ ((lane >> 3) & 2)) << 4), v1 = v0 ^ 64;
#pragma unroll
    for (int i = 0; i < 8; ++i) dma(rw, (i & 1) ? v1 : v0, (8 * w + i) * 8192 + cb * 128, lds0 + (8 * w + i) * 1024);
    wait_vm<0>();
#pragma unroll
    for (int j = 0; j < 8; ++j) b[j] = lds_frag(mine, mm * 512 + (((4 * j + g) ^ mm) << 4));
  } else {
#pragma unroll
    for (int i = 0; i < 2; ++i) {
      const int c = 48 + 2 * w + i;
      dma(rs, l16, OFF_WQKV + ((4 * cb + (c & 3)) * 16 + (c >> 2)) * KB, lds0 + c * 1024);
      dma(rs, l16, OFF_WQKV + ((32 + (cb & 1)) * 16 + 2 * w + i) * KB, lds0 + QL_KV + (2 * w + i) * 1024);
    }
    cs0 = bld4f<SC1>(rs, g * 16, OFF_CSUM + (4 * cb + 2 * hf) * 64); cs1 = bld4f<SC1>(rs, g * 16 + 64, OFF_CSUM + (4 * cb + 2 * hf) * 64);
    csk = bld4f<SC1>(rs, g * 16, OFF_CSUM + 32 * 64);
    csv = __uint_as_float(bld1<SC1>(rs, mm * 4, OFF_CSUM + 33 * 64));
    wave_wait(ws, flag_off(0, 2, (xcd * 4 + rb) * 2 + hf), (lane & 3) * 8, gettag());
#pragma unroll
    for (int j = 0; j < 8; ++j) b[j] = bld<SC1>(rs, l16, OFF_X2 + (mt * 16 + 8 * hf + j) * KB);
  }
  raw_barrier();
  hook1();
  const int q4 = (lane >> 2) & 3, p4 = lane & 3;
  const int fsw = ((q4 >> 1) & 1) | ((g & 1) << 1);
  const int abase = FIRST ? hf * 32768 + (8 * g + q4) * 128 + p4 * 8 : hf * 32768 + l16;
  auto aq = [&](int t  ) { return FIRST ? abase + ((t ^ fsw) << 5) : abase + t * 1024; };
  auto ldq = [&](int base, int j) { return FIRST ? tr_frag(lds, base + j * 4096, base + j * 4096 + 512) : lds_frag(lds, base + j * 4096); };
  const int kvb = QL_KV + hf * 8192 + (FIRST ? (8 * g + q4) * 32 + p4 * 8 : l16);
  auto ldkv = [&](int j) { return FIRST ? tr_frag(lds, kvb + j * 1024, kvb + j * 1024 + 128) : lds_frag(lds, kvb + j * 1024); };
  bf16x8 ones; for (int i = 0; i < 8; ++i) ones[i] = 0x3F80;
  if (FIRST) {
    const int cbs = aq(ml);
    f32x4 c0 = {}, c1 = {}, ck = {};
#pragma unroll
    for (int j = 0; j < 8; j += 2) { c0 = mfma16(ldq(cbs, j), ones, c0); c1 = mfma16(ldq(cbs, j + 1), ones, c1); }
    const int kv2 = kvb + ml * 2048;
#pragma unroll
    for (int j = 0; j < 2; ++j) ck = mfma16(FIRST ? tr_frag(lds, kv2 + j * 1024, kv2 + j * 1024 + 128) : lds_frag(lds, kv2 + j * 1024), ones, ck);
    if (mm == 0) { *(f32x4*)(mine + QP_CS + g * 16) = c0 + c1; *(f32x4*)(mine + QP_CK + g * 16) = ck; }
  }
  f32x4 acc[4] = {}, accs = {}, accg = {}, akv = {};
  bf16x8 a[2][4];
  const int ab0 = aq(2 * hf), ab1 = aq(2 * hf + 1), ab2 = aq(2 - 2 * hf), ab3 = aq(3 - 2 * hf);
  auto lda = [&](int j) { a[j & 1][0] = ldq(ab0, j); a[j & 1][1] = ldq(ab1, j); a[j & 1][2] = ldq(ab2, j); a[j & 1][3] = ldq(ab3, j); };
#pragma unroll
  for (int j = 0; j < 8; ++j) { accs = mfma16(ones, b[j], accs); accg = mfma16(b[j], b[j], accg); }
  if (ml == kvml) {
    if (cb & 1) {
#pragma unroll
      for (int j = 0; j < 8; ++j) akv = mfma16(b[j], ldkv(j), akv);
    } else {
#pragma unroll
      for (int j = 0; j < 8; ++j) akv = mfma16(ldkv(j), b[j], akv);
    }
    *(f32x4*)(mine + QP_KV + l16) = akv;
  }
  {
    const float s01 = (mm & 1) ? accg[1] : accg[0], s23 = (mm & 1) ? accg[3] : accg[2];
    if (g == (mm >> 2)) *(f32x2*)(mine + QP_ST + mm * 8) = (f32x2){accs[0], (mm & 2) ? s23 : s01};
  }
  lda(0);
  raw_barrier();
  float invd = 1.f / D_; PIN1(invd);
  auto stats = [&](int tokl, float& mean, float& rstd) {
    const f32x2 s = *(const f32x2*)(mine + QP_ST + tokl * 8) + *(const f32x2*)(other + QP_ST + tokl * 8);
    mean = s[0] * invd;
    rstd = __builtin_amdgcn_rsqf(fmaxf(s[1] * invd - mean * mean, 0.f) + LN_EPS);
  };
  float mean, rstd; stats(mm, mean, rstd);
  const bool kvw = hf == 1 && ml == kvml;
  if (kvw) {
    const f32x4 skv = akv + *(const f32x4*)(other + QP_KV + l16);
    if (cb & 1) {
      if (FIRST) {
        csv = 0.f;
#pragma unroll
        for (int i = 0; i < 8; ++i) csv += *(const float*)(lds + QL_P + i * 8192 + QP_CK + mm * 4);
        if (mt == 0) bst1<SC1>(__float_as_uint(csv), rs, g == 0 ? mm * 4 : OOB, OFF_CSUM + 33 * 64);
      }
      float v[4];
#pragma unroll
      for (int i = 0; i < 4; ++i) { float mn, rsd; stats(4 * g + i, mn, rsd); v[i] = (skv[i] - mn * csv) * rsd; }
      const u32x2 o = {pk2(v[0], v[1]), pk2(v[2], v[3])};
      bst2<SC1>(o, rs, (g & 1) * 272 + mm * 16 + (g >> 1) * 8, qkv + QO_VT + smul(mt, 640));
      bst2<SC1>((u32x2){0x3F803F80u, 0x3F803F80u}, rs, mm == 0 ? (g & 1) * 272 + 256 + (g >> 1) * 8 : OOB, qkv + QO_VT + smul(mt, 640));
    } else {
      if (FIRST) {
        csk = (f32x4){};
#pragma unroll
        for (int i = 0; i < 8; ++i) csk += *(const f32x4*)(lds + QL_P + i * 8192 + QP_CK + g * 16);
        if (mt == 0) bst4<SC1>(__builtin_bit_cast(u32x4, csk), rs, mm == 0 ? g * 16 : OOB, OFF_CSUM + 32 * 64);
      }
      const f32x4 k = (skv - csk * mean) * rstd;
      const u32x2 o = {pk2(k[0], k[1]), pk2(k[2], k[3])};
      bst2<SC1>(o, rs, mm * 32 + g * 8, qkv + QO_K + mt * 512);
      const unsigned n2 = row16_umax(__float_as_uint(xrow_sum(sq4(o))));
      bst1<SC1>(n2, rs, lane == 0 ? 0u : OOB, qkv + QO_KN2 + mt * 128);
    }
  }
#pragma unroll
  for (int j = 0; j < 8; ++j) {
    if (j < 7) lda(j + 1);
    SB();
#pragma unroll
    for (int t = 0; t < 4; ++t) acc[t] = mfma16(a[j & 1][t], b[j], acc[t]);
    SB();
  }
  *(f32x4*)(mine + QP_T + l16) = acc[2];
  *(f32x4*)(mine + QP_T + 1024 + l16) = acc[3];
  const unsigned fl = (xcd * 4 + rb) * 2 + (cb >> 2);
  if (kvw) {
    asm volatile("s_waitcnt vmcnt(0)" ::: "memory");
    u64 tag = gettag(); asm volatile("" : "+s"(tag));
    bst2<SC1>((u32x2){(unsigned)tag, (unsigned)(tag >> 32)}, rs, lane == 0 ? 0u : OOB, flag_off(FIRST ? 0 : 1, 3, fl) + (cb & 3) * 8);
  }
  raw_barrier();
  const char* csp = lds + QL_P + 2 * hf * 8192 + QP_CS + g * 16;
  if (FIRST) {
    cs0 = *(const f32x4*)(csp) + *(const f32x4*)(csp + 32768); cs1 = *(const f32x4*)(csp + 8192) + *(const f32x4*)(csp + 8192 + 32768);
    if (mt == 0) {
      bst4<SC1>(__builtin_bit_cast(u32x4, cs0), rs, mm == 0 ? g * 16 : OOB, OFF_CSUM + (4 * cb + 2 * hf) * 64);
      bst4<SC1>(__builtin_bit_cast(u32x4, cs1), rs, mm == 0 ? g * 16 + 64 : OOB, OFF_CSUM + (4 * cb + 2 * hf) * 64);
    }
  }
  const f32x4 s0 = acc[0] + *(const f32x4*)(other + QP_T + l16), s1 = acc[1] + *(const f32x4*)(other + QP_T + 1024 + l16);
  const float rq = rstd * QSCALE, nmq = -mean * rq;
  const f32x4 q0 = s0 * rq + cs0 * nmq, q1 = s1 * rq + cs1 * nmq;
  const u32x2 o0 = {pk2(q0[0], q0[1]), pk2(q0[2], q0[3])}, o1 = {pk2(q1[0], q1[1]), pk2(q1[2], q1[3])};
  {
    auto s0_ = __builtin_amdgcn_permlane16_swap(o0[0], o1[0], false, false);
    auto s1_ = __builtin_amdgcn_permlane16_swap(o0[1], o1[1], false, false);
    bst4<SC1>((u32x4){s0_[0], s1_[0], s0_[1], s1_[1]}, rs, (mm >> 3) * 8192 + ((g >> 1) * 32 + (2 * hf + (g & 1)) * 8 + (mm & 7)) * 16, qkv + QO_Q + mt * 16384 + cb * 1024);
  }
  float n0 = sq4(o0), n1 = sq4(o1);
  xrow_sum2(n0, n1);
  const unsigned qn = row16_umax(umax(__float_as_uint(n0), __float_as_uint(n1)));
  bst1<SC1>(qn, rs, lane == 0 ? 0u : OOB, qkv + QO_QN2 + mt * 2048 + (cb * 2 + hf) * 128);
  wg_signal(rs, flag_off(FIRST ? 0 : 1, 3, 64 + fl) + (cb & 3) * 8, tid, gettag());
}

DI void first_phase(char* lds, char* ws, rsrc_t rs, int tid, const u16* x, const u16* wq, const u16* wk, const u16* wv, const u16* wo,
                    const u16* w1, const u16* w2, u64& tag, u64 pc) {
  const int lane = tid & 63, g = lane >> 4;
  const int w = __builtin_amdgcn_readfirstlane(tid >> 6);
  const unsigned lds0 = (unsigned)(uintptr_t)lds, l16 = lane * 16;
  const int xcd = blockIdx.x & 7, idx = blockIdx.x >> 3, cb = idx & 7;
  u64 ep;
  const u16* tw; int tpitch, tks, tnb; unsigned tdst; int tkst;
  auto fetch = [&]() {
    ep = flag_ld(ws, OFF_EPOCH, 0);
    const int bi = mybid() * 4 + (w & 3);
    if (bi < 128) { tw = wo; tpitch = 1024; tks = bi >> 3; tnb = bi & 7; tdst = OFF_WO; tkst = 16; }
    else if (bi < 576) { const int cw = (bi - 128) >> 2, kq = cw / 28; tw = w1; tpitch = 3584; tks = 4 * kq + (w & 3); tnb = cw - 28 * kq; tdst = OFF_W1; tkst = 16; }
    else { const int c = bi - 576; tw = w2; tpitch = 1024; tks = c >> 3; tnb = c & 7; tdst = OFF_W2; tkst = 56; }
    if (w == 3) {
      const rsrc_t rc = __builtin_amdgcn_make_buffer_rsrc((void*)pc, 0, CODE_PF, 0x00020000);
      unsigned sv;
#pragma unroll
      for (int i = 0; i < (CODE_PF + 8191) / 8192; ++i)
        asm volatile("s_mov_b32 %0, m0\n\ts_mov_b32 m0, %1\n\ts_nop 0\n\tbuffer_load_dword %2, %3, %4 offen lds\n\ts_mov_b32 m0, %0"
                     : "=&s"(sv) : "s"(lds0 + QL_P + 7 * 8192 + 6144), "v"(lane * 128), "s"(rc), "s"(i * 8192) : "memory");
    }
    if (w < 4) {
      const rsrc_t rt = __builtin_amdgcn_make_buffer_rsrc((void*)tw, 0, D_ * DFF * 2, 0x00020000);
      const unsigned v = (lane >> 3) * tpitch + (lane & 7) * 16;
#pragma unroll
      for (int i = 0; i < 4; ++i) dma(rt, v, (32 * tks + 8 * i) * tpitch + tnb * 128, lds0 + QL_TR + w * 4096 + i * 1024);
    }
  };
  auto gettag = [&]() {
    const u64 e = ep + 1ull;
    tag = ((u64)(unsigned)__builtin_amdgcn_readfirstlane((int)(e >> 32)) << 32) | (unsigned)__builtin_amdgcn_readfirstlane((int)e);
    return tag;
  };
  auto convert = [&]() {
    const int q4 = (lane >> 2) & 3, p4 = lane & 3;
    if (w < 4) {
      wait_vm<0>();
      const int base = QL_TR + w * 4096 + (4 * g + q4) * 128 + p4 * 8;
      bf16x8 ones; for (int i = 0; i < 8; ++i) ones[i] = 0x3F80;
#pragma unroll
      for (int t = 0; t < 4; ++t) {
        const bf16x8 f = tr_frag(lds, base + t * 32, base + t * 32 + 2048);
        const int nt = 4 * tnb + t, sl = nt / 7;
        const unsigned ch = tdst == OFF_WO ? (tnb * 16 + tks) * 4 + t : tdst == OFF_W1 ? smul(sl * 16 + tks, 7) + (nt - smul(sl, 7)) : smul(nt, 56) + tks;
        bst4<SC1>(__builtin_bit_cast(u32x4, f), rs, l16, tdst + ch * KB);
        const f32x4 z = {};
        const f32x4 c = mfma16(f, ones, z);
        if ((lane & 15) == 0) *(f32x4*)(lds + QL_P + w * 8192 + 4096 + t * 64 + g * 16) = c;
      }
    } else if (w < 6) {
      const int c = (xcd * 4 + (idx >> 3)) * 2 + (w & 1), ks = c >> 2, t = c & 3;
      const int r0 = 4 * g + q4, f0 = ((r0 >> 1) & 1) | (((r0 >> 3) & 1) << 1);
      const int base = (32 * ks + r0) * 128 + ((t ^ f0) << 5) + p4 * 8;
      bst4<SC1>(__builtin_bit_cast(u32x4, tr_frag(lds, base, base + 2048)), rs, l16, OFF_WQKV + ((4 * cb + t) * 16 + ks) * KB);
    } else if (w == 6) {
      const int ks = xcd * 4 + (idx >> 3);
      if (cb < 2 && ks < 16) {
        const int base = QL_KV + (32 * ks + 4 * g + q4) * 32 + p4 * 8;
        bst4<SC1>(__builtin_bit_cast(u32x4, tr_frag(lds, base, base + 512)), rs, l16, OFF_WQKV + ((32 + cb) * 16 + ks) * KB);
      }
    }
  };
  auto colsum = [&]() {
    if (w == 7 && tdst == OFF_W1) {
      const float* c = (const float*)(lds + QL_P + 4096) + lane;
      bst1<SC1>(__float_as_uint((c[0] + c[2048]) + (c[4096] + c[6144])), rs, lane * 4, OFF_CS1 + (tks >> 2) * 7168 + tnb * 256);
    }
  };
  qkv_phase<true>(lds, ws, rs, tid, x, wq, (cb & 1) ? wv : wk, fetch, gettag);
  convert();
  raw_barrier();
  colsum();
}

DI int mytid(int w64) { int t; asm volatile("v_mbcnt_lo_u32_b32 %0, -1, %1\n\tv_mbcnt_hi_u32_b32 %0, -1, %0" : "=v"(t) : "s"(w64)); return t; }
__global__ __launch_bounds__(512, 2) void k_main(const u16* __restrict__ x, const u16* __restrict__ wq, const u16* __restrict__ wk, const u16* __restrict__ wv,
                                                 const u16* __restrict__ wo, const u16* __restrict__ w1, const u16* __restrict__ w2,
                                                 char* __restrict__ ws, u16* __restrict__ out) {
  extern __shared__ __attribute__((aligned(16))) char lds[];
  const int w64 = __builtin_amdgcn_readfirstlane(threadIdx.x & ~63);
  const rsrc_t rs = __builtin_amdgcn_make_buffer_rsrc((void*)ws, 0, WS_NEED, 0x00020000);
  u64 tag, pc;
  asm volatile("s_getpc_b64 %0" : "=s"(pc));
  if (w64 >= 512 - 64 * PF_EARLY) {
    const rsrc_t rc = __builtin_amdgcn_make_buffer_rsrc((void*)pc, 0, CODE_PF, 0x00020000);
    unsigned sv;
    asm volatile("s_mov_b32 %0, m0\n\ts_mov_b32 m0, %1\n\ts_nop 0\n\tbuffer_load_dword %2, %3, 0 offen lds\n\ts_mov_b32 m0, %0"
                 : "=&s"(sv) : "s"((unsigned)(uintptr_t)lds + QL_TR), "v"((511 - threadIdx.x) * 128), "s"(rc) : "memory");
  }
  first_phase(lds, ws, rs, mytid(w64), x, wq, wk, wv, wo, w1, w2, tag, pc);
#pragma unroll 1
  for (int layer = 0; layer < 2; ++layer) {
    bf16x8 stg[28];
    attn_phase(lds, ws, rs, x, layer, mytid(w64), tag);
    ffn1_phase(lds, ws, rs, layer, mytid(w64), stg, tag);
    ffn2_phase(lds, ws, rs, out, layer, mytid(w64), stg, tag);
    if (layer == 0) qkv_phase<false>(lds, ws, rs, mytid(w64), nullptr, nullptr, nullptr, []() {}, [&]() { return tag; });
  }
}

extern "C" void kernel_launch(void* const* d_in, const int* in_sizes, int n_in, void* d_out, int out_size, void* d_ws, size_t ws_size,
                              hipStream_t stream) {
  const u16* x = (const u16*)d_in[0];
  const u16* wq = (const u16*)d_in[1];
  const u16* wk = (const u16*)d_in[2];
  const u16* wv = (const u16*)d_in[3];
  const u16* wo = (const u16*)d_in[4];
  const u16* w1 = (const u16*)d_in[6];
  const u16* w2 = (const u16*)d_in[7];
  char* ws = (char*)d_ws;
  u16* out = (u16*)d_out;
  (void)hipFuncSetAttribute((const void*)k_main, hipFuncAttributeMaxDynamicSharedMemorySize, LDS_BYTES);
  k_main<<<256, 512, LDS_BYTES, stream>>>(x, wq, wk, wv, wo, w1, w2, ws, out);
}
```

```cpp
#include <hip/hip_runtime.h>
#include <hip/hip_bf16.h>
#include <stdint.h>
#include <type_traits>

typedef __attribute__((ext_vector_type(8))) short bf16x8;
typedef __attribute__((ext_vector_type(2))) float f32x2;
typedef __attribute__((ext_vector_type(4))) float f32x4;
typedef __attribute__((ext_vector_type(16))) float f32x16;
typedef __attribute__((ext_vector_type(4))) unsigned u32x4;
typedef __attribute__((ext_vector_type(2))) unsigned u32x2;
typedef unsigned short u16;
typedef __amdgpu_buffer_rsrc_t rsrc_t;
#define DI __device__ __forceinline__
#define LAS __attribute__((address_space(3)))

constexpr int S_ = 2048, D_ = 512, DFF = 1792;
constexpr float QSCALE = 0.25f * 1.4426950408889634f;
constexpr float LN_EPS = 1e-5f;

constexpr unsigned KB = 1024;
constexpr unsigned OFF_FLAG = 0;
constexpr unsigned OFF_EPOCH = 128 * KB;
constexpr unsigned FLAG_BYTES = 129 * KB;
constexpr unsigned OFF_WQKV = OFF_FLAG + FLAG_BYTES;
constexpr unsigned OFF_WO   = OFF_WQKV + 544 * KB;
constexpr unsigned OFF_W1   = OFF_WO + 512 * KB;
constexpr unsigned OFF_W2   = OFF_W1 + 1792 * KB;
constexpr unsigned OFF_CSUM = OFF_W2 + 1792 * KB;
constexpr unsigned OFF_CS1  = OFF_CSUM + 4 * KB;
constexpr unsigned OFF_X2   = OFF_CS1 + 28 * KB;
constexpr unsigned OFF_QKV  = OFF_X2 + 2048 * KB;
constexpr unsigned QO_X1 = 0;
constexpr unsigned QO_ST = QO_X1 + 2048 * KB;
constexpr unsigned QO_H  = QO_ST + 256 * KB;
constexpr unsigned QO_Q  = QO_H + 7168 * KB;
constexpr unsigned QO_K = QO_Q + 2048 * KB;
constexpr unsigned QO_VT = QO_K + 64 * KB;
constexpr unsigned QO_KN2 = QO_VT + 80 * KB;
constexpr unsigned QO_QN2 = QO_KN2 + 16 * KB;
constexpr unsigned QKV_BYTES = QO_QN2 + 256 * KB;
constexpr unsigned WS_NEED  = OFF_QKV + 2 * QKV_BYTES;
DI unsigned flag_off(int layer, int edge, int line) { return OFF_FLAG + ((layer * 4 + edge) * 128 + line) * 128; }

DI float bflo(unsigned v) { return __uint_as_float(v << 16); }
DI float bfhi(unsigned v) { return __uint_as_float(v & 0xffff0000u); }
DI unsigned pk2(float lo, float hi) {
  typedef __attribute__((ext_vector_type(2))) __bf16 bf16x2_;
  f32x2 v = {lo, hi};
  bf16x2_ r = __builtin_convertvector(v, bf16x2_);
  return __builtin_bit_cast(unsigned, r);
}
DI unsigned relu_pk(unsigned v) {
  typedef __attribute__((ext_vector_type(2))) short s16x2;
  const s16x2 z = {0, 0};
  return __builtin_bit_cast(unsigned, __builtin_elementwise_max(__builtin_bit_cast(s16x2, v), z));
}
DI f32x4 mfma16(bf16x8 a, bf16x8 b, f32x4 c) {
  typedef __attribute__((ext_vector_type(8))) __bf16 b8;
  return __builtin_amdgcn_mfma_f32_16x16x32_bf16(__builtin_bit_cast(b8, a), __builtin_bit_cast(b8, b), c, 0, 0, 0);
}
DI f32x16 mfma32(bf16x8 a, bf16x8 b, f32x16 c) {
  typedef __attribute__((ext_vector_type(8))) __bf16 b8;
  return __builtin_amdgcn_mfma_f32_32x32x16_bf16(__builtin_bit_cast(b8, a), __builtin_bit_cast(b8, b), c, 0, 0, 0);
}
template <int N> DI void wait_vm() { asm volatile("s_waitcnt vmcnt(%0)" :: "i"(N) : "memory"); }
DI void raw_barrier() { asm volatile("s_waitcnt lgkmcnt(0)\n\ts_barrier" ::: "memory"); }
DI bf16x8 lds_frag(const char* lds, int off) { return *(const LAS bf16x8*)((const LAS char*)lds + off); }

template <int CTRL> DI float dpp(float x) { return __builtin_bit_cast(float, __builtin_amdgcn_mov_dpp(__builtin_bit_cast(int, x), CTRL, 0xf, 0xf, true)); }
template <int CTRL> DI unsigned dppu(unsigned x) { return (unsigned)__builtin_amdgcn_mov_dpp((int)x, CTRL, 0xf, 0xf, true); }
DI unsigned umax(unsigned a, unsigned b) { return a > b ? a : b; }
DI unsigned row16_umax(unsigned x) { x = umax(x, dppu<0xB1>(x)); x = umax(x, dppu<0x4E>(x)); x = umax(x, dppu<0x141>(x)); x = umax(x, dppu<0x140>(x)); return x; }
DI float xrow_sum(float x) {
  auto s = __builtin_amdgcn_permlane16_swap(__float_as_uint(x), __float_as_uint(x), false, false);
  x = __uint_as_float(s[0]) + __uint_as_float(s[1]);
  auto t = __builtin_amdgcn_permlane32_swap(__float_as_uint(x), __float_as_uint(x), false, false);
  return __uint_as_float(t[0]) + __uint_as_float(t[1]); }
DI void xrow_sum2(float& a, float& b) {
  auto s = __builtin_amdgcn_permlane16_swap(__float_as_uint(a), __float_as_uint(b), false, false);
  const float x = __uint_as_float(s[0]) + __uint_as_float(s[1]);
  auto t = __builtin_amdgcn_permlane32_swap(__float_as_uint(x), __float_as_uint(x), false, false);
  const float y = __uint_as_float(t[0]) + __uint_as_float(t[1]);
  auto u = __builtin_amdgcn_permlane16_swap(__float_as_uint(y), __float_as_uint(y), false, false);
  a = __uint_as_float(u[0]); b = __uint_as_float(u[1]); }
DI float sq4(u32x2 o) { return bflo(o[0]) * bflo(o[0]) + bfhi(o[0]) * bfhi(o[0]) + bflo(o[1]) * bflo(o[1]) + bfhi(o[1]) * bfhi(o[1]); }
#define SB() __builtin_amdgcn_sched_barrier(0)
#define EX(x) __builtin_amdgcn_exp2f(x)
#define PIN1(a) asm volatile("" : "+v"(a))
#define PIN2(a, b) asm volatile("" : "+v"(a), "+v"(b))
#define PIN4(a, b, c, d) asm volatile("" : "+v"(a), "+v"(b), "+v"(c), "+v"(d))
constexpr int SC1 = 16;
DI void mfma16_ip(f32x4& acc, bf16x8 a, bf16x8 b) { asm volatile("v_mfma_f32_16x16x32_bf16 %0, %1, %2, %0" : "+v"(acc) : "v"(a), "v"(b)); }
template <int IMM> DI void bld_asm(bf16x8& d, rsrc_t rs, unsigned voff, unsigned soff) { asm volatile("buffer_load_dwordx4 %0, %1, %2, %3 offen offset:%4 sc1" : "+v"(d) : "v"(voff), "s"(rs), "s"(soff), "i"(IMM)); }
template <int N> DI void mfma16_w(f32x4& acc, bf16x8& a, bf16x8 b) { asm volatile("s_waitcnt vmcnt(%3)\n\tv_mfma_f32_16x16x32_bf16 %0, %1, %2, %0" : "+v"(acc), "+v"(a) : "v"(b), "i"(N)); }
template <int AUX = 0> DI bf16x8 bld(rsrc_t rs, unsigned voff, unsigned soff) { return __builtin_bit_cast(bf16x8, __builtin_amdgcn_raw_buffer_load_b128(rs, voff, soff, AUX)); }
template <int AUX = 0> DI u32x4 bld4(rsrc_t rs, unsigned voff, unsigned soff) { return __builtin_amdgcn_raw_buffer_load_b128(rs, voff, soff, AUX); }
template <int AUX = 0> DI f32x4 bld4f(rsrc_t rs, unsigned voff, unsigned soff) { return __builtin_bit_cast(f32x4, __builtin_amdgcn_raw_buffer_load_b128(rs, voff, soff, AUX)); }
template <int AUX = 0> DI unsigned bld1(rsrc_t rs, unsigned voff, unsigned soff) { return __builtin_amdgcn_raw_buffer_load_b32(rs, voff, soff, AUX); }
template <int AUX = 0> DI void bst4(u32x4 v, rsrc_t rs, unsigned voff, unsigned soff) {
  __builtin_amdgcn_raw_buffer_store_b128(v, rs, voff, soff, AUX);
  asm volatile("s_nop 1" :: "v"(v) : "memory");
}
template <int AUX = 0> DI void bst2(u32x2 v, rsrc_t rs, unsigned voff, unsigned soff) { __builtin_amdgcn_raw_buffer_store_b64(v, rs, voff, soff, AUX); }
template <int AUX = 0> DI void bst1(unsigned v, rsrc_t rs, unsigned voff, unsigned soff) { __builtin_amdgcn_raw_buffer_store_b32(v, rs, voff, soff, AUX); }
constexpr unsigned OOB = 0x7ffffff0u;
DI void dma(rsrc_t rs, unsigned voff, unsigned soff, unsigned lds_dst) {
  asm volatile("s_mov_b32 m0, %0\n\ts_nop 0\n\tbuffer_load_dwordx4 %1, %2, %3 offen sc1 lds"
               :: "s"(lds_dst), "v"(voff), "s"(rs), "s"(soff) : "m0", "memory");
}
typedef unsigned long long u64;
typedef __attribute__((address_space(1))) u64 gu64;
DI void wg_signal(rsrc_t rs, unsigned foff, int tid, u64 tag) {
  asm volatile("s_waitcnt vmcnt(0)" ::: "memory");
  raw_barrier();
  asm volatile("" : "+s"(tag));
  bst2<SC1>((u32x2){(unsigned)tag, (unsigned)(tag >> 32)}, rs, tid == 0 ? 0u : OOB, foff);
}
DI u64 flag_ld(char* ws, unsigned foff, unsigned lane_off) { return __hip_atomic_load((gu64*)(ws + foff + lane_off), __ATOMIC_RELAXED, __HIP_MEMORY_SCOPE_AGENT); }
DI void wave_wait(char* ws, unsigned foff, unsigned lane_off, u64 tag) {
#pragma unroll 1
  for (unsigned spins = 0; spins < (1u << 17); ++spins) {
    if (__all(flag_ld(ws, foff, lane_off) == tag)) break;
    __builtin_amdgcn_s_sleep(1);
  }
  asm volatile("" ::: "memory");
}

DI unsigned smul(unsigned a, unsigned b) { unsigned r; asm("s_mul_i32 %0, %1, %2" : "=s"(r) : "s"(a), "s"(b)); return r; }
DI int mybid() { int b = blockIdx.x; asm volatile("" : "+s"(b)); return b; }

constexpr int CODE_PF = 30720, PF_EARLY = 2;

constexpr int LDS_BYTES = 163840;
constexpr int AL_RO = 0;
constexpr int AL_RL = AL_RO + 32768;
constexpr int AL_O  = AL_RL + 2048;
constexpr int AL_Q  = AL_O + 4096;
constexpr int AL_FL = AL_Q + 8192;
constexpr int AL_BND = AL_FL + 256;
constexpr int AL_CS = AL_BND + 512;
constexpr int AL_W1 = 49152;
constexpr int AL_MX = AL_W1;
static_assert(AL_CS + 512 <= AL_W1 && AL_W1 + 112 * 1024 <= LDS_BYTES, "lds");
constexpr float SHIFT = 32.f;

constexpr int PRE_EARLY = 8, PRE_LATE = 4;
DI void ffn2_prefetch(rsrc_t rs, unsigned l16, int nt0, int w, bf16x8 (&stg)[28], int i) {
  const int ti = i & 3;
  stg[i] = bld<SC1>(rs, l16, OFF_W2 + smul(nt0 + ((ti & 2) ^ (2 * (w & 1))), 56 * KB) + smul(w, 7 * KB) + ((ti & 1) * 56 + (i >> 2)) * KB);
}

DI void attn_phase(char* lds, char* ws, rsrc_t rs, const u16* xin, int layer, int tid, u64 tag) {
  const int lane = tid & 63, r = lane & 31, h = lane >> 5;
  const int w = __builtin_amdgcn_readfirstlane(tid >> 6);
  const int bid = mybid(), xcd = bid & 7, idx = bid >> 3;
  const int t0 = xcd * 256 + idx * 8;
  const unsigned lds0 = (unsigned)(uintptr_t)lds;
  const unsigned qkv = OFF_QKV + smul(layer, QKV_BYTES);
  const unsigned l16 = lane * 16;
  const int f1_nt0 = smul(idx & 15, 7);
  wave_wait(ws, flag_off(layer, 3, 0), (h == 0 ? w * 8 + (lane >> 2) : 64 + (xcd * 4 + (idx >> 3)) * 2 + ((lane >> 2) & 1)) * 128 + (lane & 3) * 8, tag);
  const unsigned qs = qkv + QO_Q + t0 * 1024;
  bf16x8 qf = bld<SC1>(rs, l16, qs);
  SB();
  bf16x8 kf[8], vf[16], qn, qw;
  unsigned kn_a, qn_a, c1p;
  u64 wfl;
  const unsigned kb = qkv + QO_K + w * 8192, vb = qkv + QO_VT + smul(w, 16 * 640);
  const unsigned ko = r * 32 + 16 * h, vo = r <= 16 ? h * 272 + r * 16 : OOB;
  auto load_kv = [&](int t) {
    if (t < 7) kf[t + 1] = bld<SC1>(rs, ko + ((t + 1) & 3) * 1024, kb + ((t + 1) >> 2) * 4096);
    else qn = bld<SC1>(rs, l16 + 1024, qs);
    vf[2 * t] = bld<SC1>(rs, vo, vb + (2 * t) * 640);
    vf[2 * t + 1] = bld<SC1>(rs, vo, vb + (2 * t + 1) * 640);
    asm volatile("" ::: "memory");
    SB();
  };
  constexpr int KV_PD = 3;
  kf[0] = bld<SC1>(rs, ko, kb);
  SB();
#pragma unroll
  for (int t = 0; t < KV_PD; ++t) load_kv(t);
  ((unsigned*)(lds + AL_FL))[lane] = h;
  const unsigned wos = OFF_WO + w * 64 * KB;
  f32x4 oacc[4] = {};
  f32x16 o;
  char* const ro_w = lds + AL_RO + w * 2048 + lane * 8;
  const int lf_w0 = h == 0 ? AL_RL + (r * 8 + w) * 4 : AL_FL + (w * 4 + (r & 3)) * 4, lf_step = h == 0 ? 1024 : 0;
  const int o_wr = AL_O + (r >> 4) * 1024 + ((2 * ((w & 3) >> 1) + h) * 16 + (w >> 2) * 8 + (r & 7)) * 16 + ((r >> 3) & 1) * 8 + (w & 1) * 4;
  auto publish = [&](int g) {
    char* ro = ro_w + (g & 1) * 16384;
    *(f32x2*)(ro) = (f32x2){o[0], o[1]}; *(f32x2*)(ro + 512) = (f32x2){o[2], o[3]};
    *(f32x2*)(ro + 1024) = (f32x2){o[4], o[5]}; *(f32x2*)(ro + 1536) = (f32x2){o[6], o[7]};
    asm volatile("" ::: "memory");
    *(volatile LAS unsigned*)((LAS char*)lds + lf_w0 + (g & 1) * lf_step) = h == 0 ? __float_as_uint(o[8]) : (unsigned)(g + 1);
  };
  f32x2 ra[4]; f32x4 rl0, rl1;
  const char* const red_r = lds + AL_RO + (w >> 2) * 8192 + (w & 3) * 512 + lane * 8;
  auto reduce_ld = [&](int g) {
#pragma unroll
    for (int i = 0; i < 4; ++i) ra[i] = *(const f32x2*)(red_r + (g & 1) * 16384 + i * 2048);
    rl0 = *(const f32x4*)(lds + AL_RL + (g & 1) * 1024 + r * 32); rl1 = *(const f32x4*)(lds + AL_RL + (g & 1) * 1024 + r * 32 + 16);
  };
  auto reduce_fin = [&](int g) {
    const f32x4 l4 = rl0 + rl1;
    const f32x2 l2 = (f32x2){l4[0], l4[1]} + (f32x2){l4[2], l4[3]};
    const float inv = __builtin_amdgcn_rcpf(l2[0] + l2[1]);
    const f32x2 sa = (ra[0] + ra[1]) + (ra[2] + ra[3]);
    *(unsigned*)(lds + o_wr + (g & 1) * 2048) = pk2(sa[0] * inv, sa[1] * inv);
    asm volatile("" ::: "memory");
    *(volatile LAS unsigned*)((LAS char*)lds + AL_FL + 128 + w * 16 + (lane & 3) * 4) = (unsigned)(g + 2);
  };
  auto reduce = [&](int g) { reduce_ld(g); reduce_fin(g); };
  const int g4 = lane >> 4, mm = lane & 15;
  const unsigned fs = ((t0 >> 4) * 16 + 2 * w) * KB, fv = (mm >> 3) * 1024 + (g4 * 16 + (t0 & 15) + (mm & 7)) * 16;
  u32x4 xr;
  auto load_res = [&]() {
    if (layer == 0) {
      const u16* xp = xin + (size_t)(t0 + (mm & 7)) * D_ + 64 * w + 32 * (mm >> 3) + 4 * g4;
      const u32x2 lo = *(const u32x2*)xp, hi = *(const u32x2*)(xp + 16);
      xr = (u32x4){lo[0], lo[1], hi[0], hi[1]};
    } else xr = bld4<SC1>(rs, fv, OFF_X2 + fs);
  };
  f32x16 cinit;
  { float ns = -SHIFT; PIN1(ns);
#pragma unroll
    for (int i = 0; i < 16; ++i) cinit[i] = ns; }
  PIN1(cinit);
  bf16x8 wof[4], of;
  { unsigned z0 = 0u; PIN1(z0); const u32x4 z = {z0, z0, z0, z0}; wof[0] = wof[1] = wof[2] = wof[3] = __builtin_bit_cast(bf16x8, z); }
  auto load_c1 = [&]() { c1p = bld1<SC1>(rs, (lane >> 4) * 7168 + (lane & 15) * 4, OFF_CS1 + (f1_nt0 + (w < 7 ? w : 6)) * 64); };
  auto store_c1 = [&]() { const float c1 = xrow_sum(__uint_as_float(c1p)); if (lane < 16) ((float*)(lds + AL_CS))[w * 16 + lane] = c1; };
  u32x4 p0, p1;
  unsigned flg;
  const unsigned fl_addr = lds0 + AL_FL + lane * 4;
  f32x16 sc = mfma32(kf[0], qf, cinit);
  const unsigned w1s = OFF_W1 + f1_nt0 * 16 * KB + smul(w, 14336), w1d = lds0 + AL_W1 + smul(w, 14336);
  bf16x8 wl[16];
  auto group = [&](auto mode_tag, const int g) {
    constexpr bool FIRST = decltype(mode_tag)::value == 0, SECOND = decltype(mode_tag)::value == 1, LAST = decltype(mode_tag)::value == 3;
    constexpr bool WGT = !FIRST && !SECOND;
#pragma unroll
    for (int t = 0; t < 8; ++t) {
      if ((t & 3) == 3) __builtin_amdgcn_s_setprio(3); if ((t & 3) == 0) __builtin_amdgcn_s_setprio(2); if ((t & 3) == 1) __builtin_amdgcn_s_setprio(1); if ((t & 3) == 2) __builtin_amdgcn_s_setprio(0);
      f32x16 sn;
      if (!(LAST && t == 7)) sn = mfma32(kf[(t + 1) & 7], t < 7 ? qf : qn, cinit);
      SB();
      if (FIRST) {
        if (t + KV_PD < 8) load_kv(t + KV_PD);
        if (t == 8 - KV_PD) {
          qw = bld<SC1>(rs, l16, qs + w * 1024);
          kn_a = bld1<SC1>(rs, (lane & 15) * 128, qkv + QO_KN2 + w * 2048);
          qn_a = bld1<SC1>(rs, (lane & 15) * 128, qkv + QO_QN2 + (t0 >> 4) * 2048);
          asm volatile("" ::: "memory");
          SB();
        }
      }
      float e0 = EX(sc[0]), e1 = EX(sc[1]), e2 = EX(sc[2]), e3 = EX(sc[3]), e4 = EX(sc[4]), e5 = EX(sc[5]);
      PIN4(e0, e1, e2, e3); PIN2(e4, e5); SB();
      if (t > 0 || !FIRST) o = mfma32(vf[(2 * t + 15) & 15], __builtin_bit_cast(bf16x8, p1), o);
      SB();
      float e6 = EX(sc[6]), e7 = EX(sc[7]);
      PIN2(e6, e7); SB();
      p0[0] = pk2(e0, e1); p0[1] = pk2(e2, e3); p0[2] = pk2(e4, e5); p0[3] = pk2(e6, e7);
      PIN1(p0); SB();
      float e8 = EX(sc[8]), e9 = EX(sc[9]);
      PIN2(e8, e9); SB();
      float e10, e11, e12, e13;
      if (t == 0) {
        if (!FIRST) { e10 = EX(sc[10]); e11 = EX(sc[11]); e12 = EX(sc[12]); e13 = EX(sc[13]); PIN4(e10, e11, e12, e13); PIN1(o); SB(); publish(g - 1); SB(); }
        f32x16 z = {}; o = mfma32(vf[0], __builtin_bit_cast(bf16x8, p0), z);
      } else o = mfma32(vf[2 * t], __builtin_bit_cast(bf16x8, p0), o);
      SB();
      if (t > 0 || FIRST) { e10 = EX(sc[10]); e11 = EX(sc[11]); e12 = EX(sc[12]); e13 = EX(sc[13]); PIN4(e10, e11, e12, e13); SB(); }
      if (WGT) {
        constexpr int X = LAST ? 2 : 0, D = LAST ? 0 : 1;
        if (t == 0) mfma16_w<3>(oacc[0], wof[0], of); else if (t == 1) mfma16_w<3 + X>(oacc[1], wof[1], of);
        else if (t == 2) mfma16_w<3 + D + 2 * X>(oacc[2], wof[2], of); else if (t == 3) mfma16_w<3 + 2 * D + 3 * X>(oacc[3], wof[3], of);
        else if (t == 4) mfma16_w<3 + 3 * D + 4 * X>(oacc[0], wof[0], of); else if (t == 5) mfma16_w<3 + 3 * D + 4 * X>(oacc[1], wof[1], of);
        else if (t == 6) mfma16_w<3 + 2 * D + 4 * X>(oacc[2], wof[2], of); else mfma16_w<3 + D + 4 * X>(oacc[3], wof[3], of);
        SB();
      }
      float e14 = EX(sc[14]), e15 = EX(sc[15]);
      PIN2(e14, e15); SB();
      p1[0] = pk2(e8, e9); p1[1] = pk2(e10, e11); p1[2] = pk2(e12, e13); p1[3] = pk2(e14, e15);
      PIN1(p1); SB();
      if (WGT) {
        const unsigned so = wos + (2 * (g - 2) + (t >> 2)) * 4096;
        if ((t & 3) == 0) bld_asm<0>(wof[0], rs, l16, so); else if ((t & 3) == 1) bld_asm<1024>(wof[1], rs, l16, so);
        else if ((t & 3) == 2) bld_asm<2048>(wof[2], rs, l16, so); else bld_asm<3072>(wof[3], rs, l16, so);
        SB();
      }
      if (LAST) {
#pragma unroll
        for (int i = 2 * t; i < 2 * t + 2; ++i) { wl[i] = bld<SC1>(rs, l16 + (i & 3) * 1024, wos + (12 + (i >> 2)) * 4096); asm volatile("" ::: "memory"); }
        SB();
      }
      if (WGT && !LAST && t >= 1 && t <= 3) {
        const int pc = min(3 * (g - 2) + (t - 1), 13);
        dma(rs, l16, w1s + pc * KB, w1d + pc * KB);
        SB();
      }
      if (SECOND && t == 5) { wfl = flag_ld(ws, flag_off(1, 2, w * 8), (r >> 2) * 128 + (lane & 3) * 8); SB(); }
      if (!FIRST && t == 1) { flg = *(volatile LAS unsigned*)((LAS char*)lds + AL_FL + lane * 4); SB(); }
      if (!FIRST && t == 2) {
        asm volatile("1:\n\tv_cmp_gt_u32 vcc, %2, %0\n\ts_cbranch_vccz 2f\n\ts_sleep 1\n\tds_read_b32 %0, %1\n\ts_waitcnt lgkmcnt(0)\n\ts_branch 1b\n2:"
                     : "+v"(flg) : "v"(fl_addr), "s"(g) : "vcc", "memory");
        reduce_ld(g - 1);
        SB();
      }
      if (!FIRST && t == 3) {
        reduce_fin(g - 1);
        SB();
      }
      if (WGT && (t & 3) == 3) { of = lds_frag(lds, AL_O + (g & 1) * 2048 + (t >> 2) * 1024 + l16); SB(); }
      if (!FIRST && !LAST && t == 5) { qn = lds_frag(lds, AL_Q + (g + 1) * 1024 + l16); SB(); }
      if (!(LAST && t == 7)) sc = sn;
    }
    qf = qn;
  };
  group(std::integral_constant<int, 0>{}, 0);
  *(LAS bf16x8*)((LAS char*)lds + AL_Q + w * 1024 + l16) = qw;
  ((float*)(lds + AL_BND))[w * 16 + (lane & 15)] = __uint_as_float(kn_a) * __uint_as_float(row16_umax(qn_a));
  wg_signal(rs, flag_off(1, 2, (xcd * 4 + (idx >> 3)) * 2 + ((idx >> 2) & 1)) + (idx & 3) * 8, tid, tag);
  auto weights_ready = [&]() { wave_wait(ws, flag_off(1, 2, w * 8), (r >> 2) * 128 + (lane & 3) * 8, tag); raw_barrier(); };
  const f32x2 kq2 = *(const f32x2*)(lds + AL_BND + lane * 8);
  const bool slow = !__all(kq2[0] <= SHIFT * SHIFT * 0.998f && kq2[1] <= SHIFT * SHIFT * 0.998f);
  of = wof[0];
  if (!slow) {
    group(std::integral_constant<int, 1>{}, 1);
    if (layer == 0) {
      if (!__all(wfl == tag)) wave_wait(ws, flag_off(1, 2, w * 8), (r >> 2) * 128 + (lane & 3) * 8, tag);
      raw_barrier();
    }
#pragma unroll 1
    for (int g = 2; g < 7; ++g) group(std::integral_constant<int, 2>{}, g);
    group(std::integral_constant<int, 3>{}, 7);
    __builtin_amdgcn_s_setprio(0);
    o = mfma32(vf[15], __builtin_bit_cast(bf16x8, p1), o);
    SB();
    load_res(); load_c1();
    SB();
    publish(7);
    raw_barrier();
    reduce_ld(7);
    bf16x8 of0 = lds_frag(lds, AL_O + l16), of1 = lds_frag(lds, AL_O + 1024 + l16);
    SB();
#pragma unroll
    for (int t = 0; t < 4; ++t) mfma16_w<4>(oacc[t], wof[t], of);
    SB();
    reduce_fin(7);
    SB();
#pragma unroll
    for (int t = 0; t < 4; ++t) mfma16_ip(oacc[t], wl[t], of0);
#pragma unroll
    for (int t = 0; t < 4; ++t) mfma16_ip(oacc[t], wl[4 + t], of1);
    store_c1();
    raw_barrier();
    of0 = lds_frag(lds, AL_O + 2048 + l16); of1 = lds_frag(lds, AL_O + 3072 + l16);
#pragma unroll
    for (int t = 0; t < 4; ++t) mfma16_ip(oacc[t], wl[8 + t], of0);
#pragma unroll
    for (int t = 0; t < 4; ++t) mfma16_ip(oacc[t], wl[12 + t], of1);
    asm volatile("s_nop 7" : "+v"(oacc[0]), "+v"(oacc[1]), "+v"(oacc[2]), "+v"(oacc[3]));
  } else {
    __builtin_amdgcn_s_setprio(0);
    weights_ready();
    float* mx = (float*)(lds + AL_MX);
    const f32x16 z = {};
#pragma unroll 1
    for (int g = 0; g < 8; ++g) {
      const bf16x8 q = lds_frag(lds, AL_Q + g * 1024 + l16);
      float m = -INFINITY;
#pragma unroll
      for (int t = 0; t < 8; ++t) {
        const f32x16 s = mfma32(kf[t], q, z);
#pragma unroll
        for (int i = 0; i < 16; ++i) m = fmaxf(m, s[i]);
        SB();
      }
      { auto sw = __builtin_amdgcn_permlane32_swap(__float_as_uint(m), __float_as_uint(m), false, false); m = fmaxf(__uint_as_float(sw[0]), __uint_as_float(sw[1])); }
      if (h == 0) mx[w * 256 + g * 32 + r] = m;
    }
    __syncthreads();
#pragma unroll 1
    for (int g = 0; g < 8; ++g) {
      const bf16x8 q = lds_frag(lds, AL_Q + g * 1024 + l16);
      float m = mx[g * 32 + r];
#pragma unroll
      for (int i = 1; i < 8; ++i) m = fmaxf(m, mx[i * 256 + g * 32 + r]);
      o = z;
#pragma unroll
      for (int t = 0; t < 8; ++t) {
        const f32x16 s = mfma32(kf[t], q, z);
        u32x4 a0, a1;
#pragma unroll
        for (int i = 0; i < 4; ++i) {
          a0[i] = pk2(EX(s[2 * i] - m), EX(s[2 * i + 1] - m));
          a1[i] = pk2(EX(s[8 + 2 * i] - m), EX(s[9 + 2 * i] - m));
        }
        o = mfma32(vf[2 * t], __builtin_bit_cast(bf16x8, a0), o);
        o = mfma32(vf[2 * t + 1], __builtin_bit_cast(bf16x8, a1), o);
        SB();
      }
      publish(g);
      __syncthreads();
      reduce(g);
      __syncthreads();
      const bf16x8 b0 = lds_frag(lds, AL_O + (g & 1) * 2048 + l16), b1 = lds_frag(lds, AL_O + (g & 1) * 2048 + 1024 + l16);
#pragma unroll
      for (int t = 0; t < 4; ++t) {
        oacc[t] = mfma16(bld<SC1>(rs, l16, wos + (2 * g * 4 + t) * 1024), b0, oacc[t]);
        oacc[t] = mfma16(bld<SC1>(rs, l16, wos + ((2 * g + 1) * 4 + t) * 1024), b1, oacc[t]);
      }
    }
    load_res(); load_c1(); store_c1();
#pragma unroll 1
    for (int i = 0; i < 14; ++i) dma(rs, l16, w1s + i * KB, w1d + i * KB);
    asm volatile("s_nop 7" : "+v"(oacc[0]), "+v"(oacc[1]), "+v"(oacc[2]), "+v"(oacc[3]));
  }
  float xv[8];
  asm volatile("s_nop 1\n\t"
               "v_add_f32_dpp %0, %8, %8 row_ror:8 row_mask:0xf bank_mask:0x3\n\t"
               "v_add_f32_dpp %1, %9, %9 row_ror:8 row_mask:0xf bank_mask:0x3\n\t"
               "v_add_f32_dpp %2, %10, %10 row_ror:8 row_mask:0xf bank_mask:0x3\n\t"
               "v_add_f32_dpp %3, %11, %11 row_ror:8 row_mask:0xf bank_mask:0x3\n\t"
               "v_add_f32_dpp %4, %12, %12 row_ror:8 row_mask:0xf bank_mask:0x3\n\t"
               "v_add_f32_dpp %5, %13, %13 row_ror:8 row_mask:0xf bank_mask:0x3\n\t"
               "v_add_f32_dpp %6, %14, %14 row_ror:8 row_mask:0xf bank_mask:0x3\n\t"
               "v_add_f32_dpp %7, %15, %15 row_ror:8 row_mask:0xf bank_mask:0x3\n\t"
               "v_add_f32_dpp %0, %16, %16 row_ror:8 row_mask:0xf bank_mask:0xc\n\t"
               "v_add_f32_dpp %1, %17, %17 row_ror:8 row_mask:0xf bank_mask:0xc\n\t"
               "v_add_f32_dpp %2, %18, %18 row_ror:8 row_mask:0xf bank_mask:0xc\n\t"
               "v_add_f32_dpp %3, %19, %19 row_ror:8 row_mask:0xf bank_mask:0xc\n\t"
               "v_add_f32_dpp %4, %20, %20 row_ror:8 row_mask:0xf bank_mask:0xc\n\t"
               "v_add_f32_dpp %5, %21, %21 row_ror:8 row_mask:0xf bank_mask:0xc\n\t"
               "v_add_f32_dpp %6, %22, %22 row_ror:8 row_mask:0xf bank_mask:0xc\n\t"
               "v_add_f32_dpp %7, %23, %23 row_ror:8 row_mask:0xf bank_mask:0xc\n\t"
               : "=&v"(xv[0]), "=&v"(xv[1]), "=&v"(xv[2]), "=&v"(xv[3]), "=&v"(xv[4]), "=&v"(xv[5]), "=&v"(xv[6]), "=&v"(xv[7])
               : "v"(oacc[0][0]), "v"(oacc[0][1]), "v"(oacc[0][2]), "v"(oacc[0][3]), "v"(oacc[1][0]), "v"(oacc[1][1]), "v"(oacc[1][2]), "v"(oacc[1][3]), "v"(oacc[2][0]), "v"(oacc[2][1]), "v"(oacc[2][2]), "v"(oacc[2][3]), "v"(oacc[3][0]), "v"(oacc[3][1]), "v"(oacc[3][2]), "v"(oacc[3][3]));
  u32x4 ov;
  float sm = 0.f, sq = 0.f;
#pragma unroll
  for (int q = 0; q < 4; ++q) {
    const float a = xv[2 * q] + bflo(xr[q]), b = xv[2 * q + 1] + bfhi(xr[q]);
    ov[q] = pk2(a, b);
    sm += a + b; sq = fmaf(a, a, fmaf(b, b, sq));
  }
  bst4<SC1>(ov, rs, fv, qkv + QO_X1 + fs);
  sm += dpp<0x128>(sm); sq += dpp<0x128>(sq);
  xrow_sum2(sm, sq);
  bst2<SC1>((u32x2){__float_as_uint(sm), __float_as_uint(sq)}, rs, lane < 8 ? lane * 8 : OOB, qkv + QO_ST + t0 * 128 + w * 128);
  wg_signal(rs, flag_off(layer, 0, t0 >> 4) + (t0 & 8), tid, tag);
}

DI void ffn1_phase(char* lds, char* ws, rsrc_t rs, int layer, int tid, bf16x8 (&stg)[28], u64 tag) {
  const int lane = tid & 63, g = lane >> 4, mm = lane & 15;
  const int w = __builtin_amdgcn_readfirstlane(tid >> 6);
  const int bid = mybid(), xcd = bid & 7, idx = bid >> 3, rb = idx >> 4, cb = idx & 15;
  const int mt = xcd * 16 + rb * 8 + w, nt0 = smul(cb, 7);
  const unsigned l16 = lane * 16;
#pragma unroll
  for (int i = 0; i < PRE_EARLY; ++i) ffn2_prefetch(rs, l16, (idx & 7) * 4, w, stg, i);
  bf16x8 a[2][7];
#pragma unroll
  for (int t = 0; t < 7; ++t) a[0][t] = lds_frag(lds, AL_W1 + t * 1024 + l16);
  wave_wait(ws, flag_off(layer, 0, mt), (lane & 1) * 8, tag);
  constexpr int PD = 5;
  bf16x8 b[16];
  const unsigned lb = OFF_QKV + smul(layer, QKV_BYTES), bs = lb + QO_X1 + mt * 16 * KB;
#pragma unroll
  for (int ks = 0; ks < PD; ++ks) b[ks] = bld<SC1>(rs, l16, bs + ks * KB);
  const u32x2 st0 = __builtin_amdgcn_raw_buffer_load_b64(rs, (mm >> 3) * 1024 + g * 128 + (mm & 7) * 8, lb + QO_ST + mt * 2048, SC1);
  const u32x2 st1 = __builtin_amdgcn_raw_buffer_load_b64(rs, (mm >> 3) * 1024 + g * 128 + (mm & 7) * 8 + 512, lb + QO_ST + mt * 2048, SC1);
  const f32x4 stp = __builtin_bit_cast(f32x4, (u32x4){st0[0], st0[1], st1[0], st1[1]});
  SB();
  f32x4 acc[7] = {};
  float rstd, nmr;
#pragma unroll
  for (int ks = 0; ks < 16; ++ks) {
    if (ks < 15) {
#pragma unroll
      for (int t = 0; t < 7; ++t) a[(ks + 1) & 1][t] = lds_frag(lds, AL_W1 + ((ks + 1) * 7 + t) * 1024 + l16);
    }
    if (ks + PD < 16) b[ks + PD] = bld<SC1>(rs, l16, bs + (ks + PD) * KB);
    {
      constexpr int NMID = 28 - PRE_EARLY - PRE_LATE;
      const int lo = ks <= 10 ? (ks * 6) / 11 : 6 + (ks - 11) * 2, hi = ks + 1 <= 10 ? ((ks + 1) * 6) / 11 : 6 + (ks + 1 - 11) * 2;
#pragma unroll
      for (int i = lo; i < hi; ++i) if (i < NMID) ffn2_prefetch(rs, l16, (idx & 7) * 4, w, stg, PRE_EARLY + i);
    }
    if (ks == 8) {
      float sm = stp[0] + stp[2], sq = stp[1] + stp[3];
      xrow_sum2(sm, sq);
      const float mean = sm * (1.f / D_);
      rstd = __builtin_amdgcn_rsqf(fmaxf(sq * (1.f / D_) - mean * mean, 0.f) + LN_EPS); nmr = -mean * rstd;
      PIN2(rstd, nmr);
    }
    SB();
#pragma unroll
    for (int t = 0; t < 7; ++t) acc[t] = mfma16(a[ks & 1][t], b[ks], acc[t]);
    SB();
  }
  u32x2 hv[7];
#pragma unroll
  for (int t = 0; t < 7; ++t) {
    const f32x4 v = acc[t] * rstd + *(const f32x4*)(lds + AL_CS + t * 64 + g * 16) * nmr;
    hv[t] = (u32x2){relu_pk(pk2(v[0], v[1])), relu_pk(pk2(v[2], v[3]))};
  }
  const unsigned hs = lb + QO_H + smul(mt, 56 * KB) + (nt0 >> 1) * KB;
  if ((cb & 1) == 0) {
#pragma unroll
    for (int p = 0; p < 3; ++p) bst4<SC1>((u32x4){hv[2 * p][0], hv[2 * p][1], hv[2 * p + 1][0], hv[2 * p + 1][1]}, rs, l16, hs + p * KB);
    bst2<SC1>(hv[6], rs, lane * 8, hs + 3 * KB);
  } else {
    bst2<SC1>(hv[0], rs, lane * 8 + 512, hs);
#pragma unroll
    for (int p = 0; p < 3; ++p) bst4<SC1>((u32x4){hv[2 * p + 1][0], hv[2 * p + 1][1], hv[2 * p + 2][0], hv[2 * p + 2][1]}, rs, l16, hs + (p + 1) * KB);
  }
  wg_signal(rs, flag_off(layer, 1, (xcd * 2 + rb) * 8 + (cb >> 1)) + (cb & 1) * 8, tid, tag);
}

DI void ffn2_phase(char* lds, char* ws, rsrc_t rs, u16* out, int layer, int tid, bf16x8 (&stg)[28], u64 tag) {
  const int lane = tid & 63, g = lane >> 4, mm = lane & 15;
  const int w = __builtin_amdgcn_readfirstlane(tid >> 6);
  const int bid = mybid(), xcd = bid & 7, idx = bid >> 3, rb = idx >> 3, cb = idx & 7;
  const int fm = w >> 1, fp = w & 1, mt0 = xcd * 16 + rb * 4, nt0 = cb * 4;
  const unsigned lds0 = (unsigned)(uintptr_t)lds, l16 = lane * 16;
#pragma unroll
  for (int i = 28 - PRE_LATE; i < 28; ++i) ffn2_prefetch(rs, l16, nt0, w, stg, i);
  const unsigned fs = ((mt0 + fm) * 16 + (nt0 >> 1) + fp) * KB;
  const unsigned lb = OFF_QKV + smul(layer, QKV_BYTES);
  const u32x4 xr = bld4<SC1>(rs, l16, lb + QO_X1 + fs);
  if (layer == 0) {
#pragma unroll
    for (int i = 0; i < 6; ++i) { const int c = 6 * w + i; dma(rs, l16, OFF_WQKV + ((4 * cb + (c & 3)) * 16 + (c >> 2)) * KB, lds0 + c * 1024); }
  }
  wave_wait(ws, flag_off(layer, 1, (xcd * 2 + (rb >> 1)) * 8 + w), (lane & 1) * 8, tag);
  constexpr int PD = 6;
  bf16x8 b[28];
  auto ldb = [&](int i) {
    const unsigned so = lb + QO_H + smul(mt0 + ((fm + 1 + i / 7) & 3), 56 * KB) + smul(w, 7 * KB) + (i % 7) * KB;
    if (i % 7 == 3) {
      const u32x2 lo = __builtin_amdgcn_raw_buffer_load_b64(rs, lane * 8, so, SC1), hi = __builtin_amdgcn_raw_buffer_load_b64(rs, lane * 8 + 512, so, SC1);
      b[i] = __builtin_bit_cast(bf16x8, (u32x4){lo[0], lo[1], hi[0], hi[1]});
    } else b[i] = bld<SC1>(rs, l16, so);
  };
#pragma unroll
  for (int i = 0; i < PD; ++i) ldb(i);
  SB();
  f32x4 acc[2][4];
  auto send = [&](int mi, int pr) {
    const int d = 2 * ((fm + 1 + mi) & 3) + (pr ^ fp);
    char* xb = lds + AL_W1 + (d * 7 + ((w - d - 1) & 7)) * 2048 + l16;
    *(f32x4*)(xb) = acc[mi & 1][2 * pr]; *(f32x4*)(xb + 1024) = acc[mi & 1][2 * pr + 1];
  };
#pragma unroll
  for (int i = 0; i < 28; ++i) {
    const int mi = i / 7, j = i % 7;
    if (i + PD < 28) ldb(i + PD);
    if (mi > 0 && j == 3) send(mi - 1, 0);
    if (mi > 0 && j == 5) send(mi - 1, 1);
    SB();
#pragma unroll
    for (int t = 0; t < 4; ++t) {
      if (j == 0) { const f32x4 z = {}; acc[mi & 1][t] = mfma16(stg[t], b[i], z); }
      else acc[mi & 1][t] = mfma16(stg[4 * j + t], b[i], acc[mi & 1][t]);
    }
    SB();
  }
  send(3, 1);
  f32x4 s0 = acc[1][0] + (f32x4){bflo(xr[0]), bfhi(xr[0]), bflo(xr[1]), bfhi(xr[1])};
  f32x4 s1 = acc[1][1] + (f32x4){bflo(xr[2]), bfhi(xr[2]), bflo(xr[3]), bfhi(xr[3])};
  raw_barrier();
#pragma unroll
  for (int d = 0; d < 7; ++d) {
    const char* xb = lds + AL_W1 + (w * 7 + d) * 2048 + l16;
    s0 += *(const f32x4*)(xb); s1 += *(const f32x4*)(xb + 1024);
  }
  const u32x4 o = {pk2(s0[0], s0[1]), pk2(s0[2], s0[3]), pk2(s1[0], s1[1]), pk2(s1[2], s1[3])};
  if (layer == 1) {
    u16* op = out + (size_t)((mt0 + fm) * 16 + mm) * D_ + (nt0 + 2 * fp) * 16 + 4 * g;
    *(u32x2*)op = (u32x2){o[0], o[1]};
    *(u32x2*)(op + 16) = (u32x2){o[2], o[3]};
    asm volatile("" : "+s"(tag));
    bst2<SC1>((u32x2){(unsigned)tag, (unsigned)(tag >> 32)}, rs, (bid | tid) == 0 ? 0u : OOB, OFF_EPOCH);
  } else {
    bst4<SC1>(o, rs, l16, OFF_X2 + fs);
    wg_signal(rs, flag_off(0, 2, (xcd * 4 + rb) * 2 + (cb >> 2)) + (cb & 3) * 8, tid, tag);
  }
}

typedef short v4i16_t __attribute__((ext_vector_type(4)));
DI bf16x8 tr_frag(const char* lds, int off0, int off1) {
  const v4i16_t lo = __builtin_amdgcn_ds_read_tr16_b64_v4i16((LAS v4i16_t*)((LAS char*)lds + off0));
  const v4i16_t hi = __builtin_amdgcn_ds_read_tr16_b64_v4i16((LAS v4i16_t*)((LAS char*)lds + off1));
  return __builtin_shufflevector(lo, hi, 0, 1, 2, 3, 4, 5, 6, 7);
}
constexpr int QL_KV = 65536, QL_P = 81920, QL_TR = QL_P + 65536;
constexpr int QP_T = 0, QP_KV = 2048, QP_CS = 3072, QP_CK = 3136, QP_ST = 3200;
template <bool FIRST, class H1, class TG>
DI void qkv_phase(char* lds, char* ws, rsrc_t rs, int tid, const u16* x, const u16* wq, const u16* wkv, H1 hook1, TG gettag) {
  const int lane = tid & 63, g = lane >> 4, mm = lane & 15;
  const int w = __builtin_amdgcn_readfirstlane(tid >> 6);
  const int bid = mybid(), xcd = bid & 7, idx = bid >> 3, rb = idx >> 3, cb = idx & 7;
  const int ml = w & 3, hf = w >> 2, mt = xcd * 16 + rb * 4 + ml;
  const int kvml = cb >> 1;
  const unsigned lds0 = (unsigned)(uintptr_t)lds, l16 = lane * 16;
  const unsigned qkv = OFF_QKV + (FIRST ? 0 : QKV_BYTES);
  char* const mine = lds + QL_P + w * 8192;
  const char* const other = lds + QL_P + (w ^ 4) * 8192;
  bf16x8 b[8];
  f32x4 cs0, cs1, csk; float csv;
  if (FIRST) {
    const rsrc_t rw = __builtin_amdgcn_make_buffer_rsrc((void*)wq, 0, D_ * D_ * 2, 0x00020000);
    const rsrc_t rk = __builtin_amdgcn_make_buffer_rsrc((void*)wkv, 0, D_ * 16 * 2, 0x00020000);
    const rsrc_t rx = __builtin_amdgcn_make_buffer_rsrc((void*)x, 0, S_ * D_ * 2, 0x00020000);
#pragma unroll
    for (int i = 0; i < 2; ++i) dma(rk, l16, (2 * w + i) * 1024, lds0 + QL_KV + (2 * w + i) * 1024);
    const unsigned vx = (lane >> 5) * 1024 + (((lane & 31) ^ (lane >> 5)) << 4);
#pragma unroll
    for (int i = 0; i < 8; ++i) dma(rx, vx ^ (i << 5), mt * 16384 + i * 2048 + hf * 512, lds0 + QL_P + w * 8192 + i * 1024);
    const unsigned v0 = (lane >> 3) * 1024 + (((lane & 7) ^ ((lane >> 3) & 2)) << 4), v1 = v0 ^ 64;
#pragma unroll
    for (int i = 0; i < 8; ++i) dma(rw, (i & 1) ? v1 : v0, (8 * w + i) * 8192 + cb * 128, lds0 + (8 * w + i) * 1024);
    wait_vm<0>();
#pragma unroll
    for (int j = 0; j < 8; ++j) b[j] = lds_frag(mine, mm * 512 + (((4 * j + g) ^ mm) << 4));
  } else {
#pragma unroll
    for (int i = 0; i < 2; ++i) {
      const int c = 48 + 2 * w + i;
      dma(rs, l16, OFF_WQKV + ((4 * cb + (c & 3)) * 16 + (c >> 2)) * KB, lds0 + c * 1024);
      dma(rs, l16, OFF_WQKV + ((32 + (cb & 1)) * 16 + 2 * w + i) * KB, lds0 + QL_KV + (2 * w + i) * 1024);
    }
    cs0 = bld4f<SC1>(rs, g * 16, OFF_CSUM + (4 * cb + 2 * hf) * 64); cs1 = bld4f<SC1>(rs, g * 16 + 64, OFF_CSUM + (4 * cb + 2 * hf) * 64);
    csk = bld4f<SC1>(rs, g * 16, OFF_CSUM + 32 * 64);
    csv = __uint_as_float(bld1<SC1>(rs, mm * 4, OFF_CSUM + 33 * 64));
    wave_wait(ws, flag_off(0, 2, (xcd * 4 + rb) * 2 + hf), (lane & 3) * 8, gettag());
#pragma unroll
    for (int j = 0; j < 8; ++j) b[j] = bld<SC1>(rs, l16, OFF_X2 + (mt * 16 + 8 * hf + j) * KB);
  }
  raw_barrier();
  hook1();
  const int q4 = (lane >> 2) & 3, p4 = lane & 3;
  const int fsw = ((q4 >> 1) & 1) | ((g & 1) << 1);
  const int abase = FIRST ? hf * 32768 + (8 * g + q4) * 128 + p4 * 8 : hf * 32768 + l16;
  auto aq = [&](int t  ) { return FIRST ? abase + ((t ^ fsw) << 5) : abase + t * 1024; };
  auto ldq = [&](int base, int j) { return FIRST ? tr_frag(lds, base + j * 4096, base + j * 4096 + 512) : lds_frag(lds, base + j * 4096); };
  const int kvb = QL_KV + hf * 8192 + (FIRST ? (8 * g + q4) * 32 + p4 * 8 : l16);
  auto ldkv = [&](int j) { return FIRST ? tr_frag(lds, kvb + j * 1024, kvb + j * 1024 + 128) : lds_frag(lds, kvb + j * 1024); };
  bf16x8 ones; for (int i = 0; i < 8; ++i) ones[i] = 0x3F80;
  if (FIRST) {
    const int cbs = aq(ml);
    f32x4 c0 = {}, c1 = {}, ck = {};
#pragma unroll
    for (int j = 0; j < 8; j += 2) { c0 = mfma16(ldq(cbs, j), ones, c0); c1 = mfma16(ldq(cbs, j + 1), ones, c1); }
    const int kv2 = kvb + ml * 2048;
#pragma unroll
    for (int j = 0; j < 2; ++j) ck = mfma16(FIRST ? tr_frag(lds, kv2 + j * 1024, kv2 + j * 1024 + 128) : lds_frag(lds, kv2 + j * 1024), ones, ck);
    if (mm == 0) { *(f32x4*)(mine + QP_CS + g * 16) = c0 + c1; *(f32x4*)(mine + QP_CK + g * 16) = ck; }
  }
  f32x4 acc[4] = {}, accs = {}, accg = {}, akv = {};
  bf16x8 a[2][4];
  const int ab0 = aq(2 * hf), ab1 = aq(2 * hf + 1), ab2 = aq(2 - 2 * hf), ab3 = aq(3 - 2 * hf);
  auto lda = [&](int j) { a[j & 1][0] = ldq(ab0, j); a[j & 1][1] = ldq(ab1, j); a[j & 1][2] = ldq(ab2, j); a[j & 1][3] = ldq(ab3, j); };
#pragma unroll
  for (int j = 0; j < 8; ++j) { accs = mfma16(ones, b[j], accs); accg = mfma16(b[j], b[j], accg); }
  if (ml == kvml) {
    if (cb & 1) {
#pragma unroll
      for (int j = 0; j < 8; ++j) akv = mfma16(b[j], ldkv(j), akv);
    } else {
#pragma unroll
      for (int j = 0; j < 8; ++j) akv = mfma16(ldkv(j), b[j], akv);
    }
    *(f32x4*)(mine + QP_KV + l16) = akv;
  }
  {
    const float s01 = (mm & 1) ? accg[1] : accg[0], s23 = (mm & 1) ? accg[3] : accg[2];
    if (g == (mm >> 2)) *(f32x2*)(mine + QP_ST + mm * 8) = (f32x2){accs[0], (mm & 2) ? s23 : s01};
  }
  lda(0);
  raw_barrier();
  float invd = 1.f / D_; PIN1(invd);
  auto stats = [&](int tokl, float& mean, float& rstd) {
    const f32x2 s = *(const f32x2*)(mine + QP_ST + tokl * 8) + *(const f32x2*)(other + QP_ST + tokl * 8);
    mean = s[0] * invd;
    rstd = __builtin_amdgcn_rsqf(fmaxf(s[1] * invd - mean * mean, 0.f) + LN_EPS);
  };
  float mean, rstd; stats(mm, mean, rstd);
  const bool kvw = hf == 1 && ml == kvml;
  if (kvw) {
    const f32x4 skv = akv + *(const f32x4*)(other + QP_KV + l16);
    if (cb & 1) {
      if (FIRST) {
        csv = 0.f;
#pragma unroll
        for (int i = 0; i < 8; ++i) csv += *(const float*)(lds + QL_P + i * 8192 + QP_CK + mm * 4);
        if (mt == 0) bst1<SC1>(__float_as_uint(csv), rs, g == 0 ? mm * 4 : OOB, OFF_CSUM + 33 * 64);
      }
      float v[4];
#pragma unroll
      for (int i = 0; i < 4; ++i) { float mn, rsd; stats(4 * g + i, mn, rsd); v[i] = (skv[i] - mn * csv) * rsd; }
      const u32x2 o = {pk2(v[0], v[1]), pk2(v[2], v[3])};
      bst2<SC1>(o, rs, (g & 1) * 272 + mm * 16 + (g >> 1) * 8, qkv + QO_VT + smul(mt, 640));
      bst2<SC1>((u32x2){0x3F803F80u, 0x3F803F80u}, rs, mm == 0 ? (g & 1) * 272 + 256 + (g >> 1) * 8 : OOB, qkv + QO_VT + smul(mt, 640));
    } else {
      if (FIRST) {
        csk = (f32x4){};
#pragma unroll
        for (int i = 0; i < 8; ++i) csk += *(const f32x4*)(lds + QL_P + i * 8192 + QP_CK + g * 16);
        if (mt == 0) bst4<SC1>(__builtin_bit_cast(u32x4, csk), rs, mm == 0 ? g * 16 : OOB, OFF_CSUM + 32 * 64);
      }
      const f32x4 k = (skv - csk * mean) * rstd;
      const u32x2 o = {pk2(k[0], k[1]), pk2(k[2], k[3])};
      bst2<SC1>(o, rs, mm * 32 + g * 8, qkv + QO_K + mt * 512);
      const unsigned n2 = row16_umax(__float_as_uint(xrow_sum(sq4(o))));
      bst1<SC1>(n2, rs, lane == 0 ? 0u : OOB, qkv + QO_KN2 + mt * 128);
    }
  }
#pragma unroll
  for (int j = 0; j < 8; ++j) {
    if (j < 7) lda(j + 1);
    SB();
#pragma unroll
    for (int t = 0; t < 4; ++t) acc[t] = mfma16(a[j & 1][t], b[j], acc[t]);
    SB();
  }
  *(f32x4*)(mine + QP_T + l16) = acc[2];
  *(f32x4*)(mine + QP_T + 1024 + l16) = acc[3];
  const unsigned fl = (xcd * 4 + rb) * 2 + (cb >> 2);
  if (kvw) {
    asm volatile("s_waitcnt vmcnt(0)" ::: "memory");
    u64 tag = gettag(); asm volatile("" : "+s"(tag));
    bst2<SC1>((u32x2){(unsigned)tag, (unsigned)(tag >> 32)}, rs, lane == 0 ? 0u : OOB, flag_off(FIRST ? 0 : 1, 3, fl) + (cb & 3) * 8);
  }
  raw_barrier();
  const char* csp = lds + QL_P + 2 * hf * 8192 + QP_CS + g * 16;
  if (FIRST) {
    cs0 = *(const f32x4*)(csp) + *(const f32x4*)(csp + 32768); cs1 = *(const f32x4*)(csp + 8192) + *(const f32x4*)(csp + 8192 + 32768);
    if (mt == 0) {
      bst4<SC1>(__builtin_bit_cast(u32x4, cs0), rs, mm == 0 ? g * 16 : OOB, OFF_CSUM + (4 * cb + 2 * hf) * 64);
      bst4<SC1>(__builtin_bit_cast(u32x4, cs1), rs, mm == 0 ? g * 16 + 64 : OOB, OFF_CSUM + (4 * cb + 2 * hf) * 64);
    }
  }
  const f32x4 s0 = acc[0] + *(const f32x4*)(other + QP_T + l16), s1 = acc[1] + *(const f32x4*)(other + QP_T + 1024 + l16);
  const float rq = rstd * QSCALE, nmq = -mean * rq;
  const f32x4 q0 = s0 * rq + cs0 * nmq, q1 = s1 * rq + cs1 * nmq;
  const u32x2 o0 = {pk2(q0[0], q0[1]), pk2(q0[2], q0[3])}, o1 = {pk2(q1[0], q1[1]), pk2(q1[2], q1[3])};
  {
    auto s0_ = __builtin_amdgcn_permlane16_swap(o0[0], o1[0], false, false);
    auto s1_ = __builtin_amdgcn_permlane16_swap(o0[1], o1[1], false, false);
    bst4<SC1>((u32x4){s0_[0], s1_[0], s0_[1], s1_[1]}, rs, (mm >> 3) * 8192 + ((g >> 1) * 32 + (2 * hf + (g & 1)) * 8 + (mm & 7)) * 16, qkv + QO_Q + mt * 16384 + cb * 1024);
  }
  float n0 = sq4(o0), n1 = sq4(o1);
  xrow_sum2(n0, n1);
  const unsigned qn = row16_umax(umax(__float_as_uint(n0), __float_as_uint(n1)));
  bst1<SC1>(qn, rs, lane == 0 ? 0u : OOB, qkv + QO_QN2 + mt * 2048 + (cb * 2 + hf) * 128);
  wg_signal(rs, flag_off(FIRST ? 0 : 1, 3, 64 + fl) + (cb & 3) * 8, tid, gettag());
}

DI void first_phase(char* lds, char* ws, rsrc_t rs, int tid, const u16* x, const u16* wq, const u16* wk, const u16* wv, const u16* wo,
                    const u16* w1, const u16* w2, u64& tag, u64 pc) {
  const int lane = tid & 63, g = lane >> 4;
  const int w = __builtin_amdgcn_readfirstlane(tid >> 6);
  const unsigned lds0 = (unsigned)(uintptr_t)lds, l16 = lane * 16;
  const int xcd = blockIdx.x & 7, idx = blockIdx.x >> 3, cb = idx & 7;
  u64 ep;
  const u16* tw; int tpitch, tks, tnb; unsigned tdst;
  auto fetch = [&]() {
    ep = flag_ld(ws, OFF_EPOCH, 0);
    const int bi = mybid() * 4 + (w & 3);
    if (bi < 128) { tw = wo; tpitch = 1024; tks = bi >> 3; tnb = bi & 7; tdst = OFF_WO; }
    else if (bi < 576) { const int cw = (bi - 128) >> 2, kq = cw / 28; tw = w1; tpitch = 3584; tks = 4 * kq + (w & 3); tnb = cw - 28 * kq; tdst = OFF_W1; }
    else { const int c = bi - 576; tw = w2; tpitch = 1024; tks = c >> 3; tnb = c & 7; tdst = OFF_W2; }
    if (w == 3) {
      const rsrc_t rc = __builtin_amdgcn_make_buffer_rsrc((void*)pc, 0, CODE_PF, 0x00020000);
      unsigned sv;
#pragma unroll
      for (int i = 0; i < (CODE_PF + 8191) / 8192; ++i)
        asm volatile("s_mov_b32 %0, m0\n\ts_mov_b32 m0, %1\n\ts_nop 0\n\tbuffer_load_dword %2, %3, %4 offen lds\n\ts_mov_b32 m0, %0"
                     : "=&s"(sv) : "s"(lds0 + QL_P + 7 * 8192 + 6144), "v"(lane * 128), "s"(rc), "s"(i * 8192) : "memory");
    }
    if (w < 4) {
      const rsrc_t rt = __builtin_amdgcn_make_buffer_rsrc((void*)tw, 0, D_ * DFF * 2, 0x00020000);
      const unsigned v = (lane >> 3) * tpitch + (lane & 7) * 16;
#pragma unroll
      for (int i = 0; i < 4; ++i) dma(rt, v, (32 * tks + 8 * i) * tpitch + tnb * 128, lds0 + QL_TR + w * 4096 + i * 1024);
    }
  };
  auto gettag = [&]() {
    const u64 e = ep + 1ull;
    tag = ((u64)(unsigned)__builtin_amdgcn_readfirstlane((int)(e >> 32)) << 32) | (unsigned)__builtin_amdgcn_readfirstlane((int)e);
    return tag;
  };
  auto convert = [&]() {
    const int q4 = (lane >> 2) & 3, p4 = lane & 3;
    if (w < 4) {
      const int base = QL_TR + w * 4096 + (4 * g + q4) * 128 + p4 * 8;
      bf16x8 ones; for (int i = 0; i < 8; ++i) ones[i] = 0x3F80;
#pragma unroll
      for (int t = 0; t < 4; ++t) {
        const bf16x8 f = tr_frag(lds, base + t * 32, base + t * 32 + 2048);
        const int nt = 4 * tnb + t, sl = nt / 7;
        const unsigned ch = tdst == OFF_WO ? (tnb * 16 + tks) * 4 + t : tdst == OFF_W1 ? smul(sl * 16 + tks, 7) + (nt - smul(sl, 7)) : smul(nt, 56) + tks;
        bst4<SC1>(__builtin_bit_cast(u32x4, f), rs, l16, tdst + ch * KB);
        const f32x4 z = {};
        const f32x4 c = mfma16(f, ones, z);
        if ((lane & 15) == 0) *(f32x4*)(lds + QL_P + w * 8192 + 4096 + t * 64 + g * 16) = c;
      }
    } else if (w < 6) {
      const int c = (xcd * 4 + (idx >> 3)) * 2 + (w & 1), ks = c >> 2, t = c & 3;
      const int r0 = 4 * g + q4, f0 = ((r0 >> 1) & 1) | (((r0 >> 3) & 1) << 1);
      const int base = (32 * ks + r0) * 128 + ((t ^ f0) << 5) + p4 * 8;
      bst4<SC1>(__builtin_bit_cast(u32x4, tr_frag(lds, base, base + 2048)), rs, l16, OFF_WQKV + ((4 * cb + t) * 16 + ks) * KB);
    } else if (w == 6) {
      const int ks = xcd * 4 + (idx >> 3);
      if (cb < 2 && ks < 16) {
        const int base = QL_KV + (32 * ks + 4 * g + q4) * 32 + p4 * 8;
        bst4<SC1>(__builtin_bit_cast(u32x4, tr_frag(lds, base, base + 512)), rs, l16, OFF_WQKV + ((32 + cb) * 16 + ks) * KB);
      }
    }
  };
  auto colsum = [&]() {
    if (w == 7 && tdst == OFF_W1) {
      const float* c = (const float*)(lds + QL_P + 4096) + lane;
      bst1<SC1>(__float_as_uint((c[0] + c[2048]) + (c[4096] + c[6144])), rs, lane * 4, OFF_CS1 + (tks >> 2) * 7168 + tnb * 256);
    }
  };
  qkv_phase<true>(lds, ws, rs, tid, x, wq, (cb & 1) ? wv : wk, fetch, gettag);
  convert();
  raw_barrier();
  colsum();
}

DI int mytid(int w64) { int t; asm volatile("v_mbcnt_lo_u32_b32 %0, -1, %1\n\tv_mbcnt_hi_u32_b32 %0, -1, %0" : "=v"(t) : "s"(w64)); return t; }
__global__ __launch_bounds__(512, 2) void k_main(const u16* __restrict__ x, const u16* __restrict__ wq, const u16* __restrict__ wk, const u16* __restrict__ wv,
                                                 const u16* __restrict__ wo, const u16* __restrict__ w1, const u16* __restrict__ w2,
                                                 char* __restrict__ ws, u16* __restrict__ out) {
  extern __shared__ __attribute__((aligned(16))) char lds[];
  const int w64 = __builtin_amdgcn_readfirstlane(threadIdx.x & ~63);
  const rsrc_t rs = __builtin_amdgcn_make_buffer_rsrc((void*)ws, 0, WS_NEED, 0x00020000);
  u64 tag, pc;
  asm volatile("s_getpc_b64 %0" : "=s"(pc));
  if (w64 >= 512 - 64 * PF_EARLY) {
    const rsrc_t rc = __builtin_amdgcn_make_buffer_rsrc((void*)pc, 0, CODE_PF, 0x00020000);
    unsigned sv;
    asm volatile("s_mov_b32 %0, m0\n\ts_mov_b32 m0, %1\n\ts_nop 0\n\tbuffer_load_dword %2, %3, 0 offen lds\n\ts_mov_b32 m0, %0"
                 : "=&s"(sv) : "s"((unsigned)(uintptr_t)lds + QL_TR), "v"((511 - threadIdx.x) * 128), "s"(rc) : "memory");
  }
  first_phase(lds, ws, rs, mytid(w64), x, wq, wk, wv, wo, w1, w2, tag, pc);
#pragma unroll 1
  for (int layer = 0; layer < 2; ++layer) {
    bf16x8 stg[28];
    attn_phase(lds, ws, rs, x, layer, mytid(w64), tag);
    ffn1_phase(lds, ws, rs, layer, mytid(w64), stg, tag);
    ffn2_phase(lds, ws, rs, out, layer, mytid(w64), stg, tag);
    if (layer == 0) qkv_phase<false>(lds, ws, rs, mytid(w64), nullptr, nullptr, nullptr, []() {}, [&]() { return tag; });
  }
}

extern "C" void kernel_launch(void* const* d_in, const int* in_sizes, int n_in, void* d_out, int out_size, void* d_ws, size_t ws_size,
                              hipStream_t stream) {
  const u16* x = (const u16*)d_in[0];
  const u16* wq = (const u16*)d_in[1];
  const u16* wk = (const u16*)d_in[2];
  const u16* wv = (const u16*)d_in[3];
  const u16* wo = (const u16*)d_in[4];
  const u16* w1 = (const u16*)d_in[6];
  const u16* w2 = (const u16*)d_in[7];
  char* ws = (char*)d_ws;
  u16* out = (u16*)d_out;
  (void)hipFuncSetAttribute((const void*)k_main, hipFuncAttributeMaxDynamicSharedMemorySize, LDS_BYTES);
  k_main<<<256, 512, LDS_BYTES, stream>>>(x, wq, wk, wv, wo, w1, w2, ws, out);
}
```

```cpp
#include <hip/hip_runtime.h>
#include <hip/hip_bf16.h>
#include <stdint.h>
#include <type_traits>

typedef __attribute__((ext_vector_type(8))) short bf16x8;
typedef __attribute__((ext_vector_type(2))) float f32x2;
typedef __attribute__((ext_vector_type(4))) float f32x4;
typedef __attribute__((ext_vector_type(16))) float f32x16;
typedef __attribute__((ext_vector_type(4))) unsigned u32x4;
typedef __attribute__((ext_vector_type(2))) unsigned u32x2;
typedef unsigned short u16;
typedef __amdgpu_buffer_rsrc_t rsrc_t;
#define DI __device__ __forceinline__
#define LAS __attribute__((address_space(3)))

constexpr int S_ = 2048, D_ = 512, DFF = 1792;
constexpr float QSCALE = 0.25f * 1.4426950408889634f;
constexpr float LN_EPS = 1e-5f;

constexpr unsigned KB = 1024;
constexpr unsigned OFF_FLAG = 0;
constexpr unsigned OFF_EPOCH = 128 * KB;
constexpr unsigned FLAG_BYTES = 129 * KB;
constexpr unsigned OFF_WQKV = OFF_FLAG + FLAG_BYTES;
constexpr unsigned OFF_WO   = OFF_WQKV + 544 * KB;
constexpr unsigned OFF_W1   = OFF_WO + 512 * KB;
constexpr unsigned OFF_W2   = OFF_W1 + 1792 * KB;
constexpr unsigned OFF_CSUM = OFF_W2 + 1792 * KB;
constexpr unsigned OFF_CS1  = OFF_CSUM + 4 * KB;
constexpr unsigned OFF_X2   = OFF_CS1 + 28 * KB;
constexpr unsigned OFF_QKV  = OFF_X2 + 2048 * KB;
constexpr unsigned QO_X1 = 0;
constexpr unsigned QO_ST = QO_X1 + 2048 * KB;
constexpr unsigned QO_H  = QO_ST + 256 * KB;
constexpr unsigned QO_Q  = QO_H + 7168 * KB;
constexpr unsigned QO_K = QO_Q + 2048 * KB;
constexpr unsigned QO_VT = QO_K + 64 * KB;
constexpr unsigned QO_KN2 = QO_VT + 80 * KB;
constexpr unsigned QO_QN2 = QO_KN2 + 16 * KB;
constexpr unsigned QKV_BYTES = QO_QN2 + 256 * KB;
constexpr unsigned WS_NEED  = OFF_QKV + 2 * QKV_BYTES;
DI unsigned flag_off(int layer, int edge, int line) { return OFF_FLAG + ((layer * 4 + edge) * 128 + line) * 128; }

DI float bflo(unsigned v) { return __uint_as_float(v << 16); }
DI float bfhi(unsigned v) { return __uint_as_float(v & 0xffff0000u); }
DI unsigned pk2(float lo, float hi) {
  typedef __attribute__((ext_vector_type(2))) __bf16 bf16x2_;
  f32x2 v = {lo, hi};
  bf16x2_ r = __builtin_convertvector(v, bf16x2_);
  return __builtin_bit_cast(unsigned, r);
}
DI unsigned relu_pk(unsigned v) {
  typedef __attribute__((ext_vector_type(2))) short s16x2;
  const s16x2 z = {0, 0};
  return __builtin_bit_cast(unsigned, __builtin_elementwise_max(__builtin_bit_cast(s16x2, v), z));
}
DI f32x4 mfma16(bf16x8 a, bf16x8 b, f32x4 c) {
  typedef __attribute__((ext_vector_type(8))) __bf16 b8;
  return __builtin_amdgcn_mfma_f32_16x16x32_bf16(__builtin_bit_cast(b8, a), __builtin_bit_cast(b8, b), c, 0, 0, 0);
}
DI f32x16 mfma32(bf16x8 a, bf16x8 b, f32x16 c) {
  typedef __attribute__((ext_vector_type(8))) __bf16 b8;
  return __builtin_amdgcn_mfma_f32_32x32x16_bf16(__builtin_bit_cast(b8, a), __builtin_bit_cast(b8, b), c, 0, 0, 0);
}
template <int N> DI void wait_vm() { asm volatile("s_waitcnt vmcnt(%0)" :: "i"(N) : "memory"); }
DI void raw_barrier() { asm volatile("s_waitcnt lgkmcnt(0)\n\ts_barrier" ::: "memory"); }
DI bf16x8 lds_frag(const char* lds, int off) { return *(const LAS bf16x8*)((const LAS char*)lds + off); }

template <int CTRL> DI float dpp(float x) { return __builtin_bit_cast(float, __builtin_amdgcn_mov_dpp(__builtin_bit_cast(int, x), CTRL, 0xf, 0xf, true)); }
template <int CTRL> DI unsigned dppu(unsigned x) { return (unsigned)__builtin_amdgcn_mov_dpp((int)x, CTRL, 0xf, 0xf, true); }
DI unsigned umax(unsigned a, unsigned b) { return a > b ? a : b; }
DI unsigned row16_umax(unsigned x) { x = umax(x, dppu<0xB1>(x)); x = umax(x, dppu<0x4E>(x)); x = umax(x, dppu<0x141>(x)); x = umax(x, dppu<0x140>(x)); return x; }
DI float xrow_sum(float x) {
  auto s = __builtin_amdgcn_permlane16_swap(__float_as_uint(x), __float_as_uint(x), false, false);
  x = __uint_as_float(s[0]) + __uint_as_float(s[1]);
  auto t = __builtin_amdgcn_permlane32_swap(__float_as_uint(x), __float_as_uint(x), false, false);
  return __uint_as_float(t[0]) + __uint_as_float(t[1]); }
DI void xrow_sum2(float& a, float& b) {
  auto s = __builtin_amdgcn_permlane16_swap(__float_as_uint(a), __float_as_uint(b), false, false);
  const float x = __uint_as_float(s[0]) + __uint_as_float(s[1]);
  auto t = __builtin_amdgcn_permlane32_swap(__float_as_uint(x), __float_as_uint(x), false, false);
  const float y = __uint_as_float(t[0]) + __uint_as_float(t[1]);
  auto u = __builtin_amdgcn_permlane16_swap(__float_as_uint(y), __float_as_uint(y), false, false);
  a = __uint_as_float(u[0]); b = __uint_as_float(u[1]); }
DI float sq4(u32x2 o) { return bflo(o[0]) * bflo(o[0]) + bfhi(o[0]) * bfhi(o[0]) + bflo(o[1]) * bflo(o[1]) + bfhi(o[1]) * bfhi(o[1]); }
#define SB() __builtin_amdgcn_sched_barrier(0)
#define EX(x) __builtin_amdgcn_exp2f(x)
#define PIN1(a) asm volatile("" : "+v"(a))
#define PIN2(a, b) asm volatile("" : "+v"(a), "+v"(b))
#define PIN4(a, b, c, d) asm volatile("" : "+v"(a), "+v"(b), "+v"(c), "+v"(d))
constexpr int SC1 = 16;
DI void mfma16_ip(f32x4& acc, bf16x8 a, bf16x8 b) { asm volatile("v_mfma_f32_16x16x32_bf16 %0, %1, %2, %0" : "+v"(acc) : "v"(a), "v"(b)); }
template <int IMM> DI void bld_asm(bf16x8& d, rsrc_t rs, unsigned voff, unsigned soff) { asm volatile("buffer_load_dwordx4 %0, %1, %2, %3 offen offset:%4 sc1" : "+v"(d) : "v"(voff), "s"(rs), "s"(soff), "i"(IMM)); }
template <int N> DI void mfma16_w(f32x4& acc, bf16x8& a, bf16x8 b) { asm volatile("s_waitcnt vmcnt(%3)\n\tv_mfma_f32_16x16x32_bf16 %0, %1, %2, %0" : "+v"(acc), "+v"(a) : "v"(b), "i"(N)); }
template <int AUX = 0> DI bf16x8 bld(rsrc_t rs, unsigned voff, unsigned soff) { return __builtin_bit_cast(bf16x8, __builtin_amdgcn_raw_buffer_load_b128(rs, voff, soff, AUX)); }
template <int AUX = 0> DI u32x4 bld4(rsrc_t rs, unsigned voff, unsigned soff) { return __builtin_amdgcn_raw_buffer_load_b128(rs, voff, soff, AUX); }
template <int AUX = 0> DI f32x4 bld4f(rsrc_t rs, unsigned voff, unsigned soff) { return __builtin_bit_cast(f32x4, __builtin_amdgcn_raw_buffer_load_b128(rs, voff, soff, AUX)); }
template <int AUX = 0> DI unsigned bld1(rsrc_t rs, unsigned voff, unsigned soff) { return __builtin_amdgcn_raw_buffer_load_b32(rs, voff, soff, AUX); }
template <int AUX = 0> DI void bst4(u32x4 v, rsrc_t rs, unsigned voff, unsigned soff) {
  __builtin_amdgcn_raw_buffer_store_b128(v, rs, voff, soff, AUX);
  asm volatile("s_nop 1" :: "v"(v) : "memory");
}
template <int AUX = 0> DI void bst2(u32x2 v, rsrc_t rs, unsigned voff, unsigned soff) { __builtin_amdgcn_raw_buffer_store_b64(v, rs, voff, soff, AUX); }
template <int AUX = 0> DI void bst1(unsigned v, rsrc_t rs, unsigned voff, unsigned soff) { __builtin_amdgcn_raw_buffer_store_b32(v, rs, voff, soff, AUX); }
constexpr unsigned OOB = 0x7ffffff0u;
DI void dma(rsrc_t rs, unsigned voff, unsigned soff, unsigned lds_dst) {
  asm volatile("s_mov_b32 m0, %0\n\ts_nop 0\n\tbuffer_load_dwordx4 %1, %2, %3 offen sc1 lds"
               :: "s"(lds_dst), "v"(voff), "s"(rs), "s"(soff) : "m0", "memory");
}
typedef unsigned long long u64;
typedef __attribute__((address_space(1))) u64 gu64;
DI void wg_signal(rsrc_t rs, unsigned foff, int tid, u64 tag) {
  asm volatile("s_waitcnt vmcnt(0)" ::: "memory");
  raw_barrier();
  asm volatile("" : "+s"(tag));
  bst2<SC1>((u32x2){(unsigned)tag, (unsigned)(tag >> 32)}, rs, tid == 0 ? 0u : OOB, foff);
}
DI u64 flag_ld(char* ws, unsigned foff, unsigned lane_off) { return __hip_atomic_load((gu64*)(ws + foff + lane_off), __ATOMIC_RELAXED, __HIP_MEMORY_SCOPE_AGENT); }
DI void wave_wait(char* ws, unsigned foff, unsigned lane_off, u64 tag) {
#pragma unroll 1
  for (unsigned spins = 0; spins < (1u << 17); ++spins) {
    if (__all(flag_ld(ws, foff, lane_off) == tag)) break;
    __builtin_amdgcn_s_sleep(1);
  }
  asm volatile("" ::: "memory");
}

DI unsigned smul(unsigned a, unsigned b) { unsigned r; asm("s_mul_i32 %0, %1, %2" : "=s"(r) : "s"(a), "s"(b)); return r; }
DI int mybid() { int b = blockIdx.x; asm volatile("" : "+s"(b)); return b; }

constexpr int CODE_PF = 38912, PF_EARLY = 2;

constexpr int LDS_BYTES = 163840;
constexpr int AL_RO = 0;
constexpr int AL_RL = AL_RO + 32768;
constexpr int AL_O  = AL_RL + 2048;
constexpr int AL_Q  = AL_O + 4096;
constexpr int AL_FL = AL_Q + 8192;
constexpr int AL_BND = AL_FL + 256;
constexpr int AL_CS = AL_BND + 512;
constexpr int AL_W1 = 49152;
constexpr int AL_MX = AL_W1;
static_assert(AL_CS + 512 <= AL_W1 && AL_W1 + 112 * 1024 <= LDS_BYTES, "lds");
constexpr float SHIFT = 32.f;

constexpr int PRE_EARLY = 8, PRE_LATE = 4;
DI void ffn2_prefetch(rsrc_t rs, unsigned l16, int nt0, int w, bf16x8 (&stg)[28], int i) {
  const int ti = i & 3;
  stg[i] = bld<SC1>(rs, l16, OFF_W2 + smul(nt0 + ((ti & 2) ^ (2 * (w & 1))), 56 * KB) + smul(w, 7 * KB) + ((ti & 1) * 56 + (i >> 2)) * KB);
}

DI void attn_phase(char* lds, char* ws, rsrc_t rs, const u16* xin, int layer, int tid, u64 tag) {
  const int lane = tid & 63, r = lane & 31, h = lane >> 5;
  const int w = __builtin_amdgcn_readfirstlane(tid >> 6);
  const int bid = mybid(), xcd = bid & 7, idx = bid >> 3;
  const int t0 = xcd * 256 + idx * 8;
  const unsigned lds0 = (unsigned)(uintptr_t)lds;
  const unsigned qkv = OFF_QKV + smul(layer, QKV_BYTES);
  const unsigned l16 = lane * 16;
  const int f1_nt0 = smul(idx & 15, 7);
  wave_wait(ws, flag_off(layer, 3, 0), (h == 0 ? w * 8 + (lane >> 2) : 64 + (xcd * 4 + (idx >> 3)) * 2 + ((lane >> 2) & 1)) * 128 + (lane & 3) * 8, tag);
  const unsigned qs = qkv + QO_Q + t0 * 1024;
  bf16x8 qf = bld<SC1>(rs, l16, qs);
  SB();
  bf16x8 kf[8], vf[16], qn, qw;
  unsigned kn_a, qn_a, c1p;
  u64 wfl;
  const unsigned kb = qkv + QO_K + w * 8192, vb = qkv + QO_VT + smul(w, 16 * 640);
  const unsigned ko = r * 32 + 16 * h, vo = r <= 16 ? h * 272 + r * 16 : OOB;
  auto load_kv = [&](int t) {
    if (t < 7) kf[t + 1] = bld<SC1>(rs, ko + ((t + 1) & 3) * 1024, kb + ((t + 1) >> 2) * 4096);
    else qn = bld<SC1>(rs, l16 + 1024, qs);
    vf[2 * t] = bld<SC1>(rs, vo, vb + (2 * t) * 640);
    vf[2 * t + 1] = bld<SC1>(rs, vo, vb + (2 * t + 1) * 640);
    asm volatile("" ::: "memory");
    SB();
  };
  constexpr int KV_PD = 3;
  kf[0] = bld<SC1>(rs, ko, kb);
  SB();
#pragma unroll
  for (int t = 0; t < KV_PD; ++t) load_kv(t);
  ((unsigned*)(lds + AL_FL))[lane] = h;
  const unsigned wos = OFF_WO + w * 64 * KB;
  f32x4 oacc[4] = {};
  f32x16 o;
  char* const ro_w = lds + AL_RO + w * 2048 + lane * 8;
  const int lf_w0 = h == 0 ? AL_RL + (r * 8 + w) * 4 : AL_FL + (w * 4 + (r & 3)) * 4, lf_step = h == 0 ? 1024 : 0;
  const int o_wr = AL_O + (r >> 4) * 1024 + ((2 * ((w & 3) >> 1) + h) * 16 + (w >> 2) * 8 + (r & 7)) * 16 + ((r >> 3) & 1) * 8 + (w & 1) * 4;
  auto publish = [&](int g) {
    char* ro = ro_w + (g & 1) * 16384;
    *(f32x2*)(ro) = (f32x2){o[0], o[1]}; *(f32x2*)(ro + 512) = (f32x2){o[2], o[3]};
    *(f32x2*)(ro + 1024) = (f32x2){o[4], o[5]}; *(f32x2*)(ro + 1536) = (f32x2){o[6], o[7]};
    asm volatile("" ::: "memory");
    *(volatile LAS unsigned*)((LAS char*)lds + lf_w0 + (g & 1) * lf_step) = h == 0 ? __float_as_uint(o[8]) : (unsigned)(g + 1);
  };
  f32x2 ra[4]; f32x4 rl0, rl1;
  const char* const red_r = lds + AL_RO + (w >> 2) * 8192 + (w & 3) * 512 + lane * 8;
  auto reduce_ld = [&](int g) {
#pragma unroll
    for (int i = 0; i < 4; ++i) ra[i] = *(const f32x2*)(red_r + (g & 1) * 16384 + i * 2048);
    rl0 = *(const f32x4*)(lds + AL_RL + (g & 1) * 1024 + r * 32); rl1 = *(const f32x4*)(lds + AL_RL + (g & 1) * 1024 + r * 32 + 16);
  };
  auto reduce_fin = [&](int g) {
    const f32x4 l4 = rl0 + rl1;
    const f32x2 l2 = (f32x2){l4[0], l4[1]} + (f32x2){l4[2], l4[3]};
    const float inv = __builtin_amdgcn_rcpf(l2[0] + l2[1]);
    const f32x2 sa = (ra[0] + ra[1]) + (ra[2] + ra[3]);
    *(unsigned*)(lds + o_wr + (g & 1) * 2048) = pk2(sa[0] * inv, sa[1] * inv);
    asm volatile("" ::: "memory");
    *(volatile LAS unsigned*)((LAS char*)lds + AL_FL + 128 + w * 16 + (lane & 3) * 4) = (unsigned)(g + 2);
  };
  auto reduce = [&](int g) { reduce_ld(g); reduce_fin(g); };
  const int g4 = lane >> 4, mm = lane & 15;
  const unsigned fs = ((t0 >> 4) * 16 + 2 * w) * KB, fv = (mm >> 3) * 1024 + (g4 * 16 + (t0 & 15) + (mm & 7)) * 16;
  u32x4 xr;
  auto load_res = [&]() {
    if (layer == 0) {
      const u16* xp = xin + (size_t)(t0 + (mm & 7)) * D_ + 64 * w + 32 * (mm >> 3) + 4 * g4;
      const u32x2 lo = *(const u32x2*)xp, hi = *(const u32x2*)(xp + 16);
      xr = (u32x4){lo[0], lo[1], hi[0], hi[1]};
    } else xr = bld4<SC1>(rs, fv, OFF_X2 + fs);
  };
  f32x16 cinit;
  { float ns = -SHIFT; PIN1(ns);
#pragma unroll
    for (int i = 0; i < 16; ++i) cinit[i] = ns; }
  PIN1(cinit);
  bf16x8 wof[4], of;
  { unsigned z0 = 0u; PIN1(z0); const u32x4 z = {z0, z0, z0, z0}; wof[0] = wof[1] = wof[2] = wof[3] = __builtin_bit_cast(bf16x8, z); }
  auto load_c1 = [&]() { c1p = bld1<SC1>(rs, (lane >> 4) * 7168 + (lane & 15) * 4, OFF_CS1 + (f1_nt0 + (w < 7 ? w : 6)) * 64); };
  auto store_c1 = [&]() { const float c1 = xrow_sum(__uint_as_float(c1p)); if (lane < 16) ((float*)(lds + AL_CS))[w * 16 + lane] = c1; };
  u32x4 p0, p1;
  unsigned flg;
  const unsigned fl_addr = lds0 + AL_FL + lane * 4;
  f32x16 sc = mfma32(kf[0], qf, cinit);
  const unsigned w1s = OFF_W1 + f1_nt0 * 16 * KB + smul(w, 14336), w1d = lds0 + AL_W1 + smul(w, 14336);
  bf16x8 wl[16];
  auto group = [&](auto mode_tag, const int g) {
    constexpr bool FIRST = decltype(mode_tag)::value == 0, SECOND = decltype(mode_tag)::value == 1, LAST = decltype(mode_tag)::value == 3;
    constexpr bool WGT = !FIRST && !SECOND;
#pragma unroll
    for (int t = 0; t < 8; ++t) {
      if ((t & 3) == 3) __builtin_amdgcn_s_setprio(3); if ((t & 3) == 0) __builtin_amdgcn_s_setprio(2); if ((t & 3) == 1) __builtin_amdgcn_s_setprio(1); if ((t & 3) == 2) __builtin_amdgcn_s_setprio(0);
      f32x16 sn;
      if (!(LAST && t == 7)) sn = mfma32(kf[(t + 1) & 7], t < 7 ? qf : qn, cinit);
      SB();
      if (FIRST) {
        if (t + KV_PD < 8) load_kv(t + KV_PD);
        if (t == 8 - KV_PD) {
          qw = bld<SC1>(rs, l16, qs + w * 1024);
          kn_a = bld1<SC1>(rs, (lane & 15) * 128, qkv + QO_KN2 + w * 2048);
          qn_a = bld1<SC1>(rs, (lane & 15) * 128, qkv + QO_QN2 + (t0 >> 4) * 2048);
          asm volatile("" ::: "memory");
          SB();
        }
      }
      float e0 = EX(sc[0]), e1 = EX(sc[1]), e2 = EX(sc[2]), e3 = EX(sc[3]), e4 = EX(sc[4]), e5 = EX(sc[5]);
      PIN4(e0, e1, e2, e3); PIN2(e4, e5); SB();
      if (t > 0 || !FIRST) o = mfma32(vf[(2 * t + 15) & 15], __builtin_bit_cast(bf16x8, p1), o);
      SB();
      float e6 = EX(sc[6]), e7 = EX(sc[7]);
      PIN2(e6, e7); SB();
      p0[0] = pk2(e0, e1); p0[1] = pk2(e2, e3); p0[2] = pk2(e4, e5); p0[3] = pk2(e6, e7);
      PIN1(p0); SB();
      float e8 = EX(sc[8]), e9 = EX(sc[9]);
      PIN2(e8, e9); SB();
      float e10, e11, e12, e13;
      if (t == 0) {
        if (!FIRST) { e10 = EX(sc[10]); e11 = EX(sc[11]); e12 = EX(sc[12]); e13 = EX(sc[13]); PIN4(e10, e11, e12, e13); PIN1(o); SB(); publish(g - 1); SB(); }
        f32x16 z = {}; o = mfma32(vf[0], __builtin_bit_cast(bf16x8, p0), z);
      } else o = mfma32(vf[2 * t], __builtin_bit_cast(bf16x8, p0), o);
      SB();
      if (t > 0 || FIRST) { e10 = EX(sc[10]); e11 = EX(sc[11]); e12 = EX(sc[12]); e13 = EX(sc[13]); PIN4(e10, e11, e12, e13); SB(); }
      if (WGT) {
        constexpr int X = LAST ? 2 : 0, D = LAST ? 0 : 1;
        if (t == 0) mfma16_w<3>(oacc[0], wof[0], of); else if (t == 1) mfma16_w<3 + X>(oacc[1], wof[1], of);
        else if (t == 2) mfma16_w<3 + D + 2 * X>(oacc[2], wof[2], of); else if (t == 3) mfma16_w<3 + 2 * D + 3 * X>(oacc[3], wof[3], of);
        else if (t == 4) mfma16_w<3 + 3 * D + 4 * X>(oacc[0], wof[0], of); else if (t == 5) mfma16_w<3 + 3 * D + 4 * X>(oacc[1], wof[1], of);
        else if (t == 6) mfma16_w<3 + 2 * D + 4 * X>(oacc[2], wof[2], of); else mfma16_w<3 + D + 4 * X>(oacc[3], wof[3], of);
        SB();
      }
      float e14 = EX(sc[14]), e15 = EX(sc[15]);
      PIN2(e14, e15); SB();
      p1[0] = pk2(e8, e9); p1[1] = pk2(e10, e11); p1[2] = pk2(e12, e13); p1[3] = pk2(e14, e15);
      PIN1(p1); SB();
      if (WGT) {
        const unsigned so = wos + (2 * (g - 2) + (t >> 2)) * 4096;
        if ((t & 3) == 0) bld_asm<0>(wof[0], rs, l16, so); else if ((t & 3) == 1) bld_asm<1024>(wof[1], rs, l16, so);
        else if ((t & 3) == 2) bld_asm<2048>(wof[2], rs, l16, so); else bld_asm<3072>(wof[3], rs, l16, so);
        SB();
      }
      if (LAST) {
#pragma unroll
        for (int i = 2 * t; i < 2 * t + 2; ++i) { wl[i] = bld<SC1>(rs, l16 + (i & 3) * 1024, wos + (12 + (i >> 2)) * 4096); asm volatile("" ::: "memory"); }
        SB();
      }
      if (WGT && !LAST && t >= 1 && t <= 3) {
        const int pc = min(3 * (g - 2) + (t - 1), 13);
        dma(rs, l16, w1s + pc * KB, w1d + pc * KB);
        SB();
      }
      if (SECOND && t == 5) { wfl = flag_ld(ws, flag_off(1, 2, w * 8), (r >> 2) * 128 + (lane & 3) * 8); SB(); }
      if (!FIRST && t == 1) { flg = *(volatile LAS unsigned*)((LAS char*)lds + AL_FL + lane * 4); SB(); }
      if (!FIRST && t == 2) {
        asm volatile("1:\n\tv_cmp_gt_u32 vcc, %2, %0\n\ts_cbranch_vccz 2f\n\ts_sleep 1\n\tds_read_b32 %0, %1\n\ts_waitcnt lgkmcnt(0)\n\ts_branch 1b\n2:"
                     : "+v"(flg) : "v"(fl_addr), "s"(g) : "vcc", "memory");
        reduce_ld(g - 1);
        SB();
      }
      if (!FIRST && t == 3) {
        reduce_fin(g - 1);
        SB();
      }
      if (WGT && (t & 3) == 3) { of = lds_frag(lds, AL_O + (g & 1) * 2048 + (t >> 2) * 1024 + l16); SB(); }
      if (!FIRST && !LAST && t == 5) { qn = lds_frag(lds, AL_Q + (g + 1) * 1024 + l16); SB(); }
      if (!(LAST && t == 7)) sc = sn;
    }
    qf = qn;
  };
  group(std::integral_constant<int, 0>{}, 0);
  *(LAS bf16x8*)((LAS char*)lds + AL_Q + w * 1024 + l16) = qw;
  ((float*)(lds + AL_BND))[w * 16 + (lane & 15)] = __uint_as_float(kn_a) * __uint_as_float(row16_umax(qn_a));
  wg_signal(rs, flag_off(1, 2, (xcd * 4 + (idx >> 3)) * 2 + ((idx >> 2) & 1)) + (idx & 3) * 8, tid, tag);
  auto weights_ready = [&]() { wave_wait(ws, flag_off(1, 2, w * 8), (r >> 2) * 128 + (lane & 3) * 8, tag); raw_barrier(); };
  const f32x2 kq2 = *(const f32x2*)(lds + AL_BND + lane * 8);
  const bool slow = !__all(kq2[0] <= SHIFT * SHIFT * 0.998f && kq2[1] <= SHIFT * SHIFT * 0.998f);
  of = wof[0];
  if (!slow) {
    group(std::integral_constant<int, 1>{}, 1);
    if (layer == 0) {
      if (!__all(wfl == tag)) wave_wait(ws, flag_off(1, 2, w * 8), (r >> 2) * 128 + (lane & 3) * 8, tag);
      raw_barrier();
    }
    PIN4(wof[0], wof[1], wof[2], wof[3]); PIN4(oacc[0], oacc[1], oacc[2], oacc[3]); PIN1(of); SB();
#pragma unroll
    for (int g = 2; g < 7; ++g) group(std::integral_constant<int, 2>{}, g);
    group(std::integral_constant<int, 3>{}, 7);
    __builtin_amdgcn_s_setprio(0);
    o = mfma32(vf[15], __builtin_bit_cast(bf16x8, p1), o);
    SB();
    load_res(); load_c1();
    SB();
    publish(7);
    raw_barrier();
    reduce_ld(7);
    bf16x8 of0 = lds_frag(lds, AL_O + l16), of1 = lds_frag(lds, AL_O + 1024 + l16);
    SB();
#pragma unroll
    for (int t = 0; t < 4; ++t) mfma16_w<4>(oacc[t], wof[t], of);
    SB();
    reduce_fin(7);
    SB();
#pragma unroll
    for (int t = 0; t < 4; ++t) mfma16_ip(oacc[t], wl[t], of0);
#pragma unroll
    for (int t = 0; t < 4; ++t) mfma16_ip(oacc[t], wl[4 + t], of1);
    store_c1();
    raw_barrier();
    of0 = lds_frag(lds, AL_O + 2048 + l16); of1 = lds_frag(lds, AL_O + 3072 + l16);
#pragma unroll
    for (int t = 0; t < 4; ++t) mfma16_ip(oacc[t], wl[8 + t], of0);
#pragma unroll
    for (int t = 0; t < 4; ++t) mfma16_ip(oacc[t], wl[12 + t], of1);
    asm volatile("s_nop 7" : "+v"(oacc[0]), "+v"(oacc[1]), "+v"(oacc[2]), "+v"(oacc[3]));
  } else {
    __builtin_amdgcn_s_setprio(0);
    weights_ready();
    float* mx = (float*)(lds + AL_MX);
    const f32x16 z = {};
#pragma unroll 1
    for (int g = 0; g < 8; ++g) {
      const bf16x8 q = lds_frag(lds, AL_Q + g * 1024 + l16);
      float m = -INFINITY;
#pragma unroll
      for (int t = 0; t < 8; ++t) {
        const f32x16 s = mfma32(kf[t], q, z);
#pragma unroll
        for (int i = 0; i < 16; ++i) m = fmaxf(m, s[i]);
        SB();
      }
      { auto sw = __builtin_amdgcn_permlane32_swap(__float_as_uint(m), __float_as_uint(m), false, false); m = fmaxf(__uint_as_float(sw[0]), __uint_as_float(sw[1])); }
      if (h == 0) mx[w * 256 + g * 32 + r] = m;
    }
    __syncthreads();
#pragma unroll 1
    for (int g = 0; g < 8; ++g) {
      const bf16x8 q = lds_frag(lds, AL_Q + g * 1024 + l16);
      float m = mx[g * 32 + r];
#pragma unroll
      for (int i = 1; i < 8; ++i) m = fmaxf(m, mx[i * 256 + g * 32 + r]);
      o = z;
#pragma unroll
      for (int t = 0; t < 8; ++t) {
        const f32x16 s = mfma32(kf[t], q, z);
        u32x4 a0, a1;
#pragma unroll
        for (int i = 0; i < 4; ++i) {
          a0[i] = pk2(EX(s[2 * i] - m), EX(s[2 * i + 1] - m));
          a1[i] = pk2(EX(s[8 + 2 * i] - m), EX(s[9 + 2 * i] - m));
        }
        o = mfma32(vf[2 * t], __builtin_bit_cast(bf16x8, a0), o);
        o = mfma32(vf[2 * t + 1], __builtin_bit_cast(bf16x8, a1), o);
        SB();
      }
      publish(g);
      __syncthreads();
      reduce(g);
      __syncthreads();
      const bf16x8 b0 = lds_frag(lds, AL_O + (g & 1) * 2048 + l16), b1 = lds_frag(lds, AL_O + (g & 1) * 2048 + 1024 + l16);
#pragma unroll
      for (int t = 0; t < 4; ++t) {
        oacc[t] = mfma16(bld<SC1>(rs, l16, wos + (2 * g * 4 + t) * 1024), b0, oacc[t]);
        oacc[t] = mfma16(bld<SC1>(rs, l16, wos + ((2 * g + 1) * 4 + t) * 1024), b1, oacc[t]);
      }
    }
    load_res(); load_c1(); store_c1();
#pragma unroll 1
    for (int i = 0; i < 14; ++i) dma(rs, l16, w1s + i * KB, w1d + i * KB);
    asm volatile("s_nop 7" : "+v"(oacc[0]), "+v"(oacc[1]), "+v"(oacc[2]), "+v"(oacc[3]));
  }
  float xv[8];
  asm volatile("s_nop 1\n\t"
               "v_add_f32_dpp %0, %8, %8 row_ror:8 row_mask:0xf bank_mask:0x3\n\t"
               "v_add_f32_dpp %1, %9, %9 row_ror:8 row_mask:0xf bank_mask:0x3\n\t"
               "v_add_f32_dpp %2, %10, %10 row_ror:8 row_mask:0xf bank_mask:0x3\n\t"
               "v_add_f32_dpp %3, %11, %11 row_ror:8 row_mask:0xf bank_mask:0x3\n\t"
               "v_add_f32_dpp %4, %12, %12 row_ror:8 row_mask:0xf bank_mask:0x3\n\t"
               "v_add_f32_dpp %5, %13, %13 row_ror:8 row_mask:0xf bank_mask:0x3\n\t"
               "v_add_f32_dpp %6, %14, %14 row_ror:8 row_mask:0xf bank_mask:0x3\n\t"
               "v_add_f32_dpp %7, %15, %15 row_ror:8 row_mask:0xf bank_mask:0x3\n\t"
               "v_add_f32_dpp %0, %16, %16 row_ror:8 row_mask:0xf bank_mask:0xc\n\t"
               "v_add_f32_dpp %1, %17, %17 row_ror:8 row_mask:0xf bank_mask:0xc\n\t"
               "v_add_f32_dpp %2, %18, %18 row_ror:8 row_mask:0xf bank_mask:0xc\n\t"
               "v_add_f32_dpp %3, %19, %19 row_ror:8 row_mask:0xf bank_mask:0xc\n\t"
               "v_add_f32_dpp %4, %20, %20 row_ror:8 row_mask:0xf bank_mask:0xc\n\t"
               "v_add_f32_dpp %5, %21, %21 row_ror:8 row_mask:0xf bank_mask:0xc\n\t"
               "v_add_f32_dpp %6, %22, %22 row_ror:8 row_mask:0xf bank_mask:0xc\n\t"
               "v_add_f32_dpp %7, %23, %23 row_ror:8 row_mask:0xf bank_mask:0xc\n\t"
               : "=&v"(xv[0]), "=&v"(xv[1]), "=&v"(xv[2]), "=&v"(xv[3]), "=&v"(xv[4]), "=&v"(xv[5]), "=&v"(xv[6]), "=&v"(xv[7])
               : "v"(oacc[0][0]), "v"(oacc[0][1]), "v"(oacc[0][2]), "v"(oacc[0][3]), "v"(oacc[1][0]), "v"(oacc[1][1]), "v"(oacc[1][2]), "v"(oacc[1][3]), "v"(oacc[2][0]), "v"(oacc[2][1]), "v"(oacc[2][2]), "v"(oacc[2][3]), "v"(oacc[3][0]), "v"(oacc[3][1]), "v"(oacc[3][2]), "v"(oacc[3][3]));
  u32x4 ov;
  float sm = 0.f, sq = 0.f;
#pragma unroll
  for (int q = 0; q < 4; ++q) {
    const float a = xv[2 * q] + bflo(xr[q]), b = xv[2 * q + 1] + bfhi(xr[q]);
    ov[q] = pk2(a, b);
    sm += a + b; sq = fmaf(a, a, fmaf(b, b, sq));
  }
  bst4<SC1>(ov, rs, fv, qkv + QO_X1 + fs);
  sm += dpp<0x128>(sm); sq += dpp<0x128>(sq);
  xrow_sum2(sm, sq);
  bst2<SC1>((u32x2){__float_as_uint(sm), __float_as_uint(sq)}, rs, lane < 8 ? lane * 8 : OOB, qkv + QO_ST + t0 * 128 + w * 128);
  wg_signal(rs, flag_off(layer, 0, t0 >> 4) + (t0 & 8), tid, tag);
}

DI void ffn1_phase(char* lds, char* ws, rsrc_t rs, int layer, int tid, bf16x8 (&stg)[28], u64 tag) {
  const int lane = tid & 63, g = lane >> 4, mm = lane & 15;
  const int w = __builtin_amdgcn_readfirstlane(tid >> 6);
  const int bid = mybid(), xcd = bid & 7, idx = bid >> 3, rb = idx >> 4, cb = idx & 15;
  const int mt = xcd * 16 + rb * 8 + w, nt0 = smul(cb, 7);
  const unsigned l16 = lane * 16;
#pragma unroll
  for (int i = 0; i < PRE_EARLY; ++i) ffn2_prefetch(rs, l16, (idx & 7) * 4, w, stg, i);
  bf16x8 a[2][7];
#pragma unroll
  for (int t = 0; t < 7; ++t) a[0][t] = lds_frag(lds, AL_W1 + t * 1024 + l16);
  wave_wait(ws, flag_off(layer, 0, mt), (lane & 1) * 8, tag);
  constexpr int PD = 5;
  bf16x8 b[16];
  const unsigned lb = OFF_QKV + smul(layer, QKV_BYTES), bs = lb + QO_X1 + mt * 16 * KB;
#pragma unroll
  for (int ks = 0; ks < PD; ++ks) b[ks] = bld<SC1>(rs, l16, bs + ks * KB);
  const u32x2 st0 = __builtin_amdgcn_raw_buffer_load_b64(rs, (mm >> 3) * 1024 + g * 128 + (mm & 7) * 8, lb + QO_ST + mt * 2048, SC1);
  const u32x2 st1 = __builtin_amdgcn_raw_buffer_load_b64(rs, (mm >> 3) * 1024 + g * 128 + (mm & 7) * 8 + 512, lb + QO_ST + mt * 2048, SC1);
  const f32x4 stp = __builtin_bit_cast(f32x4, (u32x4){st0[0], st0[1], st1[0], st1[1]});
  SB();
  f32x4 acc[7] = {};
  float rstd, nmr;
#pragma unroll
  for (int ks = 0; ks < 16; ++ks) {
    if (ks < 15) {
#pragma unroll
      for (int t = 0; t < 7; ++t) a[(ks + 1) & 1][t] = lds_frag(lds, AL_W1 + ((ks + 1) * 7 + t) * 1024 + l16);
    }
    if (ks + PD < 16) b[ks + PD] = bld<SC1>(rs, l16, bs + (ks + PD) * KB);
    {
      constexpr int NMID = 28 - PRE_EARLY - PRE_LATE;
      const int lo = ks <= 10 ? (ks * 6) / 11 : 6 + (ks - 11) * 2, hi = ks + 1 <= 10 ? ((ks + 1) * 6) / 11 : 6 + (ks + 1 - 11) * 2;
#pragma unroll
      for (int i = lo; i < hi; ++i) if (i < NMID) ffn2_prefetch(rs, l16, (idx & 7) * 4, w, stg, PRE_EARLY + i);
    }
    if (ks == 8) {
      float sm = stp[0] + stp[2], sq = stp[1] + stp[3];
      xrow_sum2(sm, sq);
      const float mean = sm * (1.f / D_);
      rstd = __builtin_amdgcn_rsqf(fmaxf(sq * (1.f / D_) - mean * mean, 0.f) + LN_EPS); nmr = -mean * rstd;
      PIN2(rstd, nmr);
    }
    SB();
#pragma unroll
    for (int t = 0; t < 7; ++t) acc[t] = mfma16(a[ks & 1][t], b[ks], acc[t]);
    SB();
  }
  u32x2 hv[7];
#pragma unroll
  for (int t = 0; t < 7; ++t) {
    const f32x4 v = acc[t] * rstd + *(const f32x4*)(lds + AL_CS + t * 64 + g * 16) * nmr;
    hv[t] = (u32x2){relu_pk(pk2(v[0], v[1])), relu_pk(pk2(v[2], v[3]))};
  }
  const unsigned hs = lb + QO_H + smul(mt, 56 * KB) + (nt0 >> 1) * KB;
  if ((cb & 1) == 0) {
#pragma unroll
    for (int p = 0; p < 3; ++p) bst4<SC1>((u32x4){hv[2 * p][0], hv[2 * p][1], hv[2 * p + 1][0], hv[2 * p + 1][1]}, rs, l16, hs + p * KB);
    bst2<SC1>(hv[6], rs, lane * 8, hs + 3 * KB);
  } else {
    bst2<SC1>(hv[0], rs, lane * 8 + 512, hs);
#pragma unroll
    for (int p = 0; p < 3; ++p) bst4<SC1>((u32x4){hv[2 * p + 1][0], hv[2 * p + 1][1], hv[2 * p + 2][0], hv[2 * p + 2][1]}, rs, l16, hs + (p + 1) * KB);
  }
  wg_signal(rs, flag_off(layer, 1, (xcd * 2 + rb) * 8 + (cb >> 1)) + (cb & 1) * 8, tid, tag);
}

DI void ffn2_phase(char* lds, char* ws, rsrc_t rs, u16* out, int layer, int tid, bf16x8 (&stg)[28], u64 tag) {
  const int lane = tid & 63, g = lane >> 4, mm = lane & 15;
  const int w = __builtin_amdgcn_readfirstlane(tid >> 6);
  const int bid = mybid(), xcd = bid & 7, idx = bid >> 3, rb = idx >> 3, cb = idx & 7;
  const int fm = w >> 1, fp = w & 1, mt0 = xcd * 16 + rb * 4, nt0 = cb * 4;
  const unsigned lds0 = (unsigned)(uintptr_t)lds, l16 = lane * 16;
#pragma unroll
  for (int i = 28 - PRE_LATE; i < 28; ++i) ffn2_prefetch(rs, l16, nt0, w, stg, i);
  const unsigned fs = ((mt0 + fm) * 16 + (nt0 >> 1) + fp) * KB;
  const unsigned lb = OFF_QKV + smul(layer, QKV_BYTES);
  const u32x4 xr = bld4<SC1>(rs, l16, lb + QO_X1 + fs);
  if (layer == 0) {
#pragma unroll
    for (int i = 0; i < 6; ++i) { const int c = 6 * w + i; dma(rs, l16, OFF_WQKV + ((4 * cb + (c & 3)) * 16 + (c >> 2)) * KB, lds0 + c * 1024); }
  }
  wave_wait(ws, flag_off(layer, 1, (xcd * 2 + (rb >> 1)) * 8 + w), (lane & 1) * 8, tag);
  constexpr int PD = 6;
  bf16x8 b[28];
  auto ldb = [&](int i) {
    const unsigned so = lb + QO_H + smul(mt0 + ((fm + 1 + i / 7) & 3), 56 * KB) + smul(w, 7 * KB) + (i % 7) * KB;
    if (i % 7 == 3) {
      const u32x2 lo = __builtin_amdgcn_raw_buffer_load_b64(rs, lane * 8, so, SC1), hi = __builtin_amdgcn_raw_buffer_load_b64(rs, lane * 8 + 512, so, SC1);
      b[i] = __builtin_bit_cast(bf16x8, (u32x4){lo[0], lo[1], hi[0], hi[1]});
    } else b[i] = bld<SC1>(rs, l16, so);
  };
#pragma unroll
  for (int i = 0; i < PD; ++i) ldb(i);
  SB();
  f32x4 acc[2][4];
  auto send = [&](int mi, int pr) {
    const int d = 2 * ((fm + 1 + mi) & 3) + (pr ^ fp);
    char* xb = lds + AL_W1 + (d * 7 + ((w - d - 1) & 7)) * 2048 + l16;
    *(f32x4*)(xb) = acc[mi & 1][2 * pr]; *(f32x4*)(xb + 1024) = acc[mi & 1][2 * pr + 1];
  };
#pragma unroll
  for (int i = 0; i < 28; ++i) {
    const int mi = i / 7, j = i % 7;
    if (i + PD < 28) ldb(i + PD);
    if (mi > 0 && j == 3) send(mi - 1, 0);
    if (mi > 0 && j == 5) send(mi - 1, 1);
    SB();
#pragma unroll
    for (int t = 0; t < 4; ++t) {
      if (j == 0) { const f32x4 z = {}; acc[mi & 1][t] = mfma16(stg[t], b[i], z); }
      else acc[mi & 1][t] = mfma16(stg[4 * j + t], b[i], acc[mi & 1][t]);
    }
    SB();
  }
  send(3, 1);
  f32x4 s0 = acc[1][0] + (f32x4){bflo(xr[0]), bfhi(xr[0]), bflo(xr[1]), bfhi(xr[1])};
  f32x4 s1 = acc[1][1] + (f32x4){bflo(xr[2]), bfhi(xr[2]), bflo(xr[3]), bfhi(xr[3])};
  raw_barrier();
#pragma unroll
  for (int d = 0; d < 7; ++d) {
    const char* xb = lds + AL_W1 + (w * 7 + d) * 2048 + l16;
    s0 += *(const f32x4*)(xb); s1 += *(const f32x4*)(xb + 1024);
  }
  const u32x4 o = {pk2(s0[0], s0[1]), pk2(s0[2], s0[3]), pk2(s1[0], s1[1]), pk2(s1[2], s1[3])};
  if (layer == 1) {
    u16* op = out + (size_t)((mt0 + fm) * 16 + mm) * D_ + (nt0 + 2 * fp) * 16 + 4 * g;
    *(u32x2*)op = (u32x2){o[0], o[1]};
    *(u32x2*)(op + 16) = (u32x2){o[2], o[3]};
    asm volatile("" : "+s"(tag));
    bst2<SC1>((u32x2){(unsigned)tag, (unsigned)(tag >> 32)}, rs, (bid | tid) == 0 ? 0u : OOB, OFF_EPOCH);
  } else {
    bst4<SC1>(o, rs, l16, OFF_X2 + fs);
    wg_signal(rs, flag_off(0, 2, (xcd * 4 + rb) * 2 + (cb >> 2)) + (cb & 3) * 8, tid, tag);
  }
}

typedef short v4i16_t __attribute__((ext_vector_type(4)));
DI bf16x8 tr_frag(const char* lds, int off0, int off1) {
  const v4i16_t lo = __builtin_amdgcn_ds_read_tr16_b64_v4i16((LAS v4i16_t*)((LAS char*)lds + off0));
  const v4i16_t hi = __builtin_amdgcn_ds_read_tr16_b64_v4i16((LAS v4i16_t*)((LAS char*)lds + off1));
  return __builtin_shufflevector(lo, hi, 0, 1, 2, 3, 4, 5, 6, 7);
}
constexpr int QL_KV = 65536, QL_P = 81920, QL_TR = QL_P + 65536;
constexpr int QP_T = 0, QP_KV = 2048, QP_CS = 3072, QP_CK = 3136, QP_ST = 3200;
template <bool FIRST, class H1, class TG>
DI void qkv_phase(char* lds, char* ws, rsrc_t rs, int tid, const u16* x, const u16* wq, const u16* wkv, H1 hook1, TG gettag) {
  const int lane = tid & 63, g = lane >> 4, mm = lane & 15;
  const int w = __builtin_amdgcn_readfirstlane(tid >> 6);
  const int bid = mybid(), xcd = bid & 7, idx = bid >> 3, rb = idx >> 3, cb = idx & 7;
  const int ml = w & 3, hf = w >> 2, mt = xcd * 16 + rb * 4 + ml;
  const int kvml = cb >> 1;
  const unsigned lds0 = (unsigned)(uintptr_t)lds, l16 = lane * 16;
  const unsigned qkv = OFF_QKV + (FIRST ? 0 : QKV_BYTES);
  char* const mine = lds + QL_P + w * 8192;
  const char* const other = lds + QL_P + (w ^ 4) * 8192;
  bf16x8 b[8];
  f32x4 cs0, cs1, csk; float csv;
  if (FIRST) {
    const rsrc_t rw = __builtin_amdgcn_make_buffer_rsrc((void*)wq, 0, D_ * D_ * 2, 0x00020000);
    const rsrc_t rk = __builtin_amdgcn_make_buffer_rsrc((void*)wkv, 0, D_ * 16 * 2, 0x00020000);
    const rsrc_t rx = __builtin_amdgcn_make_buffer_rsrc((void*)x, 0, S_ * D_ * 2, 0x00020000);
#pragma unroll
    for (int i = 0; i < 2; ++i) dma(rk, l16, (2 * w + i) * 1024, lds0 + QL_KV + (2 * w + i) * 1024);
    const unsigned vx = (lane >> 5) * 1024 + (((lane & 31) ^ (lane >> 5)) << 4);
#pragma unroll
    for (int i = 0; i < 8; ++i) dma(rx, vx ^ (i << 5), mt * 16384 + i * 2048 + hf * 512, lds0 + QL_P + w * 8192 + i * 1024);
    const unsigned v0 = (lane >> 3) * 1024 + (((lane & 7) ^ ((lane >> 3) & 2)) << 4), v1 = v0 ^ 64;
#pragma unroll
    for (int i = 0; i < 8; ++i) dma(rw, (i & 1) ? v1 : v0, (8 * w + i) * 8192 + cb * 128, lds0 + (8 * w + i) * 1024);
    wait_vm<0>();
#pragma unroll
    for (int j = 0; j < 8; ++j) b[j] = lds_frag(mine, mm * 512 + (((4 * j + g) ^ mm) << 4));
  } else {
#pragma unroll
    for (int i = 0; i < 2; ++i) {
      const int c = 48 + 2 * w + i;
      dma(rs, l16, OFF_WQKV + ((4 * cb + (c & 3)) * 16 + (c >> 2)) * KB, lds0 + c * 1024);
      dma(rs, l16, OFF_WQKV + ((32 + (cb & 1)) * 16 + 2 * w + i) * KB, lds0 + QL_KV + (2 * w + i) * 1024);
    }
    cs0 = bld4f<SC1>(rs, g * 16, OFF_CSUM + (4 * cb + 2 * hf) * 64); cs1 = bld4f<SC1>(rs, g * 16 + 64, OFF_CSUM + (4 * cb + 2 * hf) * 64);
    csk = bld4f<SC1>(rs, g * 16, OFF_CSUM + 32 * 64);
    csv = __uint_as_float(bld1<SC1>(rs, mm * 4, OFF_CSUM + 33 * 64));
    wave_wait(ws, flag_off(0, 2, (xcd * 4 + rb) * 2 + hf), (lane & 3) * 8, gettag());
#pragma unroll
    for (int j = 0; j < 8; ++j) b[j] = bld<SC1>(rs, l16, OFF_X2 + (mt * 16 + 8 * hf + j) * KB);
  }
  raw_barrier();
  hook1();
  const int q4 = (lane >> 2) & 3, p4 = lane & 3;
  const int fsw = ((q4 >> 1) & 1) | ((g & 1) << 1);
  const int abase = FIRST ? hf * 32768 + (8 * g + q4) * 128 + p4 * 8 : hf * 32768 + l16;
  auto aq = [&](int t  ) { return FIRST ? abase + ((t ^ fsw) << 5) : abase + t * 1024; };
  auto ldq = [&](int base, int j) { return FIRST ? tr_frag(lds, base + j * 4096, base + j * 4096 + 512) : lds_frag(lds, base + j * 4096); };
  const int kvb = QL_KV + hf * 8192 + (FIRST ? (8 * g + q4) * 32 + p4 * 8 : l16);
  auto ldkv = [&](int j) { return FIRST ? tr_frag(lds, kvb + j * 1024, kvb + j * 1024 + 128) : lds_frag(lds, kvb + j * 1024); };
  bf16x8 ones; for (int i = 0; i < 8; ++i) ones[i] = 0x3F80;
  if (FIRST) {
    const int cbs = aq(ml);
    f32x4 c0 = {}, c1 = {}, ck = {};
#pragma unroll
    for (int j = 0; j < 8; j += 2) { c0 = mfma16(ldq(cbs, j), ones, c0); c1 = mfma16(ldq(cbs, j + 1), ones, c1); }
    const int kv2 = kvb + ml * 2048;
#pragma unroll
    for (int j = 0; j < 2; ++j) ck = mfma16(FIRST ? tr_frag(lds, kv2 + j * 1024, kv2 + j * 1024 + 128) : lds_frag(lds, kv2 + j * 1024), ones, ck);
    if (mm == 0) { *(f32x4*)(mine + QP_CS + g * 16) = c0 + c1; *(f32x4*)(mine + QP_CK + g * 16) = ck; }
  }
  f32x4 acc[4] = {}, accs = {}, accg = {}, akv = {};
  bf16x8 a[2][4];
  const int ab0 = aq(2 * hf), ab1 = aq(2 * hf + 1), ab2 = aq(2 - 2 * hf), ab3 = aq(3 - 2 * hf);
  auto lda = [&](int j) { a[j & 1][0] = ldq(ab0, j); a[j & 1][1] = ldq(ab1, j); a[j & 1][2] = ldq(ab2, j); a[j & 1][3] = ldq(ab3, j); };
#pragma unroll
  for (int j = 0; j < 8; ++j) { accs = mfma16(ones, b[j], accs); accg = mfma16(b[j], b[j], accg); }
  if (ml == kvml) {
    if (cb & 1) {
#pragma unroll
      for (int j = 0; j < 8; ++j) akv = mfma16(b[j], ldkv(j), akv);
    } else {
#pragma unroll
      for (int j = 0; j < 8; ++j) akv = mfma16(ldkv(j), b[j], akv);
    }
    *(f32x4*)(mine + QP_KV + l16) = akv;
  }
  {
    const float s01 = (mm & 1) ? accg[1] : accg[0], s23 = (mm & 1) ? accg[3] : accg[2];
    if (g == (mm >> 2)) *(f32x2*)(mine + QP_ST + mm * 8) = (f32x2){accs[0], (mm & 2) ? s23 : s01};
  }
  lda(0);
  raw_barrier();
  float invd = 1.f / D_; PIN1(invd);
  auto stats = [&](int tokl, float& mean, float& rstd) {
    const f32x2 s = *(const f32x2*)(mine + QP_ST + tokl * 8) + *(const f32x2*)(other + QP_ST + tokl * 8);
    mean = s[0] * invd;
    rstd = __builtin_amdgcn_rsqf(fmaxf(s[1] * invd - mean * mean, 0.f) + LN_EPS);
  };
  float mean, rstd; stats(mm, mean, rstd);
  const bool kvw = hf == 1 && ml == kvml;
  if (kvw) {
    const f32x4 skv = akv + *(const f32x4*)(other + QP_KV + l16);
    if (cb & 1) {
      if (FIRST) {
        csv = 0.f;
#pragma unroll
        for (int i = 0; i < 8; ++i) csv += *(const float*)(lds + QL_P + i * 8192 + QP_CK + mm * 4);
        if (mt == 0) bst1<SC1>(__float_as_uint(csv), rs, g == 0 ? mm * 4 : OOB, OFF_CSUM + 33 * 64);
      }
      float v[4];
#pragma unroll
      for (int i = 0; i < 4; ++i) { float mn, rsd; stats(4 * g + i, mn, rsd); v[i] = (skv[i] - mn * csv) * rsd; }
      const u32x2 o = {pk2(v[0], v[1]), pk2(v[2], v[3])};
      bst2<SC1>(o, rs, (g & 1) * 272 + mm * 16 + (g >> 1) * 8, qkv + QO_VT + smul(mt, 640));
      bst2<SC1>((u32x2){0x3F803F80u, 0x3F803F80u}, rs, mm == 0 ? (g & 1) * 272 + 256 + (g >> 1) * 8 : OOB, qkv + QO_VT + smul(mt, 640));
    } else {
      if (FIRST) {
        csk = (f32x4){};
#pragma unroll
        for (int i = 0; i < 8; ++i) csk += *(const f32x4*)(lds + QL_P + i * 8192 + QP_CK + g * 16);
        if (mt == 0) bst4<SC1>(__builtin_bit_cast(u32x4, csk), rs, mm == 0 ? g * 16 : OOB, OFF_CSUM + 32 * 64);
      }
      const f32x4 k = (skv - csk * mean) * rstd;
      const u32x2 o = {pk2(k[0], k[1]), pk2(k[2], k[3])};
      bst2<SC1>(o, rs, mm * 32 + g * 8, qkv + QO_K + mt * 512);
      const unsigned n2 = row16_umax(__float_as_uint(xrow_sum(sq4(o))));
      bst1<SC1>(n2, rs, lane == 0 ? 0u : OOB, qkv + QO_KN2 + mt * 128);
    }
  }
#pragma unroll
  for (int j = 0; j < 8; ++j) {
    if (j < 7) lda(j + 1);
    SB();
#pragma unroll
    for (int t = 0; t < 4; ++t) acc[t] = mfma16(a[j & 1][t], b[j], acc[t]);
    SB();
  }
  *(f32x4*)(mine + QP_T + l16) = acc[2];
  *(f32x4*)(mine + QP_T + 1024 + l16) = acc[3];
  const unsigned fl = (xcd * 4 + rb) * 2 + (cb >> 2);
  if (kvw) {
    asm volatile("s_waitcnt vmcnt(0)" ::: "memory");
    u64 tag = gettag(); asm volatile("" : "+s"(tag));
    bst2<SC1>((u32x2){(unsigned)tag, (unsigned)(tag >> 32)}, rs, lane == 0 ? 0u : OOB, flag_off(FIRST ? 0 : 1, 3, fl) + (cb & 3) * 8);
  }
  raw_barrier();
  const char* csp = lds + QL_P + 2 * hf * 8192 + QP_CS + g * 16;
  if (FIRST) {
    cs0 = *(const f32x4*)(csp) + *(const f32x4*)(csp + 32768); cs1 = *(const f32x4*)(csp + 8192) + *(const f32x4*)(csp + 8192 + 32768);
    if (mt == 0) {
      bst4<SC1>(__builtin_bit_cast(u32x4, cs0), rs, mm == 0 ? g * 16 : OOB, OFF_CSUM + (4 * cb + 2 * hf) * 64);
      bst4<SC1>(__builtin_bit_cast(u32x4, cs1), rs, mm == 0 ? g * 16 + 64 : OOB, OFF_CSUM + (4 * cb + 2 * hf) * 64);
    }
  }
  const f32x4 s0 = acc[0] + *(const f32x4*)(other + QP_T + l16), s1 = acc[1] + *(const f32x4*)(other + QP_T + 1024 + l16);
  const float rq = rstd * QSCALE, nmq = -mean * rq;
  const f32x4 q0 = s0 * rq + cs0 * nmq, q1 = s1 * rq + cs1 * nmq;
  const u32x2 o0 = {pk2(q0[0], q0[1]), pk2(q0[2], q0[3])}, o1 = {pk2(q1[0], q1[1]), pk2(q1[2], q1[3])};
  {
    auto s0_ = __builtin_amdgcn_permlane16_swap(o0[0], o1[0], false, false);
    auto s1_ = __builtin_amdgcn_permlane16_swap(o0[1], o1[1], false, false);
    bst4<SC1>((u32x4){s0_[0], s1_[0], s0_[1], s1_[1]}, rs, (mm >> 3) * 8192 + ((g >> 1) * 32 + (2 * hf + (g & 1)) * 8 + (mm & 7)) * 16, qkv + QO_Q + mt * 16384 + cb * 1024);
  }
  float n0 = sq4(o0), n1 = sq4(o1);
  xrow_sum2(n0, n1);
  const unsigned qn = row16_umax(umax(__float_as_uint(n0), __float_as_uint(n1)));
  bst1<SC1>(qn, rs, lane == 0 ? 0u : OOB, qkv + QO_QN2 + mt * 2048 + (cb * 2 + hf) * 128);
  wg_signal(rs, flag_off(FIRST ? 0 : 1, 3, 64 + fl) + (cb & 3) * 8, tid, gettag());
}

DI void first_phase(char* lds, char* ws, rsrc_t rs, int tid, const u16* x, const u16* wq, const u16* wk, const u16* wv, const u16* wo,
                    const u16* w1, const u16* w2, u64& tag, u64 pc) {
  const int lane = tid & 63, g = lane >> 4;
  const int w = __builtin_amdgcn_readfirstlane(tid >> 6);
  const unsigned lds0 = (unsigned)(uintptr_t)lds, l16 = lane * 16;
  const int xcd = blockIdx.x & 7, idx = blockIdx.x >> 3, cb = idx & 7;
  u64 ep;
  const u16* tw; int tpitch, tks, tnb; unsigned tdst;
  auto fetch = [&]() {
    ep = flag_ld(ws, OFF_EPOCH, 0);
    const int bi = mybid() * 4 + (w & 3);
    if (bi < 128) { tw = wo; tpitch = 1024; tks = bi >> 3; tnb = bi & 7; tdst = OFF_WO; }
    else if (bi < 576) { const int cw = (bi - 128) >> 2, kq = cw / 28; tw = w1; tpitch = 3584; tks = 4 * kq + (w & 3); tnb = cw - 28 * kq; tdst = OFF_W1; }
    else { const int c = bi - 576; tw = w2; tpitch = 1024; tks = c >> 3; tnb = c & 7; tdst = OFF_W2; }
    if (w == 3) {
      const rsrc_t rc = __builtin_amdgcn_make_buffer_rsrc((void*)pc, 0, CODE_PF, 0x00020000);
      unsigned sv;
#pragma unroll
      for (int i = 0; i < (CODE_PF + 8191) / 8192; ++i)
        asm volatile("s_mov_b32 %0, m0\n\ts_mov_b32 m0, %1\n\ts_nop 0\n\tbuffer_load_dword %2, %3, %4 offen lds\n\ts_mov_b32 m0, %0"
                     : "=&s"(sv) : "s"(lds0 + QL_P + 7 * 8192 + 6144), "v"(lane * 128), "s"(rc), "s"(i * 8192) : "memory");
    }
    if (w < 4) {
      const rsrc_t rt = __builtin_amdgcn_make_buffer_rsrc((void*)tw, 0, D_ * DFF * 2, 0x00020000);
      const unsigned v = (lane >> 3) * tpitch + (lane & 7) * 16;
#pragma unroll
      for (int i = 0; i < 4; ++i) dma(rt, v, (32 * tks + 8 * i) * tpitch + tnb * 128, lds0 + QL_TR + w * 4096 + i * 1024);
    }
  };
  auto gettag = [&]() {
    const u64 e = ((u64)(unsigned)__builtin_amdgcn_readfirstlane((int)(ep >> 32)) << 32) | (unsigned)__builtin_amdgcn_readfirstlane((int)ep);
    tag = e * 0x9E3779B97F4A7C15ull + 0x632BE59BD9B4E019ull;
    return tag;
  };
  auto convert = [&]() {
    const int q4 = (lane >> 2) & 3, p4 = lane & 3;
    if (w < 4) {
      const int base = QL_TR + w * 4096 + (4 * g + q4) * 128 + p4 * 8;
      bf16x8 ones; for (int i = 0; i < 8; ++i) ones[i] = 0x3F80;
#pragma unroll
      for (int t = 0; t < 4; ++t) {
        const bf16x8 f = tr_frag(lds, base + t * 32, base + t * 32 + 2048);
        const int nt = 4 * tnb + t, sl = nt / 7;
        const unsigned ch = tdst == OFF_WO ? (tnb * 16 + tks) * 4 + t : tdst == OFF_W1 ? smul(sl * 16 + tks, 7) + (nt - smul(sl, 7)) : smul(nt, 56) + tks;
        bst4<SC1>(__builtin_bit_cast(u32x4, f), rs, l16, tdst + ch * KB);
        const f32x4 z = {};
        const f32x4 c = mfma16(f, ones, z);
        if ((lane & 15) == 0) *(f32x4*)(lds + QL_P + w * 8192 + 4096 + t * 64 + g * 16) = c;
      }
    } else if (w < 6) {
      const int c = (xcd * 4 + (idx >> 3)) * 2 + (w & 1), ks = c >> 2, t = c & 3;
      const int r0 = 4 * g + q4, f0 = ((r0 >> 1) & 1) | (((r0 >> 3) & 1) << 1);
      const int base = (32 * ks + r0) * 128 + ((t ^ f0) << 5) + p4 * 8;
      bst4<SC1>(__builtin_bit_cast(u32x4, tr_frag(lds, base, base + 2048)), rs, l16, OFF_WQKV + ((4 * cb + t) * 16 + ks) * KB);
    } else if (w == 6) {
      const int ks = xcd * 4 + (idx >> 3);
      if (cb < 2 && ks < 16) {
        const int base = QL_KV + (32 * ks + 4 * g + q4) * 32 + p4 * 8;
        bst4<SC1>(__builtin_bit_cast(u32x4, tr_frag(lds, base, base + 512)), rs, l16, OFF_WQKV + ((32 + cb) * 16 + ks) * KB);
      }
    }
  };
  auto colsum = [&]() {
    if (w == 7 && tdst == OFF_W1) {
      const float* c = (const float*)(lds + QL_P + 4096) + lane;
      bst1<SC1>(__float_as_uint((c[0] + c[2048]) + (c[4096] + c[6144])), rs, lane * 4, OFF_CS1 + (tks >> 2) * 7168 + tnb * 256);
    }
  };
  qkv_phase<true>(lds, ws, rs, tid, x, wq, (cb & 1) ? wv : wk, fetch, gettag);
  convert();
  raw_barrier();
  colsum();
}

DI int mytid(int w64) { int t; asm volatile("v_mbcnt_lo_u32_b32 %0, -1, %1\n\tv_mbcnt_hi_u32_b32 %0, -1, %0" : "=v"(t) : "s"(w64)); return t; }
__global__ __launch_bounds__(512, 2) void k_main(const u16* __restrict__ x, const u16* __restrict__ wq, const u16* __restrict__ wk, const u16* __restrict__ wv,
                                                 const u16* __restrict__ wo, const u16* __restrict__ w1, const u16* __restrict__ w2,
                                                 char* __restrict__ ws, u16* __restrict__ out) {
  extern __shared__ __attribute__((aligned(16))) char lds[];
  const int w64 = __builtin_amdgcn_readfirstlane(threadIdx.x & ~63);
  const rsrc_t rs = __builtin_amdgcn_make_buffer_rsrc((void*)ws, 0, WS_NEED, 0x00020000);
  u64 tag, pc;
  asm volatile("s_getpc_b64 %0" : "=s"(pc));
  if (w64 >= 512 - 64 * PF_EARLY) {
    const rsrc_t rc = __builtin_amdgcn_make_buffer_rsrc((void*)pc, 0, CODE_PF, 0x00020000);
    unsigned sv;
    asm volatile("s_mov_b32 %0, m0\n\ts_mov_b32 m0, %1\n\ts_nop 0\n\tbuffer_load_dword %2, %3, 0 offen lds\n\ts_mov_b32 m0, %0"
                 : "=&s"(sv) : "s"((unsigned)(uintptr_t)lds + QL_TR), "v"((511 - threadIdx.x) * 128), "s"(rc) : "memory");
  }
  first_phase(lds, ws, rs, mytid(w64), x, wq, wk, wv, wo, w1, w2, tag, pc);
#pragma unroll 1
  for (int layer = 0; layer < 2; ++layer) {
    bf16x8 stg[28];
    attn_phase(lds, ws, rs, x, layer, mytid(w64), tag);
    ffn1_phase(lds, ws, rs, layer, mytid(w64), stg, tag);
    ffn2_phase(lds, ws, rs, out, layer, mytid(w64), stg, tag);
    if (layer == 0) qkv_phase<false>(lds, ws, rs, mytid(w64), nullptr, nullptr, nullptr, []() {}, [&]() { return tag; });
  }
}

extern "C" void kernel_launch(void* const* d_in, const int* in_sizes, int n_in, void* d_out, int out_size, void* d_ws, size_t ws_size,
                              hipStream_t stream) {
  const u16* x = (const u16*)d_in[0];
  const u16* wq = (const u16*)d_in[1];
  const u16* wk = (const u16*)d_in[2];
  const u16* wv = (const u16*)d_in[3];
  const u16* wo = (const u16*)d_in[4];
  const u16* w1 = (const u16*)d_in[6];
  const u16* w2 = (const u16*)d_in[7];
  char* ws = (char*)d_ws;
  u16* out = (u16*)d_out;
  (void)hipFuncSetAttribute((const void*)k_main, hipFuncAttributeMaxDynamicSharedMemorySize, LDS_BYTES);
  k_main<<<256, 512, LDS_BYTES, stream>>>(x, wq, wk, wv, wo, w1, w2, ws, out);
}
```
